```python
import torch

def _softmax(x, axis=-1):
    return torch.softmax(x, dim=axis)
def _gelu(x):
    return torch.nn.functional.gelu(x, approximate="tanh")
def _silu(x):
    return torch.nn.functional.silu(x)
def _sigmoid(x):
    return torch.sigmoid(x)
def _layernorm(x, axis=-1, eps=1e-5):
    m = x.mean(dim=axis, keepdim=True)
    v = x.var(dim=axis, keepdim=True, unbiased=False)
    return (x - m) / torch.sqrt(v + eps)
def _scatter_add(tgt, idx, src, axis=0):
    return tgt.clone().index_add_(axis, idx, src)
def _segment_sum(src, seg, n):
    out = torch.zeros((n,) + src.shape[1:], dtype=src.dtype)
    return out.index_add_(0, seg, src)
def _pad(x, pw):
    flat = tuple(v for pair in reversed(pw) for v in pair)
    return torch.nn.functional.pad(x, flat)

kernel_name = 'gated_mix__compressed_kv_cla__linear_kernel_hedgehog__prompt'

def setup_inputs():

    _w = (lambda a: (lambda t: t.to(torch.bfloat16) if t.is_floating_point() else t)(torch.as_tensor(a)))
    def _A_setup():
        B, H, Sq, Sk, D, M = 1, 32, 2048, 2048, 128, 512
        Dc = 4 * D
        return {
            "x": _w((lambda *s: torch.randn(*s, dtype=torch.bfloat16) * 0.05)(B, Sq, M)),
            "c_kv": _w((lambda *s: torch.randn(*s, dtype=torch.bfloat16) * 0.8)(B, Sk, Dc)),
            "w_q": _w((lambda *s: torch.randn(*s, dtype=torch.bfloat16) * 6.4)(M, H, D) * (M ** -0.5)),
            "w_uk": _w((lambda *s: torch.randn(*s, dtype=torch.bfloat16) * 6.4)(Dc, H, D) * (Dc ** -0.5)),
            "w_uv": _w((lambda *s: torch.randn(*s, dtype=torch.bfloat16) * 0.8)(Dc, H, D) * (Dc ** -0.5)),
            "w_o": _w((lambda *s: torch.randn(*s, dtype=torch.bfloat16) * 0.8)(H, D, M) * ((H * D) ** -0.5)),
        }
    _A_in = _A_setup()

    def _B_setup():
        B, S, D, H = 1, 2048, 512, 32
        Dh = D // H
        return {
            "x": _w((lambda *s: torch.randn(*s, dtype=torch.bfloat16) * 0.05)(B, S, D)),
            "wqkv": _w((lambda *s: torch.randn(*s, dtype=torch.bfloat16) * 1.6)(D, 3 * D)),
            "w_phi": _w((lambda *s: torch.randn(*s, dtype=torch.bfloat16) * 1.6)(H, Dh, Dh) * (Dh ** -0.5)),
            "wo": _w((lambda *s: torch.randn(*s, dtype=torch.bfloat16) * 0.05)(D, D)),
        }
    _B_in = _B_setup()

    return {
        "A_x": _A_in["x"],
        "A_c_kv": _A_in["c_kv"],
        "A_w_q": _A_in["w_q"],
        "A_w_uk": _A_in["w_uk"],
        "A_w_uv": _A_in["w_uv"],
        "A_w_o": _A_in["w_o"],
        "B_x": _B_in["x"],
        "B_wqkv": _B_in["wqkv"],
        "B_w_phi": _B_in["w_phi"],
        "B_wo": _B_in["wo"],
        "gate_w": _w((lambda *s: torch.randn(*s, dtype=torch.bfloat16) * 0.05)(512) * 16.0),
    }

def reference(A_x, A_c_kv, A_w_q, A_w_uk, A_w_uv, A_w_o, B_x, B_wqkv, B_w_phi, B_wo, gate_w):
    def _A_ref(x, c_kv, w_q, w_uk, w_uv, w_o):
        B, Sq, M = x.shape[0], x.shape[1], x.shape[2]
        Sk = c_kv.shape[1]
        H, D = w_q.shape[1], w_q.shape[2]
        q = torch.einsum("bqm,mhd->bhqd", x, w_q)
        k = torch.einsum("bkc,chd->bhkd", c_kv, w_uk)
        v = torch.einsum("bkc,chd->bhkd", c_kv, w_uv)
        scores = torch.einsum("bhqd,bhkd->bhqk", q, k) * (D ** -0.5)
        off = Sk - Sq
        qi = torch.arange(Sq)[:, None]
        kj = torch.arange(Sk)[None, :]
        cmask = kj <= (qi + off)
        scores = torch.where(cmask[None, None], scores, -1e9)
        p = _softmax(scores, axis=-1)
        ctx = torch.einsum("bhqk,bhkd->bhqd", p, v)
        return torch.einsum("bhqd,hdm->bqm", ctx, w_o)

    def _B_ref(x, wqkv, w_phi, wo):
        B, S, D = x.shape
        H = 32; Dh = D // H
        qkv = (lambda x, *d: x.permute(*d))((x @ wqkv).reshape(B, S, 3, H, Dh), 2, 0, 3, 1, 4)
        q, k, v = qkv[0], qkv[1], qkv[2]
        pq = torch.einsum("bhsd,hdf->bhsf", q, w_phi)
        pk = torch.einsum("bhsd,hdf->bhsf", k, w_phi)
        phi_q = torch.concatenate([_softmax(pq, axis=-1), _softmax(-pq, axis=-1)], axis=-1)
        phi_k = torch.concatenate([_softmax(pk, axis=-1), _softmax(-pk, axis=-1)], axis=-1)
        kv = torch.einsum("bhsf,bhsd->bhfd", phi_k, v)
        z  = torch.einsum("bhsf->bhf", phi_k)
        num = torch.einsum("bhqf,bhfd->bhqd", phi_q, kv)
        den = torch.einsum("bhqf,bhf->bhq", phi_q, z)[..., None] + 1e-6
        o = num / den
        return (lambda x, *d: x.permute(*d))(o, 0, 2, 1, 3).reshape(B, S, D) @ wo

    x = A_x
    g = _sigmoid((x * gate_w).sum(-1, keepdims=True))
    a = _A_ref(x, A_c_kv, A_w_q, A_w_uk, A_w_uv, A_w_o)
    b = _B_ref(x, B_wqkv, B_w_phi, B_wo)
    return g * a + (1 - g) * b
```

```cpp
#include <hip/hip_runtime.h>
#include <stdint.h>

typedef unsigned short u16;
typedef __bf16 bf16x8 __attribute__((ext_vector_type(8)));
typedef __bf16 bf16x2 __attribute__((ext_vector_type(2)));
typedef float f32x2 __attribute__((ext_vector_type(2)));
typedef float f32x4 __attribute__((ext_vector_type(4)));
typedef float f32x16 __attribute__((ext_vector_type(16)));
typedef unsigned u32x2 __attribute__((ext_vector_type(2)));
typedef unsigned u32x4 __attribute__((ext_vector_type(4)));
typedef short s16x4 __attribute__((ext_vector_type(4)));
typedef short s16x8 __attribute__((ext_vector_type(8)));
typedef __amdgpu_buffer_rsrc_t srd_t;
#define LAS __attribute__((address_space(3)))
#define DI __device__ __forceinline__
#define SBAR() __builtin_amdgcn_sched_barrier(0)
#define MFMA(a, b, c) __builtin_amdgcn_mfma_f32_32x32x16_bf16((a), (b), (c), 0, 0, 0)

constexpr size_t MB = 1024 * 1024;
constexpr size_t WS_KF = 0;
constexpr size_t WS_VF = 16 * MB;
constexpr size_t WS_QF = 32 * MB;
constexpr size_t WS_CF = 48 * MB;
constexpr size_t WS_PQ = 68 * MB;
constexpr size_t WS_KVP = 72 * MB;
constexpr size_t WS_GW = 73 * MB;
constexpr size_t WS_ST = 74 * MB;
constexpr size_t WS_SLAB = 76 * MB;
constexpr size_t WS_CNT = 92 * MB;

constexpr float QSCALE = 0.08838834764831845f * 1.4426950408889634f;
constexpr float THR = 8.0f;

DI unsigned cvtpk(float lo, float hi) { f32x2 v = {lo, hi}; bf16x2 b = __builtin_convertvector(v, bf16x2); return __builtin_bit_cast(unsigned, b); }
template <int S> DI u32x4 pack8(const f32x16& x) {
  return (u32x4){cvtpk(x[8 * S + 0], x[8 * S + 1]), cvtpk(x[8 * S + 2], x[8 * S + 3]), cvtpk(x[8 * S + 4], x[8 * S + 5]), cvtpk(x[8 * S + 6], x[8 * S + 7])};
}
DI bf16x8 asbf(u32x4 v) { return __builtin_bit_cast(bf16x8, v); }
DI float swap32_max(float v) { auto rr = __builtin_amdgcn_permlane32_swap(__float_as_uint(v), __float_as_uint(v), false, false); return fmaxf(__uint_as_float(rr[0]), __uint_as_float(rr[1])); }
DI float swap32_add(float v) { auto rr = __builtin_amdgcn_permlane32_swap(__float_as_uint(v), __float_as_uint(v), false, false); return __uint_as_float(rr[0]) + __uint_as_float(rr[1]); }
DI float swap32_lo(float v) { auto rr = __builtin_amdgcn_permlane32_swap(__float_as_uint(v), __float_as_uint(v), false, false); return __uint_as_float(rr[0]); }
DI float bf16round(float x) { return __uint_as_float(cvtpk(x, 0.f) << 16); }
DI srd_t mksrd(const void* p, unsigned bytes) { return __builtin_amdgcn_make_buffer_rsrc((void*)p, (short)0, (int)bytes, 0x00020000); }
DI void dma16(unsigned m0v, unsigned voff, srd_t srd, unsigned soff) {
  asm volatile("s_mov_b32 m0, %0\n\ts_nop 0\n\tbuffer_load_dwordx4 %1, %2, %3 offen lds" :: "s"(m0v), "v"(voff), "s"(srd), "s"(soff) : "memory");
}
DI void dma16m(unsigned m0v, unsigned voff, srd_t srd, unsigned soff) {
  asm volatile("s_mov_b32 m0, %0\n\ts_mov_b32 exec_hi, 0x33333333\n\tbuffer_load_dwordx4 %1, %2, %3 offen lds\n\ts_mov_b32 exec_hi, -1" :: "s"(m0v), "v"(voff), "s"(srd), "s"(soff) : "memory");
}
DI void dma16x4(unsigned m0v, unsigned voff, srd_t srd, unsigned soff) {
  asm volatile("s_mov_b32 m0, %0\n\ts_nop 0\n\t"
               "buffer_load_dwordx4 %1, %2, %3 offen lds\n\t"
               "buffer_load_dwordx4 %1, %2, %3 offen offset:1024 lds\n\t"
               "buffer_load_dwordx4 %1, %2, %3 offen offset:2048 lds\n\t"
               "buffer_load_dwordx4 %1, %2, %3 offen offset:3072 lds"
               :: "s"(m0v), "v"(voff), "s"(srd), "s"(soff) : "memory");
}
#define WAIT_BAR(N) asm volatile("s_waitcnt vmcnt(" #N ") lgkmcnt(0)\n\ts_barrier" ::: "memory")
#define LBAR() asm volatile("s_waitcnt lgkmcnt(0)\n\ts_barrier" ::: "memory")
DI bf16x8 ldsfrag(LAS const char* p) { return *(LAS const bf16x8*)p; }
DI bf16x8 trfrag(LAS const char* p0, LAS const char* p1) {
  s16x4 a = __builtin_amdgcn_ds_read_tr16_b64_v4i16((LAS s16x4*)p0);
  s16x4 b = __builtin_amdgcn_ds_read_tr16_b64_v4i16((LAS s16x4*)p1);
  return __builtin_bit_cast(bf16x8, __builtin_shufflevector(a, b, 0, 1, 2, 3, 4, 5, 6, 7));
}

struct PCtx {
  srd_t sA1, sA2, sUK, sUV, sWQ, sWB;
  unsigned lds0, voffA[2], voffB, voffS;
  unsigned soA1, soA2, soB, soS, mA, mB1, mB2;
  int w, lane;
  LAS char* L;
};
DI unsigned rfl(unsigned x) { return (unsigned)__builtin_amdgcn_readfirstlane((int)x); }
DI void p_issue(const PCtx& c, int T) {
  const unsigned st = (unsigned)(T & 1) * 65536u, kt = (unsigned)(T & 7);
  if (T < 8) {
#pragma unroll
    for (int q = 0; q < 4; ++q) dma16(st + c.mA + q * 1024u, c.voffA[q & 1], c.sA1, c.soA1 + q * 8192u + kt * 128u);
    const unsigned kb = c.soB + kt * 524288u;
    dma16(st + c.mB1, c.voffB, c.sUK, kb);
    dma16(st + c.mB1 + 1024u, c.voffB, c.sUK, kb + 128u);
    dma16(st + c.mB1 + 2048u, c.voffB, c.sUV, kb);
    dma16(st + c.mB1 + 3072u, c.voffB, c.sUV, kb + 128u);
  } else {
#pragma unroll
    for (int q = 0; q < 4; ++q) dma16(st + c.mA + q * 1024u, c.voffA[q & 1], c.sA2, c.soA2 + q * 8192u + kt * 128u);
    const unsigned kb = c.soB + kt * 524288u;
    dma16(st + c.mB2, c.voffB, c.sWQ, kb);
    dma16(st + c.mB2 + 1024u, c.voffB, c.sWQ, kb + 128u);
    dma16m(st + c.mB2 + 2048u, c.voffS, c.sWB, c.soS + kt * 196608u);
    if (c.w == 7) {
      const u16 gv = *(LAS const u16*)(c.L + 131072 + (64 * kt + c.lane) * 2);
      *(LAS u16*)(c.L + st + 32768 + ((c.lane >> 3) * 6 + 5) * 512 + (c.lane & 7) * 64 + 32) = gv;
    }
  }
}

template <int PASS, int MT, int NT, int NSUB, bool TRANS>
DI void p_pass(const PCtx& c, f32x16 (&acc)[MT][NT], unsigned arow0, unsigned nt0) {
  const int lane = c.lane, r = lane & 31, hh = lane >> 5, i16 = lane & 15;
  unsigned aB[4];
#pragma unroll
  for (int ks = 0; ks < 4; ++ks) aB[ks] = (arow0 + r) * 128u + (unsigned)(((2 * ks + hh) ^ ((r >> 1) & 7)) * 16);
  unsigned bB = 32768u + (unsigned)(hh * NSUB + nt0) * 512u + (unsigned)((i16 >> 2) * 64 + ((lane >> 4) & 1) * 32 + (i16 & 3) * 8);
  bf16x8 fa[2][MT], fb[2][NT];
#define P_LOAD(set, ks) do { _Pragma("unroll") for (int nt = 0; nt < NT; ++nt) fb[set][nt] = trfrag(c.L + bB + (ks) * (2 * NSUB * 512) + nt * 512, c.L + bB + (ks) * (2 * NSUB * 512) + nt * 512 + 256); \
                             _Pragma("unroll") for (int mt = 0; mt < MT; ++mt) fa[set][mt] = ldsfrag(c.L + aB[ks] + mt * 4096); } while (0)
#define P_MMA(set) do { _Pragma("unroll") for (int mt = 0; mt < MT; ++mt) _Pragma("unroll") for (int nt = 0; nt < NT; ++nt) \
                          acc[mt][nt] = TRANS ? MFMA(fb[set][nt], fa[set][mt], acc[mt][nt]) : MFMA(fa[set][mt], fb[set][nt], acc[mt][nt]); } while (0)
#pragma unroll
  for (int mt = 0; mt < MT; ++mt)
#pragma unroll
    for (int nt = 0; nt < NT; ++nt) acc[mt][nt] = (f32x16){};
  P_LOAD(0, 0);
#pragma unroll 1
  for (int t = 0; t < 8; ++t) {
    SBAR(); P_LOAD(1, 1); SBAR(); P_MMA(0);
    SBAR(); P_LOAD(0, 2); SBAR(); P_MMA(1);
    SBAR(); P_LOAD(1, 3); SBAR(); P_MMA(0);
    SBAR();
    if (PASS == 1 && t == 0) WAIT_BAR(16); else WAIT_BAR(0);
    SBAR();
    const int T = PASS * 8 + t + 2;
    if (T < 16) p_issue(c, T);
#pragma unroll
    for (int ks = 0; ks < 4; ++ks) aB[ks] ^= 65536u;
    bB ^= 65536u;
    if (t < 7) P_LOAD(0, 0);
    SBAR(); P_MMA(1);
  }
  SBAR();
#undef P_LOAD
#undef P_MMA
}

DI void softmax2(f32x16& x) {
#pragma unroll
  for (int s = 0; s < 2; ++s) {
    float v[8];
#pragma unroll
    for (int j = 0; j < 8; ++j) v[j] = bf16round(x[8 * s + j]);
    float mx = v[0];
#pragma unroll
    for (int j = 1; j < 8; ++j) mx = fmaxf(mx, v[j]);
    mx = swap32_max(mx);
    float sum = 0.f;
#pragma unroll
    for (int j = 0; j < 8; ++j) { v[j] = __builtin_amdgcn_exp2f((v[j] - mx) * 1.4426950408889634f); sum += v[j]; }
    sum = swap32_add(sum);
    const float inv = 1.0f / sum;
#pragma unroll
    for (int j = 0; j < 8; ++j) x[8 * s + j] = v[j] * inv;
  }
}

__global__ __launch_bounds__(512, 2) void kP(const u16* __restrict__ x, const u16* __restrict__ ckv, const u16* __restrict__ wq, const u16* __restrict__ wuk,
                                             const u16* __restrict__ wuv, const u16* __restrict__ wqkv, const u16* __restrict__ wphi, const u16* __restrict__ gatew, char* __restrict__ ws) {
  extern __shared__ __attribute__((aligned(16))) char lds[];
  PCtx c;
  c.L = (LAS char*)lds; c.lds0 = (unsigned)(uintptr_t)c.L;
  const int tid = threadIdx.x, lane = tid & 63, w = __builtin_amdgcn_readfirstlane(tid >> 6);
  const int b = blockIdx.x, h = (b & 7) * 4 + ((b >> 3) & 3), r = b >> 5;
  c.w = w; c.lane = lane;
  c.soA1 = rfl((unsigned)(256 * r + 32 * w) * 1024u);
  c.soA2 = rfl((unsigned)(w < 4 ? 128 * (15 - r) + 32 * w : 128 * r + 32 * (w - 4)) * 1024u);
  c.soB = rfl((unsigned)(8 * w) * 8192u + (unsigned)h * 256u);
  c.soS = rfl((unsigned)(8 * w) * 3072u + (unsigned)h * 32u);
  c.mA = rfl(c.lds0 + (unsigned)w * 4096u); c.mB1 = rfl(c.lds0 + 32768u + (unsigned)w * 4096u); c.mB2 = rfl(c.lds0 + 32768u + (unsigned)w * 3072u);
  c.sA1 = mksrd(ckv, 2048u * 1024u); c.sA2 = mksrd(x, 2048u * 1024u);
  c.sUK = mksrd(wuk, 512u * 8192u); c.sUV = mksrd(wuv, 512u * 8192u); c.sWQ = mksrd(wq, 512u * 8192u); c.sWB = mksrd(wqkv, 512u * 3072u);
#pragma unroll
  for (int q = 0; q < 2; ++q) c.voffA[q] = (unsigned)((lane >> 3) * 1024 + (((lane & 7) ^ ((4 * q + (lane >> 4)) & 7)) * 16));
  c.voffB = (unsigned)(((lane >> 2) & 7) * 8192 + (lane >> 5) * 64 + (lane & 3) * 16);
  { const int ch = lane & 3, sub = lane >> 5;
    c.voffS = (unsigned)(((lane >> 2) & 7) * 3072 + (sub == 0 ? (ch < 2 ? ch * 16 : 1024 + (ch - 2) * 16) : 2048 + (ch & 1) * 16)); }
  if (tid < 256) *(LAS unsigned*)(c.L + 131072 + tid * 4) = ((const unsigned*)gatew)[tid];
  asm volatile("s_waitcnt vmcnt(0) lgkmcnt(0)" ::: "memory");
  p_issue(c, 0); p_issue(c, 1);
  WAIT_BAR(8);
  const int G = w >> 2, hh = lane >> 5, r32 = lane & 31;
  {
    f32x16 acc[4][2];
    const int wm = (w >> 1) & 1, wn = w & 1;
    if (G == 0) p_pass<0, 4, 2, 8, true>(c, acc, 128u * wm, 2u * wn);
    else        p_pass<0, 4, 2, 8, false>(c, acc, 128u * wm, 4u + 2u * wn);
#pragma unroll
    for (int mt = 0; mt < 4; ++mt) {
      const int kbg = 8 * r + 4 * wm + mt, t = kbg >> 1, kb = kbg & 1;
      char* base = ws + (G == 0 ? WS_KF : WS_VF) + (size_t)(h * 32 + t) * 16384 + lane * 16;
#pragma unroll
      for (int nt = 0; nt < 2; ++nt) {
        const int dt = 2 * wn + nt;
        const int f0 = G == 0 ? kb * 8 + 2 * dt : dt * 4 + 2 * kb;
        *(u32x4*)(base + f0 * 1024) = pack8<0>(acc[mt][nt]);
        *(u32x4*)(base + f0 * 1024 + 1024) = pack8<1>(acc[mt][nt]);
      }
    }
  }
  f32x16 acc[2][3];
  const int wm = w & 3;
  p_pass<1, 2, 3, 6, true>(c, acc, 64u * wm, 3u * G);
  LBAR();
  const int rb0 = (wm < 2 ? 4 * (15 - r) + 2 * wm : 4 * r + 2 * (wm - 2));
#pragma unroll
  for (int mt = 0; mt < 2; ++mt) {
    char* qb = ws + WS_QF + (size_t)((h * 64 + rb0 + mt) * 8) * 1024 + lane * 16;
#pragma unroll
    for (int nt = 0; nt < 3; ++nt) {
      if (G == 0 || nt == 0) {
        const int dt = 3 * G + nt;
        f32x16 v = acc[mt][nt] * QSCALE;
        *(u32x4*)(qb + (2 * dt) * 1024) = pack8<0>(v);
        *(u32x4*)(qb + (2 * dt + 1) * 1024) = pack8<1>(v);
      }
    }
  }
  if (G == 1) {
    u32x4 wp;
    { const u16* wb = wphi + h * 256 + (r32 & 15); const unsigned sg = r32 >= 16 ? 0x80008000u : 0u;
#pragma unroll
      for (int jj = 0; jj < 4; ++jj) { const int d0 = 8 * (jj >> 1) + 4 * hh + 2 * (jj & 1); wp[jj] = ((unsigned)wb[d0 * 16] | ((unsigned)wb[(d0 + 1) * 16] << 16)) ^ sg; } }
    LAS char* Ik = c.L + wm * 8192; LAS char* Iv = Ik + 4096;
#pragma unroll
    for (int mt = 0; mt < 2; ++mt) {
      f32x16 pq = MFMA(asbf(wp), asbf(pack8<0>(acc[mt][1])), (f32x16){});
      f32x16 pk = MFMA(asbf(wp), asbf(pack8<1>(acc[mt][1])), (f32x16){});
      softmax2(pq); softmax2(pk);
      char* pb = ws + WS_PQ + (size_t)((h * 64 + rb0 + mt) * 2) * 1024 + lane * 16;
      *(u32x4*)(pb) = pack8<0>(pq); *(u32x4*)(pb + 1024) = pack8<1>(pq);
      const int m = 32 * mt + r32;
#pragma unroll
      for (int g = 0; g < 4; ++g) *(LAS u32x2*)(Ik + m * 64 + 16 * g + 8 * hh) = (u32x2){cvtpk(pk[4 * g], pk[4 * g + 1]), cvtpk(pk[4 * g + 2], pk[4 * g + 3])};
      const f32x16& tv = acc[mt][2];
      *(LAS u32x2*)(Iv + m * 64 + 8 * hh) = (u32x2){cvtpk(tv[0], tv[1]), cvtpk(tv[2], tv[3])};
      *(LAS u32x2*)(Iv + m * 64 + 16 + 8 * hh) = (u32x2){cvtpk(tv[4], tv[5]), cvtpk(tv[6], tv[7])};
      *(LAS u32x2*)(Iv + m * 64 + 32 + 8 * hh) = (u32x2){hh == 0 ? 0x00003f80u : 0u, 0u};
      *(LAS u32x2*)(Iv + m * 64 + 48 + 8 * hh) = (u32x2){0u, 0u};
      if (hh == 0) { const float gl = tv[8]; ((float*)(ws + WS_GW))[b * 256 + 64 * wm + m] = 1.0f / (1.0f + __expf(-gl)); }
    }
    asm volatile("s_waitcnt lgkmcnt(0)" ::: "memory");
    const int i16 = lane & 15;
    const unsigned tb = (unsigned)((8 * hh + (i16 >> 2)) * 64 + ((lane >> 4) & 1) * 32 + (i16 & 3) * 8);
    f32x16 kv = {};
#pragma unroll
    for (int ks = 0; ks < 4; ++ks) kv = MFMA(trfrag(Ik + tb + ks * 1024, Ik + tb + ks * 1024 + 256), trfrag(Iv + tb + ks * 1024, Iv + tb + ks * 1024 + 256), kv);
#pragma unroll
    for (int g = 0; g < 4; ++g) *(LAS f32x4*)(c.L + 32768 + wm * 4096 + g * 1024 + lane * 16) = (f32x4){kv[4 * g], kv[4 * g + 1], kv[4 * g + 2], kv[4 * g + 3]};
  }
  LBAR();
  if (w < 4) {
    f32x4 s = *(LAS const f32x4*)(c.L + 32768 + w * 1024 + lane * 16);
#pragma unroll
    for (int k = 1; k < 4; ++k) s += *(LAS const f32x4*)(c.L + 32768 + k * 4096 + w * 1024 + lane * 16);
    *(f32x4*)(ws + WS_KVP + (size_t)(h * 8 + r) * 4096 + w * 1024 + lane * 16) = s;
  }
}

DI void mfma_acc(f32x16& c, bf16x8 a, bf16x8 b) { asm volatile("v_mfma_f32_32x32x16_bf16 %0, %1, %2, %0" : "+v"(c) : "v"(a), "v"(b)); }
DI void mfma_zero(f32x16& c, bf16x8 a, bf16x8 b) { asm volatile("v_mfma_f32_32x32x16_bf16 %0, %1, %2, 0" : "=&v"(c) : "v"(a), "v"(b)); }
#define NOP16() asm volatile("s_nop 7\n\ts_nop 7" ::: "memory")
template <bool DOQK, bool DOPV>
DI void a_mphase(LAS const char* kp, LAS const char* vp, const bf16x8 (&q)[8], const bf16x8 (&p)[4], f32x16 (&S)[2], f32x16 (&O)[4]) {
  constexpr int N = (DOQK ? 16 : 0) + (DOPV ? 16 : 0), D = 6;
  bf16x8 f[N];
#define A_LD(i) do { if constexpr ((i) < N) { if constexpr (DOQK && (i) < 16) f[i] = ldsfrag(kp + ((((i) & 1) * 8 + ((i) >> 1)) * 1024)); \
                     else { constexpr int j_ = (i) - (DOQK ? 16 : 0); f[i] = ldsfrag(vp + (((j_ & 3) * 4 + (j_ >> 2)) * 1024)); } } } while (0)
#define A_MM(i) do { if constexpr ((i) < N) { if constexpr (DOQK && (i) < 16) { if constexpr ((i) < 2) mfma_zero(S[(i) & 1], f[i], q[(i) >> 1]); else mfma_acc(S[(i) & 1], f[i], q[(i) >> 1]); } \
                     else { constexpr int j_ = (i) - (DOQK ? 16 : 0); mfma_acc(O[j_ & 3], f[i], p[j_ >> 2]); } } } while (0)
#define A_STEP(i) do { if constexpr ((i) < N) { A_LD((i) + D); if constexpr ((i) == N - 4) { SBAR(); LBAR(); SBAR(); } A_MM(i); SBAR(); } } while (0)
  SBAR();
  A_LD(0); A_LD(1); A_LD(2); A_LD(3); A_LD(4); A_LD(5); SBAR();
  A_STEP(0); A_STEP(1); A_STEP(2); A_STEP(3); A_STEP(4); A_STEP(5); A_STEP(6); A_STEP(7);
  A_STEP(8); A_STEP(9); A_STEP(10); A_STEP(11); A_STEP(12); A_STEP(13); A_STEP(14); A_STEP(15);
  A_STEP(16); A_STEP(17); A_STEP(18); A_STEP(19); A_STEP(20); A_STEP(21); A_STEP(22); A_STEP(23);
  A_STEP(24); A_STEP(25); A_STEP(26); A_STEP(27); A_STEP(28); A_STEP(29); A_STEP(30); A_STEP(31);
  if constexpr (!DOPV) { NOP16(); SBAR(); }
#undef A_LD
#undef A_MM
#undef A_STEP
}

DI void a_finish(const f32x16& a0, const f32x16& a1, float ma, float la, LAS const char* xr, float mb, float lb, float gate, srd_t sC, unsigned voff, unsigned soff) {
  const float mt = fmaxf(ma, mb);
  const float fa = __builtin_amdgcn_exp2f(ma - mt), fb = __builtin_amdgcn_exp2f(mb - mt);
  const float sc = gate / (la * fa + lb * fb);
  const float wa = fa * sc, wb = fb * sc;
#pragma unroll
  for (int t = 0; t < 2; ++t) {
    const f32x16& a = t ? a1 : a0;
#pragma unroll
    for (int s = 0; s < 2; ++s) {
      const f32x4 b0 = *(LAS const f32x4*)(xr + t * 4096 + s * 2048), b1 = *(LAS const f32x4*)(xr + t * 4096 + s * 2048 + 1024);
      const u32x4 o = {cvtpk(a[8 * s + 0] * wa + b0[0] * wb, a[8 * s + 1] * wa + b0[1] * wb), cvtpk(a[8 * s + 2] * wa + b0[2] * wb, a[8 * s + 3] * wa + b0[3] * wb),
                       cvtpk(a[8 * s + 4] * wa + b1[0] * wb, a[8 * s + 5] * wa + b1[1] * wb), cvtpk(a[8 * s + 6] * wa + b1[2] * wb, a[8 * s + 7] * wa + b1[3] * wb)};
      __builtin_amdgcn_raw_buffer_store_b128(o, sC, voff + (unsigned)((2 * t + s) * 1024), soff, 0);
      SBAR();
    }
  }
}
DI void a_send(const f32x16& a0, const f32x16& a1, LAS char* xr) {
#pragma unroll
  for (int g = 0; g < 4; ++g) {
    *(LAS f32x4*)(xr + g * 1024) = (f32x4){a0[4 * g], a0[4 * g + 1], a0[4 * g + 2], a0[4 * g + 3]};
    *(LAS f32x4*)(xr + 4096 + g * 1024) = (f32x4){a1[4 * g], a1[4 * g + 1], a1[4 * g + 2], a1[4 * g + 3]};
  }
}

__global__ __launch_bounds__(512, 2) void kA(char* __restrict__ ws) {
  extern __shared__ __attribute__((aligned(16))) char lds[];
  LAS char* L = (LAS char*)lds; const unsigned lds0 = (unsigned)(uintptr_t)L;
  const int tid = threadIdx.x, lane = tid & 63, w = __builtin_amdgcn_readfirstlane(tid >> 6);
  const int b = blockIdx.x, h = (b & 7) * 4 + ((b >> 3) & 3), r = b >> 5;
  const int g = w >> 2, wq = w & 3, hh = lane >> 5, r32 = lane & 31;
  const int X = 15 - r, Y = r, nX = 16 - r;
  constexpr int NS = 17;
  if (b == 0 && tid < 64) *(unsigned*)(ws + WS_CNT + tid * 256) = 0u;
  const srd_t sK = mksrd(ws + WS_KF + (size_t)h * 32 * 16384, 32u * 16384u), sV = mksrd(ws + WS_VF + (size_t)h * 32 * 16384, 32u * 16384u);
  const srd_t sQ = mksrd(ws + WS_QF + (size_t)h * 64 * 8192, 64u * 8192u);
  const srd_t sC = mksrd(ws + WS_CF, 64u * 288u * 1024u), sT = mksrd(ws + WS_ST + (size_t)b * 4096, 4096u);
  const unsigned voff = (unsigned)lane * 16u;
  const unsigned ring = lds0 + (unsigned)g * 65536u + (unsigned)wq * 4096u;
#define TILE(s) ((s) < nX ? 2 * (s) + g : 2 * ((s) - nX) + g)
#define DMA_K(s) dma16x4(ring + (unsigned)((s) & 1) * 16384u, voff, sK, (unsigned)TILE(s) * 16384u + (unsigned)wq * 4096u)
#define DMA_V(s) dma16x4(ring + 32768u + (unsigned)((s) & 1) * 16384u, voff, sV, (unsigned)TILE(s) * 16384u + (unsigned)wq * 4096u)
  DMA_K(0); DMA_V(0); DMA_K(1);
  dma16x4(lds0 + 131072u + (unsigned)w * 4096u, voff, sQ, (unsigned)(4 * Y) * 8192u + (unsigned)w * 4096u);
  bf16x8 q[8];
  { const char* qb = ws + WS_QF + (size_t)((h * 64 + 4 * X + wq) * 8) * 1024 + lane * 16;
#pragma unroll
    for (int ks = 0; ks < 8; ++ks) q[ks] = *(const bf16x8*)(qb + ks * 1024); }
  const float* gw = (const float*)(ws + WS_GW) + b * 256;
  const float gateX = gw[32 * wq + r32], gateY = gw[128 + 32 * wq + r32];
  {
    f32x16 kv = {};
    const char* kp = ws + WS_KVP + (size_t)h * 8 * 4096 + lane * 16;
#pragma unroll
    for (int k = 0; k < 8; ++k)
#pragma unroll
      for (int qd = 0; qd < 4; ++qd) { const f32x4 v = *(const f32x4*)(kp + k * 4096 + qd * 1024);
#pragma unroll
        for (int e = 0; e < 4; ++e) kv[4 * qd + e] += v[e]; }
    const int rb = g == 0 ? 4 * X + wq : 4 * Y + wq;
    const char* pb = ws + WS_PQ + (size_t)((h * 64 + rb) * 2) * 1024 + lane * 16;
    const bf16x8 p0 = *(const bf16x8*)pb, p1 = *(const bf16x8*)(pb + 1024);
    f32x16 o = MFMA(asbf(pack8<0>(kv)), p0, (f32x16){});
    o = MFMA(asbf(pack8<1>(kv)), p1, o);
    const float den = swap32_lo(o[8]) + 1e-6f;
    const float sc = (1.0f - (g == 0 ? gateX : gateY)) / den;
    o = o * sc;
    *(u32x4*)(ws + WS_CF + (size_t)(rb * 288 + 256 + h) * 1024 + lane * 16) = pack8<0>(o);
  }
  WAIT_BAR(0);
  if (g == 1) LBAR();
  f32x16 O[4], S[2], keep0, keep1;
#pragma unroll
  for (int d = 0; d < 4; ++d) O[d] = (f32x16){};
  keep0 = (f32x16){}; keep1 = (f32x16){};
  bf16x8 p[4];
  float m = -1e30f, l = 0.f, mk = 0.f, lk = 0.f;
  LAS const char* kbase = L + g * 65536 + lane * 16;
  LAS char* xr = L + 131072 + wq * 8192 + lane * 16;
#define ST_LD(blk, wv) __builtin_bit_cast(f32x2, __builtin_amdgcn_raw_buffer_load_b64(sT, (unsigned)r32 * 8u, (unsigned)(((blk) * 8 + (wv)) * 256), 0))
#define CF_SO(blkrow, half) (unsigned)(((4 * (blkrow) + wq) * 288 + 8 * h + 4 * (half)) * 1024)
  auto e3 = [&](int blk) {
    const f32x2 ps = ST_LD(blk, w + 4);
    a_finish(keep0, keep1, mk, lk, xr, ps[0], ps[1], blk ? gateY : gateX, sC, voff, CF_SO(blk ? Y : X, 0));
    asm volatile("s_waitcnt lgkmcnt(0)" ::: "memory");
  };
  auto e12 = [&](int blk) {
    NOP16(); SBAR();
    const float lt = swap32_add(l);
    if (g == 0) {
      a_send(O[2], O[3], xr);
      keep0 = O[0]; keep1 = O[1]; mk = m; lk = lt;
    } else {
      const f32x2 ps = ST_LD(blk, w - 4);
      a_finish(O[2], O[3], m, lt, xr, ps[0], ps[1], blk ? gateY : gateX, sC, voff, CF_SO(blk ? Y : X, 1));
      asm volatile("s_waitcnt lgkmcnt(0)" ::: "memory");
      a_send(O[0], O[1], xr);
    }
    __builtin_amdgcn_raw_buffer_store_b64(__builtin_bit_cast(u32x2, (f32x2){m, lt}), sT, (unsigned)r32 * 8u, (unsigned)((blk * 8 + w) * 256), 0);
    asm volatile("s_waitcnt vmcnt(0)" ::: "memory");
  };
  auto lphase = [&](int s) {
    if (g == 0 && s == nX + 1) e3(0);
    if (s == nX) {
      e12(0);
#pragma unroll
      for (int d = 0; d < 4; ++d) O[d] = (f32x16){};
      m = -1e30f; l = 0.f;
    }
    if (s + 2 < NS) DMA_K(s + 2);
    if (s + 1 < NS) DMA_V(s + 1);
    if (s == nX - 1 || s == NS - 1) {
      int rel = 32 * wq + r32 - 64 * g - 4 * hh;
      asm volatile("" : "+v"(rel));
#pragma unroll
      for (int e = 0; e < 16; ++e) {
        const int cc = (e & 3) + 8 * (e >> 2);
        if (cc > rel) S[0][e] = -__builtin_inff();
        if (cc + 32 > rel) S[1][e] = -__builtin_inff();
      }
    }
    float pm = fmaxf(S[0][0], S[1][0]);
#pragma unroll
    for (int e = 1; e < 16; ++e) pm = fmaxf(fmaxf(pm, S[0][e]), S[1][e]);
    pm = swap32_max(pm);
    if (__any(pm - m > THR)) {
      NOP16(); SBAR();
      const float mn = fmaxf(m, pm), f = __builtin_amdgcn_exp2f(m - mn);
      m = mn; l *= f;
#pragma unroll
      for (int d = 0; d < 4; ++d) O[d] = O[d] * f;
    }
    float ls0 = 0.f, ls1 = 0.f;
#pragma unroll
    for (int e = 0; e < 16; ++e) {
      S[0][e] = __builtin_amdgcn_exp2f(S[0][e] - m); ls0 += S[0][e];
      S[1][e] = __builtin_amdgcn_exp2f(S[1][e] - m); ls1 += S[1][e];
    }
    l += ls0 + ls1;
    p[0] = asbf(pack8<0>(S[0])); p[1] = asbf(pack8<1>(S[0])); p[2] = asbf(pack8<0>(S[1])); p[3] = asbf(pack8<1>(S[1]));
    if (s == nX - 1) {
#pragma unroll
      for (int ks = 0; ks < 8; ++ks) q[ks] = ldsfrag(L + 131072 + wq * 8192 + ks * 1024 + lane * 16);
    }
    SBAR();
    if (s + 2 < NS) WAIT_BAR(8); else if (s + 1 < NS) WAIT_BAR(4); else WAIT_BAR(0);
    SBAR();
  };
  a_mphase<true, false>(kbase, kbase, q, p, S, O);
#pragma unroll 1
  for (int s = 0; s < NS - 1; ++s) {
    lphase(s);
    a_mphase<true, true>(kbase + ((s + 1) & 1) * 16384, kbase + 32768 + (s & 1) * 16384, q, p, S, O);
  }
  lphase(NS - 1);
  a_mphase<false, true>(kbase, kbase + 32768 + ((NS - 1) & 1) * 16384, q, p, S, O);
  if (g == 0 && nX + 1 == NS) e3(0);
  e12(1);
  LBAR();
  if (g == 0) { LBAR(); e3(1); }
#undef TILE
#undef DMA_K
#undef DMA_V
}

constexpr int O_NST = 4;
__global__ __launch_bounds__(512, 2) void kO(const u16* __restrict__ wo_a, const u16* __restrict__ wo_b, char* __restrict__ ws, u16* __restrict__ out) {
  extern __shared__ __attribute__((aligned(16))) char lds[];
  LAS char* L = (LAS char*)lds; const unsigned lds0 = (unsigned)(uintptr_t)L;
  const int tid = threadIdx.x, lane = tid & 63, w = __builtin_amdgcn_readfirstlane(tid >> 6);
  const int b = blockIdx.x, xcd = b & 7, j = xcd & 3, idx = b >> 3, mi = (xcd >> 2) * 8 + (idx >> 2), ni = idx & 3;
  const int g = w >> 2, wm = (w >> 1) & 1, wn = w & 1, hh = lane >> 5, r32 = lane & 31, i16 = lane & 15;
  const srd_t sC = mksrd(ws + WS_CF, 64u * 288u * 1024u), sWa = mksrd(wo_a, 4096u * 1024u), sWb = mksrd(wo_b, 512u * 1024u);
  const unsigned voffA = (unsigned)lane * 16u;
  const unsigned voffB = (unsigned)(((lane >> 2) & 7) * 1024 + (lane >> 5) * 64 + (lane & 3) * 16);
  auto issue = [&](int t) {
    const unsigned st = lds0 + (unsigned)(t % O_NST) * 32768u;
    const int kt = 18 * j + t;
    const unsigned so = (unsigned)((4 * mi + (w >> 1)) * 288 + 4 * kt + 2 * (w & 1)) * 1024u;
    dma16(st + (unsigned)w * 2048u, voffA, sC, so);
    dma16(st + (unsigned)w * 2048u + 1024u, voffA, sC, so + 1024u);
    if (kt < 64) {
      const unsigned kb = (unsigned)(64 * kt + 8 * w) * 1024u + (unsigned)ni * 256u;
      dma16(st + 16384u + (unsigned)w * 2048u, voffB, sWa, kb); dma16(st + 16384u + (unsigned)w * 2048u + 1024u, voffB, sWa, kb + 128u);
    } else {
      const unsigned kb = (unsigned)(64 * (kt - 64) + 8 * w) * 1024u + (unsigned)ni * 256u;
      dma16(st + 16384u + (unsigned)w * 2048u, voffB, sWb, kb); dma16(st + 16384u + (unsigned)w * 2048u + 1024u, voffB, sWb, kb + 128u);
    }
  };
  issue(0); issue(1); issue(2);
  f32x16 acc[2][2];
#pragma unroll
  for (int a = 0; a < 2; ++a)
#pragma unroll
    for (int c = 0; c < 2; ++c) acc[a][c] = (f32x16){};
  const unsigned aB = (unsigned)((2 * wm * 4 + 2 * g) * 1024 + lane * 16);
  const int p4 = i16 & 3;
  const unsigned bB = 16384u + (unsigned)((4 * g * 4 + 2 * wn) * 512 + (4 * hh + (i16 >> 2)) * 64 + ((lane >> 4) & 1) * 32 + (((p4 & 1) << 1) | (p4 >> 1)) * 8);
#pragma unroll 1
  for (int t = 0; t < 18; ++t) {
    if (t < 16) WAIT_BAR(8); else if (t == 16) WAIT_BAR(4); else WAIT_BAR(0);
    SBAR();
    if (t + 3 < 18) issue(t + 3);
    LAS const char* sp = L + (t % O_NST) * 32768;
    bf16x8 fa[2][2], fb[2][2];
#pragma unroll
    for (int ks = 0; ks < 2; ++ks) {
#pragma unroll
      for (int nt = 0; nt < 2; ++nt) fb[ks][nt] = trfrag(sp + bB + ks * 4096 + nt * 512, sp + bB + ks * 4096 + nt * 512 + 2048);
#pragma unroll
      for (int mt = 0; mt < 2; ++mt) fa[ks][mt] = ldsfrag(sp + aB + mt * 4096 + ks * 1024);
    }
#pragma unroll
    for (int ks = 0; ks < 2; ++ks)
#pragma unroll
      for (int mt = 0; mt < 2; ++mt)
#pragma unroll
        for (int nt = 0; nt < 2; ++nt) acc[mt][nt] = MFMA(fb[ks][nt], fa[ks][mt], acc[mt][nt]);
    SBAR();
  }
  LBAR();
  LAS char* rp = L + (w & 3) * 16384 + lane * 16;
  if (g == 1) {
#pragma unroll
    for (int mt = 0; mt < 2; ++mt)
#pragma unroll
      for (int nt = 0; nt < 2; ++nt)
#pragma unroll
        for (int qd = 0; qd < 4; ++qd) *(LAS f32x4*)(rp + ((mt * 2 + nt) * 4 + qd) * 1024) = (f32x4){acc[mt][nt][4 * qd], acc[mt][nt][4 * qd + 1], acc[mt][nt][4 * qd + 2], acc[mt][nt][4 * qd + 3]};
  }
  LBAR();
  if (g == 1) return;
#pragma unroll
  for (int mt = 0; mt < 2; ++mt)
#pragma unroll
    for (int nt = 0; nt < 2; ++nt)
#pragma unroll
      for (int qd = 0; qd < 4; ++qd) { const f32x4 v = *(LAS const f32x4*)(rp + ((mt * 2 + nt) * 4 + qd) * 1024);
#pragma unroll
        for (int e = 0; e < 4; ++e) acc[mt][nt][4 * qd + e] += v[e]; }
  const int tg = mi * 4 + ni;
  const srd_t sS = mksrd(ws + WS_SLAB + (size_t)tg * 4 * 65536, 4u * 65536u);
  const unsigned so = (unsigned)(w * 16384 + lane * 16);
#pragma unroll
  for (int a = 0; a < 4; ++a) {
    if (a != j) {
#pragma unroll
      for (int qd = 0; qd < 4; ++qd) {
        const f32x16& v = acc[a >> 1][a & 1];
        __builtin_amdgcn_raw_buffer_store_b128(__builtin_bit_cast(u32x4, (f32x4){v[4 * qd], v[4 * qd + 1], v[4 * qd + 2], v[4 * qd + 3]}), sS, so + (unsigned)((a * 4 + qd) * 1024), (unsigned)j * 65536u, 16);
      }
    }
  }
  asm volatile("s_waitcnt vmcnt(0)" ::: "memory");
  unsigned* cnt = (unsigned*)(ws + WS_CNT + (size_t)tg * 256);
  if (lane == 0) __hip_atomic_fetch_add(cnt, 1u, __ATOMIC_RELAXED, __HIP_MEMORY_SCOPE_AGENT);
  while (__builtin_amdgcn_readfirstlane(__hip_atomic_load(cnt, __ATOMIC_RELAXED, __HIP_MEMORY_SCOPE_AGENT)) < 16u) __builtin_amdgcn_s_sleep(1);
  __builtin_amdgcn_fence(__ATOMIC_ACQUIRE, "agent");
  f32x16 fin = j == 0 ? acc[0][0] : (j == 1 ? acc[0][1] : (j == 2 ? acc[1][0] : acc[1][1]));
#pragma unroll
  for (int jj = 0; jj < 4; ++jj) {
    if (jj != j) {
#pragma unroll
      for (int qd = 0; qd < 4; ++qd) {
        const f32x4 v = __builtin_bit_cast(f32x4, __builtin_amdgcn_raw_buffer_load_b128(sS, so + (unsigned)((j * 4 + qd) * 1024), (unsigned)jj * 65536u, 16));
#pragma unroll
        for (int e = 0; e < 4; ++e) fin[4 * qd + e] += v[e];
      }
    }
  }
  u16* op = out + (size_t)(128 * mi + 64 * wm + 32 * (j >> 1) + r32) * 512 + 128 * ni + 64 * wn + 32 * (j & 1) + 8 * hh;
  *(u32x4*)(op) = pack8<0>(fin);
  *(u32x4*)(op + 16) = pack8<1>(fin);
}

extern "C" void kernel_launch(void* const* d_in, const int* in_sizes, int n_in, void* d_out, int out_size, void* d_ws, size_t ws_size, hipStream_t stream) {
  const u16* x = (const u16*)d_in[0]; const u16* ckv = (const u16*)d_in[1]; const u16* wq = (const u16*)d_in[2];
  const u16* wuk = (const u16*)d_in[3]; const u16* wuv = (const u16*)d_in[4]; const u16* wo_a = (const u16*)d_in[5];
  const u16* wqkv = (const u16*)d_in[7]; const u16* wphi = (const u16*)d_in[8]; const u16* wo_b = (const u16*)d_in[9]; const u16* gatew = (const u16*)d_in[10];
  (void)hipFuncSetAttribute((const void*)kP, hipFuncAttributeMaxDynamicSharedMemorySize, 163840);
  (void)hipFuncSetAttribute((const void*)kA, hipFuncAttributeMaxDynamicSharedMemorySize, 163840);
  (void)hipFuncSetAttribute((const void*)kO, hipFuncAttributeMaxDynamicSharedMemorySize, 131072);
  kP<<<256, 512, 132096, stream>>>(x, ckv, wq, wuk, wuv, wqkv, wphi, gatew, (char*)d_ws);
  kA<<<256, 512, 163840, stream>>>((char*)d_ws);
  kO<<<256, 512, 131072, stream>>>(wo_a, wo_b, (char*)d_ws, (u16*)d_out);
}
```

```cpp
#define PRIO_M
#include <hip/hip_runtime.h>
#include <stdint.h>

typedef unsigned short u16;
typedef unsigned long long u64;
typedef __bf16 bf16x8 __attribute__((ext_vector_type(8)));
typedef __bf16 bf16x2 __attribute__((ext_vector_type(2)));
typedef float f32x2 __attribute__((ext_vector_type(2)));
typedef float f32x4 __attribute__((ext_vector_type(4)));
typedef float f32x16 __attribute__((ext_vector_type(16)));
typedef unsigned u32x2 __attribute__((ext_vector_type(2)));
typedef unsigned u32x4 __attribute__((ext_vector_type(4)));
typedef short s16x4 __attribute__((ext_vector_type(4)));
typedef short s16x8 __attribute__((ext_vector_type(8)));
typedef __amdgpu_buffer_rsrc_t srd_t;
#define LAS __attribute__((address_space(3)))
#define DI __device__ __forceinline__
#define SBAR() __builtin_amdgcn_sched_barrier(0)
#define MFMA(a, b, c) __builtin_amdgcn_mfma_f32_32x32x16_bf16((a), (b), (c), 0, 0, 0)

constexpr size_t MB = 1024 * 1024;
constexpr size_t WS_KF = 0;
constexpr size_t WS_VF = 16 * MB;
constexpr size_t WS_QF = 32 * MB;
constexpr size_t WS_CF = 48 * MB;
constexpr size_t WS_PQ = 68 * MB;
constexpr size_t WS_KVP = 72 * MB;
constexpr size_t WS_GW = 73 * MB;
constexpr size_t WS_ST = 74 * MB;
constexpr size_t WS_SLAB = 76 * MB;
constexpr size_t WS_CNT = 92 * MB;
constexpr size_t WS_FLG = 93 * MB;
constexpr size_t WS_DBG = 100 * MB;

constexpr float QSCALE = 0.08838834764831845f * 1.4426950408889634f;
constexpr float THR = 8.0f;

DI unsigned cvtpk(float lo, float hi) { f32x2 v = {lo, hi}; bf16x2 b = __builtin_convertvector(v, bf16x2); return __builtin_bit_cast(unsigned, b); }
template <int S> DI u32x4 pack8(const f32x16& x) {
  return (u32x4){cvtpk(x[8 * S + 0], x[8 * S + 1]), cvtpk(x[8 * S + 2], x[8 * S + 3]), cvtpk(x[8 * S + 4], x[8 * S + 5]), cvtpk(x[8 * S + 6], x[8 * S + 7])};
}
DI bf16x8 asbf(u32x4 v) { return __builtin_bit_cast(bf16x8, v); }
DI float swap32_max(float v) { auto rr = __builtin_amdgcn_permlane32_swap(__float_as_uint(v), __float_as_uint(v), false, false); return fmaxf(__uint_as_float(rr[0]), __uint_as_float(rr[1])); }
DI float swap32_add(float v) { auto rr = __builtin_amdgcn_permlane32_swap(__float_as_uint(v), __float_as_uint(v), false, false); return __uint_as_float(rr[0]) + __uint_as_float(rr[1]); }
DI float swap32_lo(float v) { auto rr = __builtin_amdgcn_permlane32_swap(__float_as_uint(v), __float_as_uint(v), false, false); return __uint_as_float(rr[0]); }
DI float bf16round(float x) { return __uint_as_float(cvtpk(x, 0.f) << 16); }
DI srd_t mksrd(const void* p, unsigned bytes) { return __builtin_amdgcn_make_buffer_rsrc((void*)p, (short)0, (int)bytes, 0x00020000); }
DI unsigned rfl(unsigned x) { return (unsigned)__builtin_amdgcn_readfirstlane((int)x); }
template <int OFF> DI void dma16o(unsigned m0v, unsigned voff, srd_t srd, unsigned soff) {
  asm volatile("s_mov_b32 m0, %0\n\ts_nop 0\n\tbuffer_load_dwordx4 %1, %2, %3 offen offset:%4 lds" :: "s"(m0v), "v"(voff), "s"(srd), "s"(soff), "i"(OFF) : "memory");
}
template <int OFF> DI void dma16n(unsigned voff, srd_t srd, unsigned soff) {
  asm volatile("buffer_load_dwordx4 %0, %1, %2 offen offset:%3 lds" :: "v"(voff), "s"(srd), "s"(soff), "i"(OFF) : "memory");
}
template <unsigned MOFF, unsigned SOFF> DI void dma16c(unsigned mb, unsigned voff, srd_t srd, unsigned sb) {
  unsigned t;
  asm volatile("s_add_u32 m0, %1, %5\n\ts_add_u32 %0, %4, %6\n\tbuffer_load_dwordx4 %2, %3, %0 offen lds" : "=&s"(t) : "s"(mb), "v"(voff), "s"(srd), "s"(sb), "i"(MOFF), "i"(SOFF) : "memory", "scc");
}
template <unsigned MOFF, unsigned SOFF> DI void dma16m(unsigned mb, unsigned voff, srd_t srd, unsigned sb) {
  unsigned t;
  asm volatile("s_add_u32 m0, %1, %5\n\ts_add_u32 %0, %4, %6\n\ts_mov_b32 exec_hi, 0x33333333\n\tbuffer_load_dwordx4 %2, %3, %0 offen lds\n\ts_mov_b32 exec_hi, -1"
               : "=&s"(t) : "s"(mb), "v"(voff), "s"(srd), "s"(sb), "i"(MOFF), "i"(SOFF) : "memory", "scc");
}
template <int N> DI void wait_bar() { asm volatile("s_waitcnt vmcnt(%0) lgkmcnt(0)\n\ts_barrier" :: "i"(N) : "memory"); }
template <int N> DI void wait_vm() { asm volatile("s_waitcnt vmcnt(%0)" :: "i"(N) : "memory"); }
#define LBAR() asm volatile("s_waitcnt lgkmcnt(0)\n\ts_barrier" ::: "memory")
#define LWAIT() asm volatile("s_waitcnt lgkmcnt(0)" ::: "memory")
#define NOP16() asm volatile("s_nop 7\n\ts_nop 7" ::: "memory")
DI bf16x8 ldsfrag(LAS const char* p) { return *(LAS const bf16x8*)p; }
DI bf16x8 trfrag(LAS const char* p0, LAS const char* p1) {
  s16x4 a = __builtin_amdgcn_ds_read_tr16_b64_v4i16((LAS s16x4*)p0);
  s16x4 b = __builtin_amdgcn_ds_read_tr16_b64_v4i16((LAS s16x4*)p1);
  return __builtin_bit_cast(bf16x8, __builtin_shufflevector(a, b, 0, 1, 2, 3, 4, 5, 6, 7));
}
DI void st128(u32x4 v, srd_t s, unsigned vo, unsigned so) { __builtin_amdgcn_raw_buffer_store_b128(v, s, vo + so, 0, 0); }
DI void st128_wt(u32x4 v, srd_t s, unsigned vo, unsigned so) { __builtin_amdgcn_raw_buffer_store_b128(v, s, vo + so, 0, 16); }
#define TS(x)

struct PCtx {
  srd_t sA1, sA2, sUK, sUV, sWQ, sWB, sWP, sK, sV;
  unsigned voffA[2], voffB, voffS, voff;
  unsigned soA1, soA2, soB, soS, soKa, soKb, so2, mA, mB2, m2;
  unsigned gv[8];
  u32x2 fl; u64 tag; const u64* flg;
  u32x4 wp;
  int w, lane, h;
  LAS char* L;
};
#define TS3(x) do { SBAR(); asm volatile("s_memtime %0\n\ts_waitcnt lgkmcnt(0)" : "=s"(x) :: "memory"); SBAR(); } while (0)
template <int T, int I> DI void p_pieceA(const PCtx& c) {
  if constexpr (T < 16) {
    constexpr unsigned st = (unsigned)(T % 3) * 32768u, kt = (unsigned)(T & 7);
    if constexpr (T < 8) dma16c<st + I * 1024u, I * 8192u + kt * 128u>(c.mA, c.voffA[I & 1], c.sA1, c.soA1);
    else                 dma16c<st + I * 1024u, I * 8192u + kt * 128u>(c.mA, c.voffA[I & 1], c.sA2, c.soA2);
  }
}
template <int T, int I> DI void p_pieceB(const PCtx& c) {
  constexpr unsigned st = 98304u + (unsigned)(T & 1) * 32768u, kt = (unsigned)(T & 7);
  if constexpr (T < 8) {
    if constexpr (I < 2) dma16c<st + I * 1024u, kt * 524288u + I * 128u>(c.mA, c.voffB, c.sUK, c.soB);
    else                 dma16c<st + I * 1024u, kt * 524288u + (I - 2) * 128u>(c.mA, c.voffB, c.sUV, c.soB);
  } else if constexpr (T < 16) {
    if constexpr (I < 2) dma16c<st + I * 1024u, kt * 524288u + I * 128u>(c.mB2, c.voffB, c.sWQ, c.soB);
    else if constexpr (I == 2) dma16m<st + 2048u, kt * 196608u>(c.mB2, c.voffS, c.sWB, c.soS);
    else if (c.w == 7) *(LAS u16*)(c.L + st + ((c.lane >> 3) * 6 + 5) * 512 + (c.lane & 7) * 64 + 32) = (u16)c.gv[kt];
  }
}
DI void poll_spin(const u64* f, u64 tag, int lane) {
  while (!__all(__hip_atomic_load(f + (lane & 7), __ATOMIC_RELAXED, __HIP_MEMORY_SCOPE_AGENT) == tag)) __builtin_amdgcn_s_sleep(2);
}
DI void poll_issue(u32x2& fl, const u64* f, int lane) { asm volatile("global_load_dwordx2 %0, %1, off sc1" : "+v"(fl) : "v"(f + (lane & 7)) : "memory"); }
DI void poll_check(u32x2& fl, const u64* f, u64 tag, int lane) {
  asm volatile("" : "+v"(fl));
  if (!__all(fl[0] == (unsigned)tag && fl[1] == (unsigned)(tag >> 32))) poll_spin(f, tag, lane);
  asm volatile("buffer_inv sc1" ::: "memory");
}
template <int B> DI void p_hook(PCtx& c) {
  if constexpr (B == 9) { if (c.w == 0) dma16c<155648u, 0u>(c.mA, c.voff, c.sWP, (unsigned)c.h * 512u); }
  if constexpr (B == 10) { if (c.w == 0 && c.lane == 0) __hip_atomic_store((u64*)c.flg + (blockIdx.x >> 5), c.tag, __ATOMIC_RELAXED, __HIP_MEMORY_SCOPE_AGENT); }
  if constexpr (B == 11) { if (c.w == 0) poll_issue(c.fl, c.flg, c.lane); }
  if constexpr (B == 12) { if (c.w == 0) poll_check(c.fl, c.flg, c.tag, c.lane); }
  if constexpr (B == 13) {
    if (c.w >= 4) {
      const int r32 = c.lane & 31, hh = c.lane >> 5; LAS const u16* wb = (LAS const u16*)(c.L + 155648) + (r32 & 15); const unsigned sg = r32 >= 16 ? 0x80008000u : 0u;
#pragma unroll
      for (int jj = 0; jj < 4; ++jj) { const int d0 = 8 * (jj >> 1) + 4 * hh + 2 * (jj & 1); c.wp[jj] = ((unsigned)wb[d0 * 16] | ((unsigned)wb[(d0 + 1) * 16] << 16)) ^ sg; }
    }
  }
  if constexpr (B == 14) {
    dma16c<32768u, 0u>(c.mA, c.voff, c.sK, c.soKa); dma16c<32768u + 1024u, 1024u>(c.mA, c.voff, c.sK, c.soKa);
    dma16c<32768u + 2048u, 2048u>(c.mA, c.voff, c.sK, c.soKa); dma16c<32768u + 3072u, 3072u>(c.mA, c.voff, c.sK, c.soKa);
  }
  if constexpr (B == 15) {
    dma16c<65536u, 0u>(c.m2, c.voff, c.sV, c.so2); dma16c<65536u + 1024u, 1024u>(c.m2, c.voff, c.sV, c.so2);
    dma16c<98304u, 16384u>(c.mA, c.voff, c.sK, c.soKa); dma16c<98304u + 1024u, 16384u + 1024u>(c.mA, c.voff, c.sK, c.soKa);
    dma16c<98304u + 2048u, 16384u + 2048u>(c.mA, c.voff, c.sK, c.soKa); dma16c<98304u + 3072u, 16384u + 3072u>(c.mA, c.voff, c.sK, c.soKa);
  }
  if constexpr (B == 16) {
    dma16c<0u, 16384u>(c.m2, c.voff, c.sV, c.so2); dma16c<1024u, 16384u + 1024u>(c.m2, c.voff, c.sV, c.so2);
  }
}

template <int PASS, int MT, int NT, int NSUB, bool TRANS>
struct PPass {
  PCtx& c; f32x16 (&acc)[MT][NT];
  unsigned aB[2][4], bB;
  bf16x8 fa[2][MT], fb[2][NT];
  template <int SET, int T, int KS> DI void load() {
    constexpr int bo = (T & 1) * 32768 + KS * (2 * NSUB * 512), sl = T % 3;
#pragma unroll
    for (int nt = 0; nt < NT; ++nt) fb[SET][nt] = trfrag(c.L + bB + bo + nt * 512, c.L + bB + bo + nt * 512 + 256);
#pragma unroll
    for (int mt = 0; mt < MT; ++mt) fa[SET][mt] = ldsfrag(c.L + aB[sl >> 1][KS] + (sl & 1) * 32768 + mt * 4096);
  }
  template <int SET, int I> DI void mma1() {
    if constexpr (I < MT * NT) {
      constexpr int mt = I / NT, nt = I % NT;
      acc[mt][nt] = TRANS ? MFMA(fb[SET][nt], fa[SET][mt], acc[mt][nt]) : MFMA(fa[SET][mt], fb[SET][nt], acc[mt][nt]);
    }
  }
  template <int SET> DI void mma() { mma1<SET, 0>(); mma1<SET, 1>(); mma1<SET, 2>(); mma1<SET, 3>(); mma1<SET, 4>(); mma1<SET, 5>(); mma1<SET, 6>(); mma1<SET, 7>(); }
  template <int SET, int T, int I0> DI void mmaA() {
    mma1<SET, 0>(); mma1<SET, 1>(); SBAR(); p_pieceA<T, I0>(c); SBAR(); mma1<SET, 2>(); mma1<SET, 3>(); mma1<SET, 4>(); SBAR(); p_pieceA<T, I0 + 1>(c); SBAR(); mma1<SET, 5>(); mma1<SET, 6>(); mma1<SET, 7>();
  }
  template <int t> DI void tile() {
    constexpr int T = PASS * 8 + t;
    SBAR(); load<1, T, 1>(); SBAR(); mmaA<0, T + 2, 0>();
    SBAR(); load<0, T, 2>(); SBAR(); mmaA<1, T + 2, 2>();
    SBAR(); load<1, T, 3>(); SBAR(); mma<0>();
    SBAR();
    if constexpr (T == 8) wait_bar<20>(); else if constexpr (T == 15) LBAR(); else wait_bar<4>();
    SBAR();
    if constexpr (t < 7) load<0, T + 1, 0>();
    SBAR();
    mma1<1, 0>(); p_pieceB<T + 2, 0>(c); SBAR();
    mma1<1, 1>(); SBAR(); mma1<1, 2>(); p_pieceB<T + 2, 1>(c); SBAR();
    mma1<1, 3>(); SBAR(); mma1<1, 4>(); p_pieceB<T + 2, 2>(c); SBAR();
    mma1<1, 5>(); SBAR(); mma1<1, 6>(); p_pieceB<T + 2, 3>(c); SBAR();
    mma1<1, 7>(); SBAR();
    p_hook<T + 1>(c); SBAR();
  }
  DI void run(unsigned arow0, unsigned nt0) {
    const int lane = c.lane, r = lane & 31, hh = lane >> 5, i16 = lane & 15;
#pragma unroll
    for (int ks = 0; ks < 4; ++ks) { aB[0][ks] = (arow0 + r) * 128u + (unsigned)(((2 * ks + hh) ^ ((r >> 1) & 7)) * 16); aB[1][ks] = aB[0][ks] + 65536u; asm volatile("" : "+v"(aB[1][ks])); }
    bB = 98304u + (unsigned)(hh * NSUB + nt0) * 512u + (unsigned)((i16 >> 2) * 64 + ((lane >> 4) & 1) * 32 + (i16 & 3) * 8); asm volatile("" : "+v"(bB));
#pragma unroll
    for (int mt = 0; mt < MT; ++mt)
#pragma unroll
      for (int nt = 0; nt < NT; ++nt) acc[mt][nt] = (f32x16){};
    load<0, PASS * 8, 0>();
    tile<0>(); tile<1>(); tile<2>(); tile<3>(); tile<4>(); tile<5>(); tile<6>(); tile<7>();
  }
};
template <int PASS, int MT, int NT, int NSUB, bool TRANS>
DI void p_pass(PCtx& c, f32x16 (&acc)[MT][NT], unsigned arow0, unsigned nt0) { PPass<PASS, MT, NT, NSUB, TRANS> p{c, acc}; p.run(arow0, nt0); }

DI void softmax2(f32x16& x) {
#pragma unroll
  for (int s = 0; s < 2; ++s) {
    float v[8];
#pragma unroll
    for (int j = 0; j < 8; ++j) v[j] = bf16round(x[8 * s + j]);
    float mx = v[0];
#pragma unroll
    for (int j = 1; j < 8; ++j) mx = fmaxf(mx, v[j]);
    mx = swap32_max(mx);
    float sum = 0.f;
#pragma unroll
    for (int j = 0; j < 8; ++j) { v[j] = __builtin_amdgcn_exp2f((v[j] - mx) * 1.4426950408889634f); sum += v[j]; }
    sum = swap32_add(sum);
    const float inv = 1.0f / sum;
#pragma unroll
    for (int j = 0; j < 8; ++j) x[8 * s + j] = v[j] * inv;
  }
}

DI void mfma_acc(f32x16& c, bf16x8 a, bf16x8 b) { asm volatile("v_mfma_f32_32x32x16_bf16 %0, %1, %2, %0" : "+v"(c) : "v"(a), "v"(b)); }
DI void mfma_acc2(f32x16& c, bf16x8 a, bf16x8 b, bf16x8 nx) { asm volatile("v_mfma_f32_32x32x16_bf16 %0, %1, %2, %0" : "+v"(c) : "v"(a), "v"(b), "v"(nx)); }
DI void mfma_init2(f32x16& c, bf16x8 a, bf16x8 b, const f32x16& ci, bf16x8 nx) { asm volatile("v_mfma_f32_32x32x16_bf16 %0, %1, %2, %3" : "=&v"(c) : "v"(a), "v"(b), "v"(ci), "v"(nx)); }
DI float sum2(unsigned pk, float acc) { return __builtin_amdgcn_fdot2_f32_bf16(__builtin_bit_cast(bf16x2, pk), __builtin_bit_cast(bf16x2, 0x3f803f80u), acc, false); }
DI float sum8(bf16x8 p, float acc) { const u32x4 u = __builtin_bit_cast(u32x4, p); return sum2(u[3], sum2(u[2], sum2(u[1], sum2(u[0], acc)))); }
DI float max3f(float a, float b, float c) { float r; asm("v_max3_f32 %0, %1, %2, %3" : "=v"(r) : "v"(a), "v"(b), "v"(c)); return r; }
DI float max2f(float a, float b) { float r; asm("v_max_f32_e32 %0, %1, %2" : "=v"(r) : "v"(a), "v"(b)); return r; }
#define TS2X(a)
#define STP_PARAM
#define STP_ARG
#define TS2(acc)
template <bool DOQK, bool DOPV>
DI void a_mphase(LAS const char* kp, LAS const char* vp, const bf16x8 (&q)[8], bf16x8 (&p)[4], f32x16 (&S)[2], f32x16 (&O)[4], const f32x16& negm, float& l, float& pm0 STP_PARAM) {
  constexpr int N = (DOQK ? 16 : 0) + (DOPV ? 16 : 0), D = 6;
  bf16x8 f[N];
  unsigned pw[8]; float ls = 0.f, e0 = 0.f;
#define A_ISQK(i) (DOQK && (!DOPV || (((i) >> 3) & 1) == 0))
#define A_IX(i) ((DOPV && DOQK) ? (((i) >> 4) * 8 + ((i) & 7)) : (i))
#define A_LD(i) do { if constexpr ((i) < N) { if constexpr (A_ISQK(i)) f[i] = ldsfrag(kp + A_IX(i) * 1024); \
                     else f[i] = ldsfrag(vp + ((A_IX(i) & 3) * 4 + (A_IX(i) >> 2)) * 1024); } } while (0)
#define A_MM(i) do { if constexpr (A_ISQK(i)) { if constexpr ((A_IX(i) & 7) == 0) mfma_init2(S[A_IX(i) >> 3], f[i], q[0], negm, f[(i) + 1]); \
                       else if constexpr ((i) & 1) mfma_acc(S[A_IX(i) >> 3], f[i], q[A_IX(i) & 7]); else mfma_acc2(S[A_IX(i) >> 3], f[i], q[A_IX(i) & 7], f[((i) + 1) % N]); } \
                     else if constexpr ((i) & 1) mfma_acc(O[A_IX(i) & 3], f[i], p[A_IX(i) >> 2]); else mfma_acc2(O[A_IX(i) & 3], f[i], p[A_IX(i) >> 2], f[((i) + 1) % N]); } while (0)
#define A_SM(e) do { const float x_ = __builtin_amdgcn_exp2f(S[1][(e) & 15]); ls += x_; \
                     if constexpr ((e) & 1) { pw[((e) & 15) >> 1] = cvtpk(e0, x_); asm volatile("" : "+v"(pw[((e) & 15) >> 1]), "+v"(ls)); } else { e0 = x_; asm volatile("" : "+v"(e0), "+v"(ls)); } \
                     if constexpr ((e) == 7) p[2] = asbf((u32x4){pw[0], pw[1], pw[2], pw[3]}); if constexpr ((e) == 15) p[3] = asbf((u32x4){pw[4], pw[5], pw[6], pw[7]}); } while (0)
#define A_MX(j) do { if constexpr ((j) == 0) pm0 = max2f(S[0][0], S[0][1]); else pm0 = max3f(pm0, S[0][2 * ((j) & 7)], S[0][2 * ((j) & 7) + 1]); asm volatile("" : "+v"(pm0)); } while (0)
#define A_STEP(i) do { if constexpr ((i) < N) { A_LD((i) + D); if constexpr ((i) == N - 4) { SBAR(); TS2(a); LBAR(); TS2(b); SBAR(); } A_MM(i); \
                     if constexpr (DOQK && DOPV && (i) < 16) A_SM(i); \
                     if constexpr (DOQK && DOPV && (i) >= 16 && (i) < 24) A_MX((i) - 16); \
                     if constexpr (!DOQK && (i) < 8) { A_SM(2 * (i)); A_SM(2 * (i) + 1); } SBAR(); } } while (0)
  SBAR();
  __builtin_amdgcn_s_setprio(1);
  A_LD(0); A_LD(1); A_LD(2); A_LD(3); A_LD(4); A_LD(5); SBAR();
  A_STEP(0); A_STEP(1); A_STEP(2); A_STEP(3); A_STEP(4); A_STEP(5); A_STEP(6); A_STEP(7);
  A_STEP(8); A_STEP(9); A_STEP(10); A_STEP(11); A_STEP(12); A_STEP(13); A_STEP(14); A_STEP(15);
  A_STEP(16); A_STEP(17); A_STEP(18); A_STEP(19); A_STEP(20); A_STEP(21); A_STEP(22); A_STEP(23);
  A_STEP(24); A_STEP(25); A_STEP(26); A_STEP(27); A_STEP(28); A_STEP(29); A_STEP(30); A_STEP(31);
  if constexpr (DOPV) l += ls;
  if constexpr (!DOPV) { NOP16(); SBAR(); A_MX(0); A_MX(1); A_MX(2); A_MX(3); A_MX(4); A_MX(5); A_MX(6); A_MX(7); }
  __builtin_amdgcn_s_setprio(0);
  TS2X(c);
#undef A_ISQK
#undef A_IX
#undef A_LD
#undef A_MM
#undef A_SM
#undef A_MX
#undef A_STEP
}

DI void a_finish(const f32x16& a0, const f32x16& a1, float ma, float la, LAS const char* xr, float mb, float lb, float gate, srd_t sC, unsigned voff, unsigned soff) {
  const float mt = fmaxf(ma, mb);
  const float fa = __builtin_amdgcn_exp2f(ma - mt), fb = __builtin_amdgcn_exp2f(mb - mt);
  const float sc = gate / (la * fa + lb * fb);
  const float wa = fa * sc, wb = fb * sc;
#pragma unroll
  for (int t = 0; t < 2; ++t) {
    const f32x16& a = t ? a1 : a0;
#pragma unroll
    for (int s = 0; s < 2; ++s) {
      const f32x4 b0 = *(LAS const f32x4*)(xr + t * 4096 + s * 2048), b1 = *(LAS const f32x4*)(xr + t * 4096 + s * 2048 + 1024);
      const u32x4 o = {cvtpk(a[8 * s + 0] * wa + b0[0] * wb, a[8 * s + 1] * wa + b0[1] * wb), cvtpk(a[8 * s + 2] * wa + b0[2] * wb, a[8 * s + 3] * wa + b0[3] * wb),
                       cvtpk(a[8 * s + 4] * wa + b1[0] * wb, a[8 * s + 5] * wa + b1[1] * wb), cvtpk(a[8 * s + 6] * wa + b1[2] * wb, a[8 * s + 7] * wa + b1[3] * wb)};
      st128(o, sC, voff + (unsigned)((2 * t + s) * 1024), soff);
      SBAR();
    }
  }
}
DI void a_send(const f32x16& a0, const f32x16& a1, LAS char* xr) {
#pragma unroll
  for (int g = 0; g < 4; ++g) {
    *(LAS f32x4*)(xr + g * 1024) = (f32x4){a0[4 * g], a0[4 * g + 1], a0[4 * g + 2], a0[4 * g + 3]};
    *(LAS f32x4*)(xr + 4096 + g * 1024) = (f32x4){a1[4 * g], a1[4 * g + 1], a1[4 * g + 2], a1[4 * g + 3]};
  }
}

__global__ __launch_bounds__(512, 2) void kF(const u16* __restrict__ x, const u16* __restrict__ ckv, const u16* __restrict__ wq_, const u16* __restrict__ wuk,
                                             const u16* __restrict__ wuv, const u16* __restrict__ wqkv, const u16* __restrict__ wphi, const u16* __restrict__ gatew, char* __restrict__ ws) {
  extern __shared__ __attribute__((aligned(16))) char lds[];
  LAS char* L = (LAS char*)lds; const unsigned lds0 = (unsigned)(uintptr_t)L;
  const int tid = threadIdx.x, lane = tid & 63, w = __builtin_amdgcn_readfirstlane(tid >> 6);
  const int b = blockIdx.x, h = (b & 7) * 4 + ((b >> 3) & 3), r = b >> 5;
  const int g = w >> 2, wq = w & 3, hh = lane >> 5, r32 = lane & 31;
  const int X = 15 - r, Y = r, nX = 16 - r;
  constexpr int NS = 17;
  const unsigned voff = (unsigned)lane * 16u;
  TS(t0);
  bf16x8 q[8];
  u64 tag; const u64* flg = (const u64*)(ws + WS_FLG + (size_t)h * 256);
  const srd_t sK = mksrd(ws + WS_KF + (size_t)h * 32 * 16384, 32u * 16384u), sV = mksrd(ws + WS_VF + (size_t)h * 32 * 16384, 32u * 16384u);
  const srd_t sW = mksrd(ws + WS_QF, (unsigned)(WS_DBG - WS_QF));
  {
    PCtx c;
    c.fl = (u32x2){0u, 0u}; c.wp = (u32x4){0u, 0u, 0u, 0u};
    c.L = L; c.w = w; c.lane = lane; c.h = h; c.voff = voff; c.flg = flg; c.sK = sK; c.sV = sV;
    c.soA1 = rfl((unsigned)(256 * r + 32 * w) * 1024u);
    c.soA2 = rfl((unsigned)(w < 4 ? 128 * X + 32 * w : 128 * Y + 32 * (w - 4)) * 1024u);
    c.soB = rfl((unsigned)(8 * w) * 8192u + (unsigned)h * 256u);
    c.soS = rfl((unsigned)(8 * w) * 3072u + (unsigned)h * 32u);
    c.soKa = rfl((unsigned)(g * 32768 + wq * 4096)); c.so2 = rfl((unsigned)w * 2048u);
    c.mA = rfl(lds0 + (unsigned)w * 4096u); c.mB2 = rfl(lds0 + (unsigned)w * 3072u); c.m2 = rfl(lds0 + (unsigned)w * 2048u);
    c.sA1 = mksrd(ckv, 2048u * 1024u); c.sA2 = mksrd(x, 2048u * 1024u);
    c.sUK = mksrd(wuk, 512u * 8192u); c.sUV = mksrd(wuv, 512u * 8192u); c.sWQ = mksrd(wq_, 512u * 8192u); c.sWB = mksrd(wqkv, 512u * 3072u); c.sWP = mksrd(wphi, 32u * 512u);
#pragma unroll
    for (int k = 0; k < 2; ++k) c.voffA[k] = (unsigned)((lane >> 3) * 1024 + (((lane & 7) ^ ((4 * k + (lane >> 4)) & 7)) * 16));
    c.voffB = (unsigned)(((lane >> 2) & 7) * 8192 + (lane >> 5) * 64 + (lane & 3) * 16);
    { const int ch = lane & 3, sub = lane >> 5;
      c.voffS = (unsigned)(((lane >> 2) & 7) * 3072 + (sub == 0 ? (ch < 2 ? ch * 16 : 1024 + (ch - 2) * 16) : 2048 + (ch & 1) * 16)); }
    if (b == 0 && tid < 64) *(unsigned*)(ws + WS_CNT + tid * 256) = 0u;
    u32x2 ep;
    asm volatile("global_load_dwordx2 %0, %1, off sc1" : "=v"(ep) : "v"(flg + 16) : "memory");
#pragma unroll
    for (int k = 0; k < 8; ++k) asm volatile("global_load_ushort %0, %1, off offset:%2" : "=v"(c.gv[k]) : "v"(gatew + lane), "i"(k * 128) : "memory");
    p_pieceA<0, 0>(c); p_pieceA<0, 1>(c); p_pieceA<0, 2>(c); p_pieceA<0, 3>(c);
    p_pieceB<0, 0>(c); p_pieceB<0, 1>(c); p_pieceB<0, 2>(c); p_pieceB<0, 3>(c);
    p_pieceB<1, 0>(c); p_pieceB<1, 1>(c); p_pieceB<1, 2>(c); p_pieceB<1, 3>(c);
    p_pieceA<1, 0>(c); p_pieceA<1, 1>(c); p_pieceA<1, 2>(c); p_pieceA<1, 3>(c);
    wait_bar<8>();
    asm volatile("" : "+v"(ep), "+v"(c.gv[0]), "+v"(c.gv[1]), "+v"(c.gv[2]), "+v"(c.gv[3]), "+v"(c.gv[4]), "+v"(c.gv[5]), "+v"(c.gv[6]), "+v"(c.gv[7]));
    tag = (((u64)rfl(ep[1]) << 32) | rfl(ep[0])) + 1ull; c.tag = tag;
    {
      f32x16 acc[4][2];
      const int wm = (w >> 1) & 1, wn = w & 1;
      if (g == 0) p_pass<0, 4, 2, 8, true>(c, acc, 128u * wm, 2u * wn);
      else        p_pass<0, 4, 2, 8, false>(c, acc, 128u * wm, 4u + 2u * wn);
      const srd_t sO = g == 0 ? sK : sV;
#pragma unroll
      for (int mt = 0; mt < 4; ++mt) {
        const int kbg = 8 * r + 4 * wm + mt, t = kbg >> 1, kb = kbg & 1;
#pragma unroll
        for (int nt = 0; nt < 2; ++nt) {
          const int dt = 2 * wn + nt;
          const unsigned so = (unsigned)(t * 16384 + (g == 0 ? kb * 8 + 2 * dt : dt * 4 + 2 * kb) * 1024);
          st128_wt(pack8<0>(acc[mt][nt]), sO, voff, so);
          st128_wt(pack8<1>(acc[mt][nt]), sO, voff + 1024u, so);
        }
      }
    }
    TS(t1);
    f32x16 acc[2][3];
    p_pass<1, 2, 3, 6, true>(c, acc, 64u * wq, 3u * g);
    TS(t2);
    if (wq < 2) {
#pragma unroll
      for (int mt = 0; mt < 2; ++mt)
#pragma unroll
        for (int nt = 0; nt < 3; ++nt)
          if (g == 0 || nt == 0) {
            const f32x16 v = acc[mt][nt] * QSCALE; LAS char* qd = L + 131072 + ((2 * wq + mt) * 8 + 2 * (3 * g + nt)) * 1024 + lane * 16;
            *(LAS u32x4*)qd = pack8<0>(v); *(LAS u32x4*)(qd + 1024) = pack8<1>(v);
          }
    }
    wait_bar<2>();
#pragma unroll
    for (int ks = 0; ks < 8; ++ks) q[ks] = ldsfrag(L + 131072 + wq * 8192 + ks * 1024 + lane * 16);
    if (wq >= 2) {
#pragma unroll
      for (int mt = 0; mt < 2; ++mt)
#pragma unroll
        for (int nt = 0; nt < 3; ++nt)
          if (g == 0 || nt == 0) {
            const f32x16 v = acc[mt][nt] * QSCALE; const unsigned so = (unsigned)(b * 32768 + ((2 * (wq - 2) + mt) * 8 + 2 * (3 * g + nt)) * 1024);
            st128(pack8<0>(v), sW, voff, so); st128(pack8<1>(v), sW, voff + 1024u, so);
          }
    }
    if (g == 1) {
      LAS char* Ik = L + 16384 + wq * 4096; LAS char* Iv = Ik + 2048;
      const int i16 = lane & 15;
      const unsigned tb = (unsigned)((8 * hh + (i16 >> 2)) * 64 + ((lane >> 4) & 1) * 32 + (i16 & 3) * 8);
      f32x16 kv = {};
#pragma unroll
      for (int mt = 0; mt < 2; ++mt) {
        f32x16 pq = MFMA(asbf(c.wp), asbf(pack8<0>(acc[mt][1])), (f32x16){});
        f32x16 pk = MFMA(asbf(c.wp), asbf(pack8<1>(acc[mt][1])), (f32x16){});
        softmax2(pq); softmax2(pk);
        const unsigned so = (unsigned)(WS_PQ - WS_QF) + (unsigned)(b * 16384 + (2 * wq + mt) * 2048);
        st128(pack8<0>(pq), sW, voff, so); st128(pack8<1>(pq), sW, voff + 1024u, so);
#pragma unroll
        for (int k = 0; k < 4; ++k) *(LAS u32x2*)(Ik + r32 * 64 + 16 * k + 8 * hh) = (u32x2){cvtpk(pk[4 * k], pk[4 * k + 1]), cvtpk(pk[4 * k + 2], pk[4 * k + 3])};
        const f32x16& tv = acc[mt][2];
        *(LAS u32x2*)(Iv + r32 * 64 + 8 * hh) = (u32x2){cvtpk(tv[0], tv[1]), cvtpk(tv[2], tv[3])};
        *(LAS u32x2*)(Iv + r32 * 64 + 16 + 8 * hh) = (u32x2){cvtpk(tv[4], tv[5]), cvtpk(tv[6], tv[7])};
        *(LAS u32x2*)(Iv + r32 * 64 + 32 + 8 * hh) = (u32x2){hh == 0 ? 0x00003f80u : 0u, 0u};
        *(LAS u32x2*)(Iv + r32 * 64 + 48 + 8 * hh) = (u32x2){0u, 0u};
        const float gt = 1.0f / (1.0f + __expf(-swap32_lo(tv[8])));
        __builtin_amdgcn_raw_buffer_store_b32(__float_as_uint(gt), sW, (unsigned)r32 * 4u, (unsigned)(WS_GW - WS_QF) + (unsigned)(b * 1024 + (64 * wq + 32 * mt) * 4), 0);
        LWAIT();
#pragma unroll
        for (int ks = 0; ks < 2; ++ks) kv = MFMA(trfrag(Ik + tb + ks * 1024, Ik + tb + ks * 1024 + 256), trfrag(Iv + tb + ks * 1024, Iv + tb + ks * 1024 + 256), kv);
        LWAIT();
      }
#pragma unroll
      for (int k = 0; k < 4; ++k) *(LAS f32x4*)(Ik + k * 1024 + lane * 16) = (f32x4){kv[4 * k], kv[4 * k + 1], kv[4 * k + 2], kv[4 * k + 3]};
      LBAR();
      f32x4 s4 = *(LAS const f32x4*)(L + 16384 + wq * 1024 + lane * 16);
#pragma unroll
      for (int k = 1; k < 4; ++k) s4 += *(LAS const f32x4*)(L + 16384 + k * 4096 + wq * 1024 + lane * 16);
      st128_wt(__builtin_bit_cast(u32x4, s4), sW, voff, (unsigned)(WS_KVP - WS_QF) + (unsigned)((h * 8 + r) * 4096 + wq * 1024));
    }
  }
  TS(t3);
  const unsigned ringK = rfl(lds0 + (g ? 98304u : 32768u) + (unsigned)wq * 4096u), ringV = rfl(lds0 + (g ? 0u : 65536u) + (unsigned)wq * 4096u);
  f32x16 O[4], S[2], keep0 = {}, keep1 = {}, negm = {};
#pragma unroll
  for (int d = 0; d < 4; ++d) O[d] = (f32x16){};
  bf16x8 p[4];
  float m = 0.f, l = 0.f, mk = 0.f, lk = 0.f, pm0 = 0.f;
  float gate[2] = {0.f, 0.f}; f32x2 ps = {0.f, 0.f};
  u32x2 fl2 = {0u, 0u};
  LAS const char* kbase = L + (g ? 98304 : 32768) + lane * 16;
  LAS const char* vbase = L + (g ? 0 : 65536) + lane * 16;
  LAS char* xr = L + 131072 + wq * 8192 + lane * 16;
#define TILE(s) ((s) < nX ? 2 * (s) + g : 2 * ((s) - nX) + g)
#define CF_SO(blkrow, half) ((unsigned)(WS_CF - WS_QF) + (unsigned)(((4 * (blkrow) + wq) * 288 + 8 * h + 4 * (half)) * 1024))
#define ST_SO(wv) ((unsigned)(WS_ST - WS_QF) + (unsigned)(b * 2048 + (wv) * 256))
#define ST_LOAD(wv) asm volatile("buffer_load_dwordx2 %0, %1, %2, %3 offen" : "+v"(ps) : "v"((unsigned)r32 * 8u), "s"(sW), "s"(rfl(ST_SO(wv))) : "memory")
#define L_SOFTMAX(DIAG, FIRST, D0, D1, D2, D3, D4, D5, D6, D7) do { \
    if (DIAG) { int rel = 32 * wq + r32 - 64 * g - 4 * hh; \
      _Pragma("unroll") for (int e = 0; e < 16; ++e) { const int cc = (e & 3) + 8 * (e >> 2); if ((e & 3) == 0) { asm volatile("" : "+v"(rel)); SBAR(); } \
        if (cc > rel) S[0][e] = -__builtin_inff(); if (cc + 32 > rel) S[1][e] = -__builtin_inff(); } \
      pm0 = max2f(S[0][0], S[0][1]); _Pragma("unroll") for (int e = 2; e < 16; e += 2) pm0 = max3f(pm0, S[0][e], S[0][e + 1]); } \
    float pm = max3f(pm0, S[1][0], S[1][1]), pn = max3f(S[1][2], S[1][3], S[1][4]); \
    pm = max3f(pm, S[1][5], S[1][6]); pn = max3f(pn, S[1][7], S[1][8]); pm = max3f(pm, S[1][9], S[1][10]); pn = max3f(pn, S[1][11], S[1][12]); \
    pm = max3f(pm, S[1][13], S[1][14]); pm = max3f(pm, pn, S[1][15]); \
    { auto rr = __builtin_amdgcn_permlane32_swap(__float_as_uint(pm), __float_as_uint(pm), false, false); pm = max2f(__uint_as_float(rr[0]), __uint_as_float(rr[1])); } \
    SBAR(); TS2X(f); D0; SBAR(); \
    const bool first_ = (FIRST); \
    if (first_ || __builtin_expect(__any(pm > THR), 0)) { \
      NOP16(); SBAR(); \
      const float dl = fmaxf(pm, first_ ? -1e30f : 0.f); \
      m += dl; \
      if (!first_) { const float f = __builtin_amdgcn_exp2f(-dl); l *= f; _Pragma("unroll") for (int d = 0; d < 4; ++d) O[d] = O[d] * f; } \
      _Pragma("unroll") for (int e = 0; e < 16; ++e) { S[0][e] -= dl; S[1][e] -= dl; negm[e] = -m; } \
    } \
    TS2X(g); float ls0 = l, ls1 = 0.f; unsigned pw[8]; \
    SM_GRP(0, D1); SM_GRP(2, D2); SM_GRP(4, D3); SM_GRP(6, D4); SM_GRP(8, D5); SM_GRP(10, D6); SM_GRP(12, D7); SM_GRP(14, (void)0); \
    l = ls0 + ls1; \
    p[0] = asbf((u32x4){pw[0], pw[1], pw[2], pw[3]}); p[1] = asbf((u32x4){pw[4], pw[5], pw[6], pw[7]}); } while (0)
#define SM_GRP(e0, DMA) do { const float x0 = __builtin_amdgcn_exp2f(S[0][e0]), x1 = __builtin_amdgcn_exp2f(S[0][(e0) + 1]); \
      ls0 += x0; ls1 += x1; pw[(e0) >> 1] = cvtpk(x0, x1); asm volatile("" : "+v"(pw[(e0) >> 1]), "+v"(ls0), "+v"(ls1)); DMA; SBAR(); } while (0)
#define L_SUM() do { } while (0)
  float mo = 0.f, lo = 0.f;
  auto l_pre = [&](int s) {
    if (s == nX) {
      NOP16(); SBAR();
      mo = m; lo = swap32_add(l);
      if (g == 1) ST_LOAD(w - 4);
      __builtin_amdgcn_raw_buffer_store_b64(__builtin_bit_cast(u32x2, (f32x2){mo, lo}), sW, (unsigned)r32 * 8u, ST_SO(w), 0);
      if (g == 0) { a_send(O[2], O[3], xr); keep0 = O[0]; keep1 = O[1]; mk = mo; lk = lo; }
      m = 0.f; l = 0.f;
    }
    if (g == 0 && s == nX + 1) ST_LOAD(w + 4);
  };
  auto l_post = [&](int s) {
    if (s == nX) {
      if (g == 1) {
        wait_vm<8>(); asm volatile("" : "+v"(ps));
        a_finish(O[2], O[3], mo, lo, xr, ps[0], ps[1], gate[0], sW, voff, CF_SO(X, 1));
        LWAIT();
        a_send(O[0], O[1], xr);
      }
#pragma unroll
      for (int d = 0; d < 4; ++d) O[d] = (f32x16){};
    }
    if (g == 0 && s == nX + 1) {
      wait_vm<8>(); asm volatile("" : "+v"(ps));
      a_finish(keep0, keep1, mk, lk, xr, ps[0], ps[1], gate[0], sW, voff, CF_SO(X, 0));
    }
  };
  unsigned mK = ringK, mV = ringV + 16384u, oK = (unsigned)(4 + g) * 16384u + (unsigned)wq * 4096u, oV = (unsigned)(2 + g) * 16384u + (unsigned)wq * 4096u;
  LAS const char* kp = kbase + 16384; LAS const char* vp = vbase;
#define A_TAIL() do { \
    asm volatile("" : "+v"(p[0]), "+v"(p[1]), "+v"(l), "+v"(S[1])); \
    SBAR(); TS2(d); wait_bar<8>(); TS2(e); SBAR(); \
    a_mphase<true, true>(kp, vp, q, p, S, O, negm, l, pm0 STP_ARG); \
    mK ^= 16384u; mV ^= 16384u; kp = (LAS const char*)((unsigned)(uintptr_t)kp ^ 16384u); vp = (LAS const char*)((unsigned)(uintptr_t)vp ^ 16384u); } while (0)
#define A_FAST(FIRST) do { \
    if (!(FIRST)) L_SUM(); \
    L_SOFTMAX(false, FIRST, dma16o<0>(mK, voff, sK, oK), dma16n<1024>(voff, sK, oK), dma16n<2048>(voff, sK, oK), dma16n<3072>(voff, sK, oK), \
              dma16o<0>(mV, voff, sV, oV), dma16n<1024>(voff, sV, oV), dma16n<2048>(voff, sV, oV), dma16n<3072>(voff, sV, oV)); \
    oK += 32768u; oV += 32768u; TS2X(h); A_TAIL(); } while (0)
#define A_SLOW(s) do { \
    L_SUM(); l_pre(s); \
    oK = (s) < 15 ? (unsigned)TILE((s) + 2) * 16384u + (unsigned)wq * 4096u : (unsigned)(WS_KVP - WS_QF) + (unsigned)(h * 32768 + w * 4096); \
    oV = (unsigned)TILE((s) + 1) * 16384u + (unsigned)wq * 4096u; \
    const srd_t sKx = (s) < 15 ? sK : sW; \
    L_SOFTMAX((s) == nX - 1, (s) == nX, dma16o<0>(mK, voff, sKx, oK), dma16n<1024>(voff, sKx, oK), dma16n<2048>(voff, sKx, oK), dma16n<3072>(voff, sKx, oK), \
              dma16o<0>(mV, voff, sV, oV), dma16n<1024>(voff, sV, oV), dma16n<2048>(voff, sV, oV), dma16n<3072>(voff, sV, oV)); \
    oK += 32768u; oV += 32768u; TS2X(h); \
    l_post(s); \
    if ((s) == nX - 1) { _Pragma("unroll") for (int ks = 0; ks < 8; ++ks) q[ks] = ldsfrag(L + 131072 + wq * 8192 + ks * 1024 + lane * 16); negm = (f32x16){}; } \
    A_TAIL(); } while (0)
  a_mphase<true, false>(kbase, vbase, q, p, S, O, negm, l, pm0 STP_ARG);
  A_FAST(true);
  if (w == 0 && lane == 0) __hip_atomic_store((u64*)flg + 8 + r, tag, __ATOMIC_RELAXED, __HIP_MEMORY_SCOPE_AGENT);
  A_FAST(false);
  {
    const unsigned mq = rfl(lds0 + 131072u + (unsigned)w * 4096u), oq = rfl((unsigned)(b * 32768 + w * 4096));
    dma16o<0>(mq, voff, sW, oq); dma16n<1024>(voff, sW, oq); dma16n<2048>(voff, sW, oq); dma16n<3072>(voff, sW, oq);
    const unsigned go = rfl((unsigned)(WS_GW - WS_QF) + (unsigned)(b * 1024 + wq * 128));
    asm volatile("buffer_load_dword %0, %2, %3, %4 offen\n\tbuffer_load_dword %1, %2, %3, %4 offen offset:512" : "+v"(gate[0]), "+v"(gate[1]) : "v"((unsigned)r32 * 4u), "s"(sW), "s"(go) : "memory");
    poll_issue(fl2, flg + 8, lane);
  }
  A_FAST(false);
  asm volatile("" : "+v"(gate[0]), "+v"(gate[1]), "+v"(fl2));
  if (w == 0) { poll_check(fl2, flg + 8, tag, lane); if (r == 0 && lane == 0) __hip_atomic_store((u64*)flg + 16, tag, __ATOMIC_RELAXED, __HIP_MEMORY_SCOPE_AGENT); }
  {
    int s = 3;
#pragma unroll 1
    for (int blk = 0; blk < 2; ++blk) {
      const int e1 = blk ? 15 : nX - 2, e2 = blk ? 16 : (nX + 2 < 16 ? nX + 2 : 16);
#pragma unroll 1
      for (; s < e1; ++s) A_FAST(false);
#pragma unroll 1
      for (; s < e2; ++s) A_SLOW(s);
    }
  }
  TS(t4);
  {
    L_SUM(); l_pre(16);
    const unsigned mp = rfl(lds0 + (g ? 98304u : 32768u) + (unsigned)wq * 2048u), op = rfl((unsigned)(WS_PQ - WS_QF) + (unsigned)(b * 16384 + w * 2048));
    L_SOFTMAX(true, nX == 16, dma16o<0>(mp, voff, sW, op), dma16n<1024>(voff, sW, op), (void)0, (void)0, (void)0, (void)0, (void)0, (void)0);
    if (16 == nX || 16 == nX + 1) wait_vm<0>();
    l_post(16);
    asm volatile("" : "+v"(p[0]), "+v"(p[1]), "+v"(l), "+v"(S[1]));
    SBAR(); wait_bar<2>(); SBAR();
  }
  a_mphase<false, true>(kbase, vbase, q, p, S, O, negm, l, pm0 STP_ARG);
  NOP16(); SBAR();
  L_SUM();
  if (g == 0 && nX == 16) {
    ST_LOAD(w + 4); wait_vm<0>(); asm volatile("" : "+v"(ps));
    a_finish(keep0, keep1, mk, lk, xr, ps[0], ps[1], gate[0], sW, voff, CF_SO(X, 0));
    LWAIT();
  }
  LAS f32x2* stl = (LAS f32x2*)(L + 81920) + r32;
  auto stage2 = [&]() {
    f32x16 kv = {};
#pragma unroll
    for (int k = 0; k < 8; ++k)
#pragma unroll
      for (int qd = 0; qd < 4; ++qd) { const f32x4 v = *(LAS const f32x4*)(L + (k < 4 ? 49152 : 114688) + (k & 3) * 4096 + qd * 1024 + lane * 16);
#pragma unroll
        for (int e = 0; e < 4; ++e) kv[4 * qd + e] += v[e]; }
    const bf16x8 p0 = ldsfrag(kbase + wq * 2048), p1 = ldsfrag(kbase + wq * 2048 + 1024);
    f32x16 o = MFMA(asbf(pack8<0>(kv)), p0, (f32x16){});
    o = MFMA(asbf(pack8<1>(kv)), p1, o);
    const float sc = (1.0f - (g ? gate[1] : gate[0])) / (swap32_lo(o[8]) + 1e-6f);
    o = o * sc;
    st128(pack8<0>(o), sW, voff, (unsigned)(WS_CF - WS_QF) + (unsigned)(((4 * (g ? Y : X) + wq) * 288 + 256 + h) * 1024));
  };
  const float lt = swap32_add(l);
  if (g == 0) {
    a_send(O[2], O[3], xr);
    stl[w * 32] = (f32x2){m, lt};
    wait_bar<0>();
    stage2();
    LBAR();
    const f32x2 po = stl[(w + 4) * 32];
    a_finish(O[0], O[1], m, lt, xr, po[0], po[1], gate[1], sW, voff, CF_SO(Y, 0));
  } else {
    const f32x2 po = stl[(w - 4) * 32];
    a_finish(O[2], O[3], m, lt, xr, po[0], po[1], gate[1], sW, voff, CF_SO(Y, 1));
    LWAIT();
    a_send(O[0], O[1], xr);
    stl[w * 32] = (f32x2){m, lt};
    wait_bar<4>();
    stage2();
  }
#undef TILE
#undef CF_SO
#undef ST_SO
#undef ST_LOAD
#undef L_SOFTMAX
#undef SM_GRP
#undef L_SUM
#undef A_TAIL
#undef A_FAST
#undef A_SLOW
}
constexpr int O_NST = 4;
__global__ __launch_bounds__(512, 2) void kO(const u16* __restrict__ wo_a, const u16* __restrict__ wo_b, char* __restrict__ ws, u16* __restrict__ out) {
  extern __shared__ __attribute__((aligned(16))) char lds[];
  LAS char* L = (LAS char*)lds; const unsigned lds0 = (unsigned)(uintptr_t)L;
  const int tid = threadIdx.x, lane = tid & 63, w = __builtin_amdgcn_readfirstlane(tid >> 6);
  const int b = blockIdx.x, xcd = b & 7, j = xcd & 3, idx = b >> 3, mi = (xcd >> 2) * 8 + (idx >> 2), ni = idx & 3;
  const int g = w >> 2, wm = (w >> 1) & 1, wn = w & 1, hh = lane >> 5, r32 = lane & 31, i16 = lane & 15;
  const srd_t sC = mksrd(ws + WS_CF, 64u * 288u * 1024u), sWa = mksrd(wo_a, 4096u * 1024u), sWb = mksrd(wo_b, 512u * 1024u);
  const unsigned voffA = (unsigned)lane * 16u;
  const unsigned voffB = (unsigned)(((lane >> 2) & 7) * 1024 + (lane >> 5) * 64 + (lane & 3) * 16);
  auto issue = [&](int t) {
    const unsigned st = lds0 + (unsigned)(t % O_NST) * 32768u;
    const int kt = 18 * j + t;
    const unsigned so = (unsigned)((4 * mi + (w >> 1)) * 288 + 4 * kt + 2 * (w & 1)) * 1024u;
    dma16o<0>(st + (unsigned)w * 2048u, voffA, sC, so);
    dma16o<0>(st + (unsigned)w * 2048u + 1024u, voffA, sC, so + 1024u);
    if (kt < 64) {
      const unsigned kb = (unsigned)(64 * kt + 8 * w) * 1024u + (unsigned)ni * 256u;
      dma16o<0>(st + 16384u + (unsigned)w * 2048u, voffB, sWa, kb); dma16o<0>(st + 16384u + (unsigned)w * 2048u + 1024u, voffB, sWa, kb + 128u);
    } else {
      const unsigned kb = (unsigned)(64 * (kt - 64) + 8 * w) * 1024u + (unsigned)ni * 256u;
      dma16o<0>(st + 16384u + (unsigned)w * 2048u, voffB, sWb, kb); dma16o<0>(st + 16384u + (unsigned)w * 2048u + 1024u, voffB, sWb, kb + 128u);
    }
  };
  TS(z0);
  issue(0); issue(1); issue(2);
  f32x16 acc[2][2];
#pragma unroll
  for (int a = 0; a < 2; ++a)
#pragma unroll
    for (int c = 0; c < 2; ++c) acc[a][c] = (f32x16){};
  const unsigned aB = (unsigned)((2 * wm * 4 + 2 * g) * 1024 + lane * 16);
  const int p4 = i16 & 3;
  const unsigned bB = 16384u + (unsigned)((4 * g * 4 + 2 * wn) * 512 + (4 * hh + (i16 >> 2)) * 64 + ((lane >> 4) & 1) * 32 + (((p4 & 1) << 1) | (p4 >> 1)) * 8);
#pragma unroll 1
  for (int t = 0; t < 18; ++t) {
    if (t < 16) wait_bar<8>(); else if (t == 16) wait_bar<4>(); else wait_bar<0>();
    SBAR();
    if (t + 3 < 18) issue(t + 3);
    LAS const char* sp = L + (t % O_NST) * 32768;
    bf16x8 fa[2][2], fb[2][2];
#pragma unroll
    for (int ks = 0; ks < 2; ++ks) {
#pragma unroll
      for (int nt = 0; nt < 2; ++nt) fb[ks][nt] = trfrag(sp + bB + ks * 4096 + nt * 512, sp + bB + ks * 4096 + nt * 512 + 2048);
#pragma unroll
      for (int mt = 0; mt < 2; ++mt) fa[ks][mt] = ldsfrag(sp + aB + mt * 4096 + ks * 1024);
    }
#pragma unroll
    for (int ks = 0; ks < 2; ++ks)
#pragma unroll
      for (int mt = 0; mt < 2; ++mt)
#pragma unroll
        for (int nt = 0; nt < 2; ++nt) acc[mt][nt] = MFMA(fb[ks][nt], fa[ks][mt], acc[mt][nt]);
    SBAR();
  }
  TS(z1);
  LBAR();
  LAS char* rp = L + (w & 3) * 16384 + lane * 16;
  if (g == 1) {
#pragma unroll
    for (int mt = 0; mt < 2; ++mt)
#pragma unroll
      for (int nt = 0; nt < 2; ++nt)
#pragma unroll
        for (int qd = 0; qd < 4; ++qd) *(LAS f32x4*)(rp + ((mt * 2 + nt) * 4 + qd) * 1024) = (f32x4){acc[mt][nt][4 * qd], acc[mt][nt][4 * qd + 1], acc[mt][nt][4 * qd + 2], acc[mt][nt][4 * qd + 3]};
  }
  LBAR();
  if (g == 1) return;
#pragma unroll
  for (int mt = 0; mt < 2; ++mt)
#pragma unroll
    for (int nt = 0; nt < 2; ++nt)
#pragma unroll
      for (int qd = 0; qd < 4; ++qd) { const f32x4 v = *(LAS const f32x4*)(rp + ((mt * 2 + nt) * 4 + qd) * 1024);
#pragma unroll
        for (int e = 0; e < 4; ++e) acc[mt][nt][4 * qd + e] += v[e]; }
  const int tg = mi * 4 + ni;
  const srd_t sS = mksrd(ws + WS_SLAB + (size_t)tg * 4 * 65536, 4u * 65536u);
  const unsigned so = (unsigned)(w * 16384 + lane * 16);
#pragma unroll
  for (int a = 0; a < 4; ++a) {
    if (a != j) {
#pragma unroll
      for (int qd = 0; qd < 4; ++qd) {
        const f32x16& v = acc[a >> 1][a & 1];
        __builtin_amdgcn_raw_buffer_store_b128(__builtin_bit_cast(u32x4, (f32x4){v[4 * qd], v[4 * qd + 1], v[4 * qd + 2], v[4 * qd + 3]}), sS, so + (unsigned)((a * 4 + qd) * 1024) + (unsigned)j * 65536u, 0, 16);
      }
    }
  }
  TS(z2);
  asm volatile("s_waitcnt vmcnt(0)" ::: "memory");
  TS(z3);
  unsigned* cnt = (unsigned*)(ws + WS_CNT + (size_t)tg * 256);
  if (lane == 0) __hip_atomic_fetch_add(cnt, 1u, __ATOMIC_RELAXED, __HIP_MEMORY_SCOPE_AGENT);
  while (__builtin_amdgcn_readfirstlane(__hip_atomic_load(cnt, __ATOMIC_RELAXED, __HIP_MEMORY_SCOPE_AGENT)) < 16u) __builtin_amdgcn_s_sleep(1);
  TS(z4);
  __builtin_amdgcn_fence(__ATOMIC_ACQUIRE, "agent");
  f32x16 fin = j == 0 ? acc[0][0] : (j == 1 ? acc[0][1] : (j == 2 ? acc[1][0] : acc[1][1]));
#pragma unroll
  for (int jj = 0; jj < 4; ++jj) {
    if (jj != j) {
#pragma unroll
      for (int qd = 0; qd < 4; ++qd) {
        const f32x4 v = __builtin_bit_cast(f32x4, __builtin_amdgcn_raw_buffer_load_b128(sS, so + (unsigned)((j * 4 + qd) * 1024), (unsigned)jj * 65536u, 16));
#pragma unroll
        for (int e = 0; e < 4; ++e) fin[4 * qd + e] += v[e];
      }
    }
  }
  u16* op = out + (size_t)(128 * mi + 64 * wm + 32 * (j >> 1) + r32) * 512 + 128 * ni + 64 * wn + 32 * (j & 1) + 8 * hh;
  *(u32x4*)(op) = pack8<0>(fin);
  *(u32x4*)(op + 16) = pack8<1>(fin);
}

extern "C" void kernel_launch(void* const* d_in, const int* in_sizes, int n_in, void* d_out, int out_size, void* d_ws, size_t ws_size, hipStream_t stream) {
  const u16* x = (const u16*)d_in[0]; const u16* ckv = (const u16*)d_in[1]; const u16* wq = (const u16*)d_in[2];
  const u16* wuk = (const u16*)d_in[3]; const u16* wuv = (const u16*)d_in[4]; const u16* wo_a = (const u16*)d_in[5];
  const u16* wqkv = (const u16*)d_in[7]; const u16* wphi = (const u16*)d_in[8]; const u16* wo_b = (const u16*)d_in[9]; const u16* gatew = (const u16*)d_in[10];
  (void)hipFuncSetAttribute((const void*)kF, hipFuncAttributeMaxDynamicSharedMemorySize, 163840);
  (void)hipFuncSetAttribute((const void*)kO, hipFuncAttributeMaxDynamicSharedMemorySize, 131072);
  kF<<<256, 512, 163840, stream>>>(x, ckv, wq, wuk, wuv, wqkv, wphi, gatew, (char*)d_ws);
  kO<<<256, 512, 131072, stream>>>(wo_a, wo_b, (char*)d_ws, (u16*)d_out);
}
```

```cpp
#define PRIO_M
#include <hip/hip_runtime.h>
#include <stdint.h>

typedef unsigned short u16;
typedef unsigned long long u64;
typedef __bf16 bf16x8 __attribute__((ext_vector_type(8)));
typedef __bf16 bf16x2 __attribute__((ext_vector_type(2)));
typedef float f32x2 __attribute__((ext_vector_type(2)));
typedef float f32x4 __attribute__((ext_vector_type(4)));
typedef float f32x16 __attribute__((ext_vector_type(16)));
typedef unsigned u32x2 __attribute__((ext_vector_type(2)));
typedef unsigned u32x4 __attribute__((ext_vector_type(4)));
typedef short s16x4 __attribute__((ext_vector_type(4)));
typedef short s16x8 __attribute__((ext_vector_type(8)));
typedef __amdgpu_buffer_rsrc_t srd_t;
#define LAS __attribute__((address_space(3)))
#define DI __device__ __forceinline__
#define SBAR() __builtin_amdgcn_sched_barrier(0)
#define MFMA(a, b, c) __builtin_amdgcn_mfma_f32_32x32x16_bf16((a), (b), (c), 0, 0, 0)

constexpr size_t MB = 1024 * 1024;
constexpr size_t WS_KF = 0;
constexpr size_t WS_VF = 16 * MB;
constexpr size_t WS_QF = 32 * MB;
constexpr size_t WS_CF = 48 * MB;
constexpr size_t WS_PQ = 68 * MB;
constexpr size_t WS_KVP = 72 * MB;
constexpr size_t WS_GW = 73 * MB;
constexpr size_t WS_ST = 74 * MB;
constexpr size_t WS_SLAB = 76 * MB;
constexpr size_t WS_CNT = 92 * MB;
constexpr size_t WS_FLG = 93 * MB;
constexpr size_t WS_DBG = 100 * MB;

constexpr float QSCALE = 0.08838834764831845f * 1.4426950408889634f;
constexpr float THR = 8.0f;

DI unsigned cvtpk(float lo, float hi) { f32x2 v = {lo, hi}; bf16x2 b = __builtin_convertvector(v, bf16x2); return __builtin_bit_cast(unsigned, b); }
template <int S> DI u32x4 pack8(const f32x16& x) {
  return (u32x4){cvtpk(x[8 * S + 0], x[8 * S + 1]), cvtpk(x[8 * S + 2], x[8 * S + 3]), cvtpk(x[8 * S + 4], x[8 * S + 5]), cvtpk(x[8 * S + 6], x[8 * S + 7])};
}
DI bf16x8 asbf(u32x4 v) { return __builtin_bit_cast(bf16x8, v); }
DI float swap32_max(float v) { auto rr = __builtin_amdgcn_permlane32_swap(__float_as_uint(v), __float_as_uint(v), false, false); return fmaxf(__uint_as_float(rr[0]), __uint_as_float(rr[1])); }
DI float swap32_add(float v) { auto rr = __builtin_amdgcn_permlane32_swap(__float_as_uint(v), __float_as_uint(v), false, false); return __uint_as_float(rr[0]) + __uint_as_float(rr[1]); }
DI float swap32_lo(float v) { auto rr = __builtin_amdgcn_permlane32_swap(__float_as_uint(v), __float_as_uint(v), false, false); return __uint_as_float(rr[0]); }
DI float bf16round(float x) { return __uint_as_float(cvtpk(x, 0.f) << 16); }
DI srd_t mksrd(const void* p, unsigned bytes) { return __builtin_amdgcn_make_buffer_rsrc((void*)p, (short)0, (int)bytes, 0x00020000); }
DI unsigned rfl(unsigned x) { return (unsigned)__builtin_amdgcn_readfirstlane((int)x); }
template <int OFF> DI void dma16o(unsigned m0v, unsigned voff, srd_t srd, unsigned soff) {
  asm volatile("s_mov_b32 m0, %0\n\ts_nop 0\n\tbuffer_load_dwordx4 %1, %2, %3 offen offset:%4 sc1 lds" :: "s"(m0v), "v"(voff), "s"(srd), "s"(soff), "i"(OFF) : "memory");
}
template <unsigned MOFF, unsigned SOFF> DI void dma16s(unsigned mb, unsigned voff, srd_t srd, unsigned sb) {
  unsigned t;
  asm volatile("s_add_u32 m0, %1, %5\n\ts_add_u32 %0, %4, %6\n\tbuffer_load_dwordx4 %2, %3, %0 offen sc1 lds" : "=&s"(t) : "s"(mb), "v"(voff), "s"(srd), "s"(sb), "i"(MOFF), "i"(SOFF) : "memory", "scc");
}
template <int OFF> DI void dma16n(unsigned voff, srd_t srd, unsigned soff) {
  asm volatile("buffer_load_dwordx4 %0, %1, %2 offen offset:%3 sc1 lds" :: "v"(voff), "s"(srd), "s"(soff), "i"(OFF) : "memory");
}
template <unsigned MOFF, unsigned SOFF> DI void dma16c(unsigned mb, unsigned voff, srd_t srd, unsigned sb) {
  unsigned t;
  asm volatile("s_add_u32 m0, %1, %5\n\ts_add_u32 %0, %4, %6\n\tbuffer_load_dwordx4 %2, %3, %0 offen lds" : "=&s"(t) : "s"(mb), "v"(voff), "s"(srd), "s"(sb), "i"(MOFF), "i"(SOFF) : "memory", "scc");
}
template <unsigned MOFF, unsigned SOFF> DI void dma16m(unsigned mb, unsigned voff, srd_t srd, unsigned sb) {
  unsigned t;
  asm volatile("s_add_u32 m0, %1, %5\n\ts_add_u32 %0, %4, %6\n\ts_mov_b32 exec_hi, 0x33333333\n\tbuffer_load_dwordx4 %2, %3, %0 offen lds\n\ts_mov_b32 exec_hi, -1"
               : "=&s"(t) : "s"(mb), "v"(voff), "s"(srd), "s"(sb), "i"(MOFF), "i"(SOFF) : "memory", "scc");
}
template <int N> DI void wait_bar() { asm volatile("s_waitcnt vmcnt(%0) lgkmcnt(0)\n\ts_barrier" :: "i"(N) : "memory"); }
template <int N> DI void wait_vm() { asm volatile("s_waitcnt vmcnt(%0)" :: "i"(N) : "memory"); }
#define LBAR() asm volatile("s_waitcnt lgkmcnt(0)\n\ts_barrier" ::: "memory")
#define LWAIT() asm volatile("s_waitcnt lgkmcnt(0)" ::: "memory")
#define NOP16() asm volatile("s_nop 7\n\ts_nop 7" ::: "memory")
DI bf16x8 ldsfrag(LAS const char* p) { return *(LAS const bf16x8*)p; }
DI bf16x8 trfrag(LAS const char* p0, LAS const char* p1) {
  s16x4 a = __builtin_amdgcn_ds_read_tr16_b64_v4i16((LAS s16x4*)p0);
  s16x4 b = __builtin_amdgcn_ds_read_tr16_b64_v4i16((LAS s16x4*)p1);
  return __builtin_bit_cast(bf16x8, __builtin_shufflevector(a, b, 0, 1, 2, 3, 4, 5, 6, 7));
}
DI void st128(u32x4 v, srd_t s, unsigned vo, unsigned so) { __builtin_amdgcn_raw_buffer_store_b128(v, s, vo + so, 0, 0); }
DI void st128_wt(u32x4 v, srd_t s, unsigned vo, unsigned so) { __builtin_amdgcn_raw_buffer_store_b128(v, s, vo + so, 0, 16); }
#define TS(x)

struct PCtx {
  srd_t sA1, sA2, sUK, sUV, sWQ, sWB, sWP, sK, sV;
  unsigned voffA[2], voffB, voffS, voff;
  unsigned soA1, soA2, soB, soS, soKa, so2, mA, mB2, m2;
  unsigned gv[8];
  u32x2 fl; u64 tag; const u64* flg;
  u32x4 wp;
  u32x4 kvp[8]; unsigned kvo;
  int w, lane, h;
  LAS char* L;
};
#define TS3(x) do { SBAR(); asm volatile("s_memtime %0\n\ts_waitcnt lgkmcnt(0)" : "=s"(x) :: "memory"); SBAR(); } while (0)
template <int T, int I> DI void p_pieceA(const PCtx& c) {
  if constexpr (T < 24) {
    constexpr unsigned st = (unsigned)(T % 3) * 32768u, kt = (unsigned)(T & 7);
    if constexpr (T < 16) dma16c<st + I * 1024u, I * 8192u + kt * 128u>(c.mA, c.voffA[I & 1], c.sA1, c.soA1);
    else                  dma16c<st + I * 1024u, I * 8192u + kt * 128u>(c.mA, c.voffA[I & 1], c.sA2, c.soA2);
  }
}
template <int T, int I> DI void p_pieceB(const PCtx& c) {
  constexpr unsigned st = 98304u + (unsigned)(T & 1) * 32768u, kt = (unsigned)(T & 7);
  if constexpr (T < 16) {
    if constexpr (I < 2) dma16c<st + I * 1024u, kt * 524288u + I * 128u>(c.m2, c.voffB, T < 8 ? c.sUK : c.sUV, c.soB);
  } else if constexpr (T < 24) {
    if constexpr (I < 2) dma16c<st + I * 1024u, kt * 524288u + I * 128u>(c.mB2, c.voffB, c.sWQ, c.soB);
    else if constexpr (I == 2) dma16m<st + 2048u, kt * 196608u>(c.mB2, c.voffS, c.sWB, c.soS);
    else if (c.w == 7) *(LAS u16*)(c.L + st + ((c.lane >> 3) * 6 + 5) * 512 + (c.lane & 7) * 64 + 32) = (u16)c.gv[kt];
  }
}
template <int I, bool ISV> DI void p_kvstore(const PCtx& c) {
  if constexpr (I >= 0 && I < 8) {
    constexpr int mt = I >> 2, nt = (I >> 1) & 1, sh = I & 1;
    constexpr unsigned off = (unsigned)((ISV ? nt * 4 + 2 * mt + sh : mt * 8 + 2 * nt + sh) * 1024);
    __builtin_amdgcn_raw_buffer_store_b128(c.kvp[I], ISV ? c.sV : c.sK, c.kvo + off, 0, 16);
  }
}
DI void poll_spin(const u64* f, u64 tag, int lane) {
  while (!__all(__hip_atomic_load(f + (lane & 7), __ATOMIC_RELAXED, __HIP_MEMORY_SCOPE_AGENT) == tag)) __builtin_amdgcn_s_sleep(2);
}
DI void poll_issue(u32x2& fl, const u64* f, int lane) { asm volatile("global_load_dwordx2 %0, %1, off sc1" : "+v"(fl) : "v"(f + (lane & 7)) : "memory"); }
DI void poll_check(u32x2& fl, const u64* f, u64 tag, int lane) {
  asm volatile("" : "+v"(fl));
  if (!__all(fl[0] == (unsigned)tag && fl[1] == (unsigned)(tag >> 32))) poll_spin(f, tag, lane);
  asm volatile("" ::: "memory");
}
template <int B> DI void p_hook(PCtx& c) {
  if constexpr (B == 17) { if (c.w == 0) dma16c<155648u, 0u>(c.mA, c.voff, c.sWP, (unsigned)c.h * 512u); }
  if constexpr (B == 21) {
    if (c.w == 0) { if (c.lane == 0) __hip_atomic_store((u64*)c.flg + (blockIdx.x >> 5), c.tag, __ATOMIC_RELAXED, __HIP_MEMORY_SCOPE_AGENT); }
    if (c.w >= 4) {
      const int r32 = c.lane & 31, hh = c.lane >> 5; LAS const u16* wb = (LAS const u16*)(c.L + 155648) + (r32 & 15); const unsigned sg = r32 >= 16 ? 0x80008000u : 0u;
#pragma unroll
      for (int jj = 0; jj < 4; ++jj) { const int d0 = 8 * (jj >> 1) + 4 * hh + 2 * (jj & 1); c.wp[jj] = ((unsigned)wb[d0 * 16] | ((unsigned)wb[(d0 + 1) * 16] << 16)) ^ sg; }
    }
  }
  if constexpr (B == 22) { if (c.w == 0) poll_issue(c.fl, c.flg, c.lane); }
  if constexpr (B == 23) { if (c.w == 0) poll_check(c.fl, c.flg, c.tag, c.lane); }
  if constexpr (B == 24) {
    dma16s<32768u, 0u>(c.mA, c.voff, c.sK, c.soKa); dma16s<32768u + 1024u, 1024u>(c.mA, c.voff, c.sK, c.soKa);
    dma16s<32768u + 2048u, 2048u>(c.mA, c.voff, c.sK, c.soKa); dma16s<32768u + 3072u, 3072u>(c.mA, c.voff, c.sK, c.soKa);
    dma16s<98304u, 16384u>(c.mA, c.voff, c.sK, c.soKa); dma16s<98304u + 1024u, 16384u + 1024u>(c.mA, c.voff, c.sK, c.soKa);
    dma16s<98304u + 2048u, 16384u + 2048u>(c.mA, c.voff, c.sK, c.soKa); dma16s<98304u + 3072u, 16384u + 3072u>(c.mA, c.voff, c.sK, c.soKa);
    dma16s<65536u, 0u>(c.m2, c.voff, c.sV, c.so2); dma16s<65536u + 1024u, 1024u>(c.m2, c.voff, c.sV, c.so2);
    dma16s<0u, 16384u>(c.m2, c.voff, c.sV, c.so2); dma16s<1024u, 16384u + 1024u>(c.m2, c.voff, c.sV, c.so2);
  }
}

template <int PASS, int MT, int NT, int NSUB, bool TRANS>
struct PPass {
  PCtx& c; f32x16 (&acc)[MT][NT];
  unsigned aB[2][4], bB;
  bf16x8 fa[2][MT], fb[2][NT];
  template <int SET, int T, int KS> DI void load() {
    constexpr int bo = (T & 1) * 32768 + KS * (2 * NSUB * 512), sl = T % 3;
#pragma unroll
    for (int nt = 0; nt < NT; ++nt) fb[SET][nt] = trfrag(c.L + bB + bo + nt * 512, c.L + bB + bo + nt * 512 + 256);
#pragma unroll
    for (int mt = 0; mt < MT; ++mt) fa[SET][mt] = ldsfrag(c.L + aB[sl >> 1][KS] + (sl & 1) * 32768 + mt * 4096);
  }
  template <int SET, int I> DI void mma1() {
    if constexpr (I < MT * NT) {
      constexpr int mt = I / NT, nt = I % NT;
      acc[mt][nt] = TRANS ? MFMA(fb[SET][nt], fa[SET][mt], acc[mt][nt]) : MFMA(fa[SET][mt], fb[SET][nt], acc[mt][nt]);
    }
  }
  template <int SET, int T, int I0, int S0> DI void mmaA() {
    mma1<SET, 0>(); SBAR(); p_pieceA<T, I0>(c); SBAR(); mma1<SET, 1>(); SBAR(); p_kvstore<S0, PASS == 2>(c); SBAR(); mma1<SET, 2>(); SBAR(); p_pieceA<T, I0 + 1>(c); SBAR();
    mma1<SET, 3>(); mma1<SET, 4>(); mma1<SET, 5>();
  }
  template <int t> DI void tile() {
    constexpr int T = PASS * 8 + t;
    constexpr int SA = PASS == 1 ? t : (PASS == 2 && t < 4 ? 2 * t : -1), SB = PASS == 2 && t < 4 ? 2 * t + 1 : -1;
    constexpr int NST = (SA >= 0 ? 1 : 0) + (SB >= 0 ? 1 : 0);
    SBAR(); load<1, T, 1>(); SBAR(); mmaA<0, T + 2, 0, SA>();
    SBAR(); load<0, T, 2>(); SBAR(); mmaA<1, T + 2, 2, -1>();
    SBAR(); load<1, T, 3>(); SBAR(); mmaA<0, 24, 0, SB>();
    SBAR();
    if constexpr (T == 23) LBAR(); else if constexpr (T == 22) wait_bar<0>(); else wait_bar<4 + NST>();
    SBAR();
    if constexpr (t < 7) load<0, T + 1, 0>();
    SBAR();
    mma1<1, 0>(); p_pieceB<T + 2, 0>(c); SBAR();
    mma1<1, 1>(); p_pieceB<T + 2, 1>(c); SBAR();
    mma1<1, 2>(); p_pieceB<T + 2, 2>(c); SBAR();
    mma1<1, 3>(); p_pieceB<T + 2, 3>(c); SBAR();
    mma1<1, 4>(); mma1<1, 5>(); SBAR();
    p_hook<T + 1>(c); SBAR();
  }
  DI void run(unsigned arow0, unsigned nt0) {
    const int lane = c.lane, r = lane & 31, hh = lane >> 5, i16 = lane & 15;
#pragma unroll
    for (int ks = 0; ks < 4; ++ks) { aB[0][ks] = (arow0 + r) * 128u + (unsigned)(((2 * ks + hh) ^ ((r >> 1) & 7)) * 16); aB[1][ks] = aB[0][ks] + 65536u; asm volatile("" : "+v"(aB[1][ks])); }
    bB = 98304u + (unsigned)(hh * NSUB + nt0) * 512u + (unsigned)((i16 >> 2) * 64 + ((lane >> 4) & 1) * 32 + (i16 & 3) * 8); asm volatile("" : "+v"(bB));
#pragma unroll
    for (int mt = 0; mt < MT; ++mt)
#pragma unroll
      for (int nt = 0; nt < NT; ++nt) acc[mt][nt] = (f32x16){};
    load<0, PASS * 8, 0>();
    tile<0>(); tile<1>(); tile<2>(); tile<3>(); tile<4>(); tile<5>(); tile<6>(); tile<7>();
  }
};
template <int PASS, int MT, int NT, int NSUB, bool TRANS>
DI void p_pass(PCtx& c, f32x16 (&acc)[MT][NT], unsigned arow0, unsigned nt0) { PPass<PASS, MT, NT, NSUB, TRANS> p{c, acc}; p.run(arow0, nt0); }

DI void softmax2(f32x16& x) {
#pragma unroll
  for (int s = 0; s < 2; ++s) {
    float v[8];
#pragma unroll
    for (int j = 0; j < 8; ++j) v[j] = bf16round(x[8 * s + j]);
    float mx = v[0];
#pragma unroll
    for (int j = 1; j < 8; ++j) mx = fmaxf(mx, v[j]);
    mx = swap32_max(mx);
    float sum = 0.f;
#pragma unroll
    for (int j = 0; j < 8; ++j) { v[j] = __builtin_amdgcn_exp2f((v[j] - mx) * 1.4426950408889634f); sum += v[j]; }
    sum = swap32_add(sum);
    const float inv = 1.0f / sum;
#pragma unroll
    for (int j = 0; j < 8; ++j) x[8 * s + j] = v[j] * inv;
  }
}

DI void mfma_acc(f32x16& c, bf16x8 a, bf16x8 b) { asm volatile("v_mfma_f32_32x32x16_bf16 %0, %1, %2, %0" : "+v"(c) : "v"(a), "v"(b)); }
DI void mfma_acc2(f32x16& c, bf16x8 a, bf16x8 b, bf16x8 nx) { asm volatile("v_mfma_f32_32x32x16_bf16 %0, %1, %2, %0" : "+v"(c) : "v"(a), "v"(b), "v"(nx)); }
DI void mfma_init2(f32x16& c, bf16x8 a, bf16x8 b, const f32x16& ci, bf16x8 nx) { asm volatile("v_mfma_f32_32x32x16_bf16 %0, %1, %2, %3" : "=&v"(c) : "v"(a), "v"(b), "v"(ci), "v"(nx)); }
DI float sum2(unsigned pk, float acc) { return __builtin_amdgcn_fdot2_f32_bf16(__builtin_bit_cast(bf16x2, pk), __builtin_bit_cast(bf16x2, 0x3f803f80u), acc, false); }
DI float sum8(bf16x8 p, float acc) { const u32x4 u = __builtin_bit_cast(u32x4, p); return sum2(u[3], sum2(u[2], sum2(u[1], sum2(u[0], acc)))); }
DI float max3f(float a, float b, float c) { float r; asm("v_max3_f32 %0, %1, %2, %3" : "=v"(r) : "v"(a), "v"(b), "v"(c)); return r; }
DI float max2f(float a, float b) { float r; asm("v_max_f32_e32 %0, %1, %2" : "=v"(r) : "v"(a), "v"(b)); return r; }
#define TS2X(a)
#define STP_PARAM
#define STP_ARG
#define TS2(acc)
template <bool DOQK, bool DOPV>
DI void a_mphase(LAS const char* kp, LAS const char* vp, const bf16x8 (&q)[8], bf16x8 (&p)[4], f32x16 (&S)[2], f32x16 (&O)[4], const f32x16& negm, float& l, float& pm0 STP_PARAM) {
  constexpr int N = (DOQK ? 16 : 0) + (DOPV ? 16 : 0), D = 6;
  bf16x8 f[N];
  unsigned pw[8]; float ls = 0.f, e0 = 0.f;
#define A_ISQK(i) (DOQK && (!DOPV || (((i) >> 3) & 1) == 0))
#define A_IX(i) ((DOPV && DOQK) ? (((i) >> 4) * 8 + ((i) & 7)) : (i))
#define A_LD(i) do { if constexpr ((i) < N) { if constexpr (A_ISQK(i)) f[i] = ldsfrag(kp + A_IX(i) * 1024); \
                     else f[i] = ldsfrag(vp + ((A_IX(i) & 3) * 4 + (A_IX(i) >> 2)) * 1024); } } while (0)
#define A_MM(i) do { if constexpr (A_ISQK(i)) { if constexpr ((A_IX(i) & 7) == 0) mfma_init2(S[A_IX(i) >> 3], f[i], q[0], negm, f[(i) + 1]); \
                       else if constexpr ((i) & 1) mfma_acc(S[A_IX(i) >> 3], f[i], q[A_IX(i) & 7]); else mfma_acc2(S[A_IX(i) >> 3], f[i], q[A_IX(i) & 7], f[((i) + 1) % N]); } \
                     else if constexpr ((i) & 1) mfma_acc(O[A_IX(i) & 3], f[i], p[A_IX(i) >> 2]); else mfma_acc2(O[A_IX(i) & 3], f[i], p[A_IX(i) >> 2], f[((i) + 1) % N]); } while (0)
#define A_SM(e) do { const float x_ = __builtin_amdgcn_exp2f(S[1][(e) & 15]); ls += x_; \
                     if constexpr ((e) & 1) { pw[((e) & 15) >> 1] = cvtpk(e0, x_); asm volatile("" : "+v"(pw[((e) & 15) >> 1]), "+v"(ls)); } else { e0 = x_; asm volatile("" : "+v"(e0), "+v"(ls)); } \
                     if constexpr ((e) == 7) p[2] = asbf((u32x4){pw[0], pw[1], pw[2], pw[3]}); if constexpr ((e) == 15) p[3] = asbf((u32x4){pw[4], pw[5], pw[6], pw[7]}); } while (0)
#define A_MX(j) do { if constexpr ((j) == 0) pm0 = max2f(S[0][0], S[0][1]); else pm0 = max3f(pm0, S[0][2 * ((j) & 7)], S[0][2 * ((j) & 7) + 1]); asm volatile("" : "+v"(pm0)); } while (0)
#define A_STEP(i) do { if constexpr ((i) < N) { A_LD((i) + D); if constexpr ((i) == N - 4) { SBAR(); TS2(a); LBAR(); TS2(b); SBAR(); } A_MM(i); \
                     if constexpr (DOQK && DOPV && (i) < 16) A_SM(i); \
                     if constexpr (DOQK && DOPV && (i) >= 16 && (i) < 24) A_MX((i) - 16); \
                     if constexpr (!DOQK && (i) < 8) { A_SM(2 * (i)); A_SM(2 * (i) + 1); } SBAR(); } } while (0)
  SBAR();
  __builtin_amdgcn_s_setprio(1);
  A_LD(0); A_LD(1); A_LD(2); A_LD(3); A_LD(4); A_LD(5); SBAR();
  A_STEP(0); A_STEP(1); A_STEP(2); A_STEP(3); A_STEP(4); A_STEP(5); A_STEP(6); A_STEP(7);
  A_STEP(8); A_STEP(9); A_STEP(10); A_STEP(11); A_STEP(12); A_STEP(13); A_STEP(14); A_STEP(15);
  A_STEP(16); A_STEP(17); A_STEP(18); A_STEP(19); A_STEP(20); A_STEP(21); A_STEP(22); A_STEP(23);
  A_STEP(24); A_STEP(25); A_STEP(26); A_STEP(27); A_STEP(28); A_STEP(29); A_STEP(30); A_STEP(31);
  if constexpr (DOPV) l += ls;
  if constexpr (!DOPV) { NOP16(); SBAR(); A_MX(0); A_MX(1); A_MX(2); A_MX(3); A_MX(4); A_MX(5); A_MX(6); A_MX(7); }
  __builtin_amdgcn_s_setprio(0);
  TS2X(c);
#undef A_ISQK
#undef A_IX
#undef A_LD
#undef A_MM
#undef A_SM
#undef A_MX
#undef A_STEP
}

DI void a_finish(const f32x16& a0, const f32x16& a1, float ma, float la, LAS const char* xr, float mb, float lb, float gate, srd_t sC, unsigned voff, unsigned soff) {
  const float mt = fmaxf(ma, mb);
  const float fa = __builtin_amdgcn_exp2f(ma - mt), fb = __builtin_amdgcn_exp2f(mb - mt);
  const float sc = gate / (la * fa + lb * fb);
  const float wa = fa * sc, wb = fb * sc;
#pragma unroll
  for (int t = 0; t < 2; ++t) {
    const f32x16& a = t ? a1 : a0;
#pragma unroll
    for (int s = 0; s < 2; ++s) {
      const f32x4 b0 = *(LAS const f32x4*)(xr + t * 4096 + s * 2048), b1 = *(LAS const f32x4*)(xr + t * 4096 + s * 2048 + 1024);
      const u32x4 o = {cvtpk(a[8 * s + 0] * wa + b0[0] * wb, a[8 * s + 1] * wa + b0[1] * wb), cvtpk(a[8 * s + 2] * wa + b0[2] * wb, a[8 * s + 3] * wa + b0[3] * wb),
                       cvtpk(a[8 * s + 4] * wa + b1[0] * wb, a[8 * s + 5] * wa + b1[1] * wb), cvtpk(a[8 * s + 6] * wa + b1[2] * wb, a[8 * s + 7] * wa + b1[3] * wb)};
      st128(o, sC, voff + (unsigned)((2 * t + s) * 1024), soff);
      SBAR();
    }
  }
}
DI void a_send(const f32x16& a0, const f32x16& a1, LAS char* xr) {
#pragma unroll
  for (int g = 0; g < 4; ++g) {
    *(LAS f32x4*)(xr + g * 1024) = (f32x4){a0[4 * g], a0[4 * g + 1], a0[4 * g + 2], a0[4 * g + 3]};
    *(LAS f32x4*)(xr + 4096 + g * 1024) = (f32x4){a1[4 * g], a1[4 * g + 1], a1[4 * g + 2], a1[4 * g + 3]};
  }
}

__global__ __launch_bounds__(512, 2) void kF(const u16* __restrict__ x, const u16* __restrict__ ckv, const u16* __restrict__ wq_, const u16* __restrict__ wuk,
                                             const u16* __restrict__ wuv, const u16* __restrict__ wqkv, const u16* __restrict__ wphi, const u16* __restrict__ gatew, char* __restrict__ ws) {
  extern __shared__ __attribute__((aligned(16))) char lds[];
  LAS char* L = (LAS char*)lds; const unsigned lds0 = (unsigned)(uintptr_t)L;
  const int tid = threadIdx.x, lane = tid & 63, w = __builtin_amdgcn_readfirstlane(tid >> 6);
  const int b = blockIdx.x, h = (b & 7) * 4 + ((b >> 3) & 3), r = b >> 5;
  const int g = w >> 2, wq = w & 3, hh = lane >> 5, r32 = lane & 31;
  const int X = 15 - r, Y = r, nX = 16 - r;
  constexpr int NS = 17;
  const unsigned voff = (unsigned)lane * 16u;
  TS(t0);
  bf16x8 q[8];
  u64 tag; const u64* flg = (const u64*)(ws + WS_FLG + (size_t)h * 256);
  const srd_t sK = mksrd(ws + WS_KF + (size_t)h * 32 * 16384, 32u * 16384u), sV = mksrd(ws + WS_VF + (size_t)h * 32 * 16384, 32u * 16384u);
  const srd_t sW = mksrd(ws + WS_QF, (unsigned)(WS_DBG - WS_QF));
  {
    PCtx c;
    c.fl = (u32x2){0u, 0u}; c.wp = (u32x4){0u, 0u, 0u, 0u};
    c.L = L; c.w = w; c.lane = lane; c.h = h; c.voff = voff; c.flg = flg; c.sK = sK; c.sV = sV;
    c.soA1 = rfl((unsigned)(256 * r + 32 * w) * 1024u);
    c.soA2 = rfl((unsigned)(w < 4 ? 128 * X + 32 * w : 128 * Y + 32 * (w - 4)) * 1024u);
    c.soB = rfl((unsigned)(8 * w) * 8192u + (unsigned)h * 256u);
    c.soS = rfl((unsigned)(8 * w) * 3072u + (unsigned)h * 32u);
    c.soKa = rfl((unsigned)(g * 32768 + wq * 4096)); c.so2 = rfl((unsigned)w * 2048u);
    c.mA = rfl(lds0 + (unsigned)w * 4096u); c.mB2 = rfl(lds0 + (unsigned)w * 3072u); c.m2 = rfl(lds0 + (unsigned)w * 2048u);
    c.sA1 = mksrd(ckv, 2048u * 1024u); c.sA2 = mksrd(x, 2048u * 1024u);
    c.sUK = mksrd(wuk, 512u * 8192u); c.sUV = mksrd(wuv, 512u * 8192u); c.sWQ = mksrd(wq_, 512u * 8192u); c.sWB = mksrd(wqkv, 512u * 3072u); c.sWP = mksrd(wphi, 32u * 512u);
#pragma unroll
    for (int k = 0; k < 2; ++k) c.voffA[k] = (unsigned)((lane >> 3) * 1024 + (((lane & 7) ^ ((4 * k + (lane >> 4)) & 7)) * 16));
    c.voffB = (unsigned)(((lane >> 2) & 7) * 8192 + (lane >> 5) * 64 + (lane & 3) * 16);
    { const int ch = lane & 3, sub = lane >> 5;
      c.voffS = (unsigned)(((lane >> 2) & 7) * 3072 + (sub == 0 ? (ch < 2 ? ch * 16 : 1024 + (ch - 2) * 16) : 2048 + (ch & 1) * 16)); }
    u32x2 ep;
    asm volatile("global_load_dwordx2 %0, %1, off sc1" : "=v"(ep) : "v"(flg + 16) : "memory");
#pragma unroll
    for (int k = 0; k < 8; ++k) asm volatile("global_load_ushort %0, %1, off offset:%2" : "=v"(c.gv[k]) : "v"(gatew + lane), "i"(k * 128) : "memory");
    p_pieceA<0, 0>(c); p_pieceA<0, 1>(c); p_pieceA<0, 2>(c); p_pieceA<0, 3>(c);
    p_pieceB<0, 0>(c); p_pieceB<0, 1>(c);
    p_pieceB<1, 0>(c); p_pieceB<1, 1>(c);
    p_pieceA<1, 0>(c); p_pieceA<1, 1>(c); p_pieceA<1, 2>(c); p_pieceA<1, 3>(c);
    wait_bar<6>();
    asm volatile("" : "+v"(ep), "+v"(c.gv[0]), "+v"(c.gv[1]), "+v"(c.gv[2]), "+v"(c.gv[3]), "+v"(c.gv[4]), "+v"(c.gv[5]), "+v"(c.gv[6]), "+v"(c.gv[7]));
    tag = (((u64)rfl(ep[1]) << 32) | rfl(ep[0])) + 1ull; c.tag = tag;
    {
      f32x16 acc[2][2];
      const int wm = w >> 1, wn = w & 1;
      p_pass<0, 2, 2, 4, true>(c, acc, 64u * wm, 2u * wn);
#pragma unroll
      for (int i = 0; i < 4; ++i) { c.kvp[2 * i] = pack8<0>(acc[i >> 1][i & 1]); c.kvp[2 * i + 1] = pack8<1>(acc[i >> 1][i & 1]); }
      c.kvo = voff + (unsigned)((4 * r + wm) * 16384 + 4 * wn * 1024);
      p_pass<1, 2, 2, 4, false>(c, acc, 64u * wm, 2u * wn);
#pragma unroll
      for (int i = 0; i < 4; ++i) { c.kvp[2 * i] = pack8<0>(acc[i >> 1][i & 1]); c.kvp[2 * i + 1] = pack8<1>(acc[i >> 1][i & 1]); }
      c.kvo = voff + (unsigned)((4 * r + wm) * 16384 + 8 * wn * 1024);
    }
    TS(t1);
    f32x16 acc[2][3];
    p_pass<2, 2, 3, 6, true>(c, acc, 64u * wq, 3u * g);
    TS(t2);
    if (wq < 2) {
#pragma unroll
      for (int mt = 0; mt < 2; ++mt)
#pragma unroll
        for (int nt = 0; nt < 3; ++nt)
          if (g == 0 || nt == 0) {
            const f32x16 v = acc[mt][nt] * QSCALE; LAS char* qd = L + 131072 + ((2 * wq + mt) * 8 + 2 * (3 * g + nt)) * 1024 + lane * 16;
            *(LAS u32x4*)qd = pack8<0>(v); *(LAS u32x4*)(qd + 1024) = pack8<1>(v);
          }
    }
    wait_bar<0>();
#pragma unroll
    for (int ks = 0; ks < 8; ++ks) q[ks] = ldsfrag(L + 131072 + wq * 8192 + ks * 1024 + lane * 16);
    if (wq >= 2) {
#pragma unroll
      for (int mt = 0; mt < 2; ++mt)
#pragma unroll
        for (int nt = 0; nt < 3; ++nt)
          if (g == 0 || nt == 0) {
            const f32x16 v = acc[mt][nt] * QSCALE; const unsigned so = (unsigned)(b * 32768 + ((2 * (wq - 2) + mt) * 8 + 2 * (3 * g + nt)) * 1024);
            st128(pack8<0>(v), sW, voff, so); st128(pack8<1>(v), sW, voff + 1024u, so);
          }
    }
    if (g == 1) {
      LAS char* Ik = L + 16384 + wq * 4096; LAS char* Iv = Ik + 2048;
      const int i16 = lane & 15;
      const unsigned tb = (unsigned)((8 * hh + (i16 >> 2)) * 64 + ((lane >> 4) & 1) * 32 + (i16 & 3) * 8);
      f32x16 kv = {};
#pragma unroll
      for (int mt = 0; mt < 2; ++mt) {
        f32x16 pq = MFMA(asbf(c.wp), asbf(pack8<0>(acc[mt][1])), (f32x16){});
        f32x16 pk = MFMA(asbf(c.wp), asbf(pack8<1>(acc[mt][1])), (f32x16){});
        softmax2(pq); softmax2(pk);
        const unsigned so = (unsigned)(WS_PQ - WS_QF) + (unsigned)(b * 16384 + (2 * wq + mt) * 2048);
        st128(pack8<0>(pq), sW, voff, so); st128(pack8<1>(pq), sW, voff + 1024u, so);
#pragma unroll
        for (int k = 0; k < 4; ++k) *(LAS u32x2*)(Ik + r32 * 64 + 16 * k + 8 * hh) = (u32x2){cvtpk(pk[4 * k], pk[4 * k + 1]), cvtpk(pk[4 * k + 2], pk[4 * k + 3])};
        const f32x16& tv = acc[mt][2];
        *(LAS u32x2*)(Iv + r32 * 64 + 8 * hh) = (u32x2){cvtpk(tv[0], tv[1]), cvtpk(tv[2], tv[3])};
        *(LAS u32x2*)(Iv + r32 * 64 + 16 + 8 * hh) = (u32x2){cvtpk(tv[4], tv[5]), cvtpk(tv[6], tv[7])};
        *(LAS u32x2*)(Iv + r32 * 64 + 32 + 8 * hh) = (u32x2){hh == 0 ? 0x00003f80u : 0u, 0u};
        *(LAS u32x2*)(Iv + r32 * 64 + 48 + 8 * hh) = (u32x2){0u, 0u};
        const float gt = 1.0f / (1.0f + __expf(-swap32_lo(tv[8])));
        __builtin_amdgcn_raw_buffer_store_b32(__float_as_uint(gt), sW, (unsigned)r32 * 4u, (unsigned)(WS_GW - WS_QF) + (unsigned)(b * 1024 + (64 * wq + 32 * mt) * 4), 0);
        LWAIT();
#pragma unroll
        for (int ks = 0; ks < 2; ++ks) kv = MFMA(trfrag(Ik + tb + ks * 1024, Ik + tb + ks * 1024 + 256), trfrag(Iv + tb + ks * 1024, Iv + tb + ks * 1024 + 256), kv);
        LWAIT();
      }
#pragma unroll
      for (int k = 0; k < 4; ++k) *(LAS f32x4*)(Ik + k * 1024 + lane * 16) = (f32x4){kv[4 * k], kv[4 * k + 1], kv[4 * k + 2], kv[4 * k + 3]};
      LBAR();
      f32x4 s4 = *(LAS const f32x4*)(L + 16384 + wq * 1024 + lane * 16);
#pragma unroll
      for (int k = 1; k < 4; ++k) s4 += *(LAS const f32x4*)(L + 16384 + k * 4096 + wq * 1024 + lane * 16);
      st128_wt(__builtin_bit_cast(u32x4, s4), sW, voff, (unsigned)(WS_KVP - WS_QF) + (unsigned)((h * 8 + r) * 4096 + wq * 1024));
    }
  }
  TS(t3);
  const unsigned ringK = rfl(lds0 + (g ? 98304u : 32768u) + (unsigned)wq * 4096u), ringV = rfl(lds0 + (g ? 0u : 65536u) + (unsigned)wq * 4096u);
  f32x16 O[4], S[2], keep0 = {}, keep1 = {}, negm = {};
#pragma unroll
  for (int d = 0; d < 4; ++d) O[d] = (f32x16){};
  bf16x8 p[4];
  float m = 0.f, l = 0.f, mk = 0.f, lk = 0.f, pm0 = 0.f;
  float gate[2] = {0.f, 0.f}; f32x2 ps = {0.f, 0.f};
  u32x2 fl2 = {0u, 0u};
  LAS const char* kbase = L + (g ? 98304 : 32768) + lane * 16;
  LAS const char* vbase = L + (g ? 0 : 65536) + lane * 16;
  LAS char* xr = L + 131072 + wq * 8192 + lane * 16;
#define TILE(s) ((s) < nX ? 2 * (s) + g : 2 * ((s) - nX) + g)
#define CF_SO(blkrow, half) ((unsigned)(WS_CF - WS_QF) + (unsigned)(((4 * (blkrow) + wq) * 288 + 8 * h + 4 * (half)) * 1024))
#define ST_SO(wv) ((unsigned)(WS_ST - WS_QF) + (unsigned)(b * 2048 + (wv) * 256))
#define ST_LOAD(wv) asm volatile("buffer_load_dwordx2 %0, %1, %2, %3 offen" : "+v"(ps) : "v"((unsigned)r32 * 8u), "s"(sW), "s"(rfl(ST_SO(wv))) : "memory")
#define L_SOFTMAX(DIAG, FIRST, D0, D1, D2, D3, D4, D5, D6, D7) do { \
    if (DIAG) { int rel = 32 * wq + r32 - 64 * g - 4 * hh; \
      _Pragma("unroll") for (int e = 0; e < 16; ++e) { const int cc = (e & 3) + 8 * (e >> 2); if ((e & 3) == 0) { asm volatile("" : "+v"(rel)); SBAR(); } \
        if (cc > rel) S[0][e] = -__builtin_inff(); if (cc + 32 > rel) S[1][e] = -__builtin_inff(); } \
      pm0 = max2f(S[0][0], S[0][1]); _Pragma("unroll") for (int e = 2; e < 16; e += 2) pm0 = max3f(pm0, S[0][e], S[0][e + 1]); } \
    float pm = max3f(pm0, S[1][0], S[1][1]), pn = max3f(S[1][2], S[1][3], S[1][4]); \
    pm = max3f(pm, S[1][5], S[1][6]); pn = max3f(pn, S[1][7], S[1][8]); pm = max3f(pm, S[1][9], S[1][10]); pn = max3f(pn, S[1][11], S[1][12]); \
    pm = max3f(pm, S[1][13], S[1][14]); pm = max3f(pm, pn, S[1][15]); \
    { auto rr = __builtin_amdgcn_permlane32_swap(__float_as_uint(pm), __float_as_uint(pm), false, false); pm = max2f(__uint_as_float(rr[0]), __uint_as_float(rr[1])); } \
    SBAR(); TS2X(f); D0; SBAR(); \
    const bool first_ = (FIRST); \
    if (first_ || __builtin_expect(__any(pm > THR), 0)) { \
      NOP16(); SBAR(); \
      const float dl = fmaxf(pm, first_ ? -1e30f : 0.f); \
      m += dl; \
      if (!first_) { const float f = __builtin_amdgcn_exp2f(-dl); l *= f; _Pragma("unroll") for (int d = 0; d < 4; ++d) O[d] = O[d] * f; } \
      _Pragma("unroll") for (int e = 0; e < 16; ++e) { S[0][e] -= dl; S[1][e] -= dl; negm[e] = -m; } \
    } \
    TS2X(g); float ls0 = l, ls1 = 0.f; unsigned pw[8]; \
    SM_GRP(0, D1); SM_GRP(2, D2); SM_GRP(4, D3); SM_GRP(6, D4); SM_GRP(8, D5); SM_GRP(10, D6); SM_GRP(12, D7); SM_GRP(14, (void)0); \
    l = ls0 + ls1; \
    p[0] = asbf((u32x4){pw[0], pw[1], pw[2], pw[3]}); p[1] = asbf((u32x4){pw[4], pw[5], pw[6], pw[7]}); } while (0)
#define SM_GRP(e0, DMA) do { const float x0 = __builtin_amdgcn_exp2f(S[0][e0]), x1 = __builtin_amdgcn_exp2f(S[0][(e0) + 1]); \
      ls0 += x0; ls1 += x1; pw[(e0) >> 1] = cvtpk(x0, x1); asm volatile("" : "+v"(pw[(e0) >> 1]), "+v"(ls0), "+v"(ls1)); DMA; SBAR(); } while (0)
#define L_SUM() do { } while (0)
  float mo = 0.f, lo = 0.f;
  auto l_pre = [&](int s) {
    if (s == nX) {
      NOP16(); SBAR();
      mo = m; lo = swap32_add(l);
      if (g == 1) ST_LOAD(w - 4);
      __builtin_amdgcn_raw_buffer_store_b64(__builtin_bit_cast(u32x2, (f32x2){mo, lo}), sW, (unsigned)r32 * 8u, ST_SO(w), 0);
      if (g == 0) { a_send(O[2], O[3], xr); keep0 = O[0]; keep1 = O[1]; mk = mo; lk = lo; }
      m = 0.f; l = 0.f;
    }
    if (g == 0 && s == nX + 1) ST_LOAD(w + 4);
  };
  auto l_post = [&](int s) {
    if (s == nX) {
      if (g == 1) {
        wait_vm<8>(); asm volatile("" : "+v"(ps));
        a_finish(O[2], O[3], mo, lo, xr, ps[0], ps[1], gate[0], sW, voff, CF_SO(X, 1));
        LWAIT();
        a_send(O[0], O[1], xr);
      }
#pragma unroll
      for (int d = 0; d < 4; ++d) O[d] = (f32x16){};
    }
    if (g == 0 && s == nX + 1) {
      wait_vm<8>(); asm volatile("" : "+v"(ps));
      a_finish(keep0, keep1, mk, lk, xr, ps[0], ps[1], gate[0], sW, voff, CF_SO(X, 0));
    }
  };
  unsigned mK = ringK, mV = ringV + 16384u, oK = (unsigned)(4 + g) * 16384u + (unsigned)wq * 4096u, oV = (unsigned)(2 + g) * 16384u + (unsigned)wq * 4096u;
  LAS const char* kp = kbase + 16384; LAS const char* vp = vbase;
#define A_TAIL() do { \
    asm volatile("" : "+v"(p[0]), "+v"(p[1]), "+v"(l), "+v"(S[1])); \
    SBAR(); TS2(d); wait_bar<8>(); TS2(e); SBAR(); \
    a_mphase<true, true>(kp, vp, q, p, S, O, negm, l, pm0 STP_ARG); \
    mK ^= 16384u; mV ^= 16384u; kp = (LAS const char*)((unsigned)(uintptr_t)kp ^ 16384u); vp = (LAS const char*)((unsigned)(uintptr_t)vp ^ 16384u); } while (0)
#define A_FAST(FIRST) do { \
    if (!(FIRST)) L_SUM(); \
    L_SOFTMAX(false, FIRST, dma16o<0>(mK, voff, sK, oK), dma16n<1024>(voff, sK, oK), dma16n<2048>(voff, sK, oK), dma16n<3072>(voff, sK, oK), \
              dma16o<0>(mV, voff, sV, oV), dma16n<1024>(voff, sV, oV), dma16n<2048>(voff, sV, oV), dma16n<3072>(voff, sV, oV)); \
    oK += 32768u; oV += 32768u; TS2X(h); A_TAIL(); } while (0)
#define A_SLOW(s) do { \
    L_SUM(); l_pre(s); \
    oK = (s) < 15 ? (unsigned)TILE((s) + 2) * 16384u + (unsigned)wq * 4096u : (unsigned)(WS_KVP - WS_QF) + (unsigned)(h * 32768 + w * 4096); \
    oV = (unsigned)TILE((s) + 1) * 16384u + (unsigned)wq * 4096u; \
    const srd_t sKx = (s) < 15 ? sK : sW; \
    L_SOFTMAX((s) == nX - 1, (s) == nX, dma16o<0>(mK, voff, sKx, oK), dma16n<1024>(voff, sKx, oK), dma16n<2048>(voff, sKx, oK), dma16n<3072>(voff, sKx, oK), \
              dma16o<0>(mV, voff, sV, oV), dma16n<1024>(voff, sV, oV), dma16n<2048>(voff, sV, oV), dma16n<3072>(voff, sV, oV)); \
    oK += 32768u; oV += 32768u; TS2X(h); \
    l_post(s); \
    if ((s) == nX - 1) { _Pragma("unroll") for (int ks = 0; ks < 8; ++ks) q[ks] = ldsfrag(L + 131072 + wq * 8192 + ks * 1024 + lane * 16); negm = (f32x16){}; } \
    A_TAIL(); } while (0)
  a_mphase<true, false>(kbase, vbase, q, p, S, O, negm, l, pm0 STP_ARG);
  A_FAST(true);
  if (w == 0 && lane == 0) __hip_atomic_store((u64*)flg + 8 + r, tag, __ATOMIC_RELAXED, __HIP_MEMORY_SCOPE_AGENT);
  A_FAST(false);
  {
    const unsigned mq = rfl(lds0 + 131072u + (unsigned)w * 4096u), oq = rfl((unsigned)(b * 32768 + w * 4096));
    dma16o<0>(mq, voff, sW, oq); dma16n<1024>(voff, sW, oq); dma16n<2048>(voff, sW, oq); dma16n<3072>(voff, sW, oq);
    const unsigned go = rfl((unsigned)(WS_GW - WS_QF) + (unsigned)(b * 1024 + wq * 128));
    asm volatile("buffer_load_dword %0, %2, %3, %4 offen\n\tbuffer_load_dword %1, %2, %3, %4 offen offset:512" : "+v"(gate[0]), "+v"(gate[1]) : "v"((unsigned)r32 * 4u), "s"(sW), "s"(go) : "memory");
    poll_issue(fl2, flg + 8, lane);
  }
  A_FAST(false);
  asm volatile("" : "+v"(gate[0]), "+v"(gate[1]), "+v"(fl2));
  if (w == 0) { poll_check(fl2, flg + 8, tag, lane); if (r == 0 && lane == 0) __hip_atomic_store((u64*)flg + 16, tag, __ATOMIC_RELAXED, __HIP_MEMORY_SCOPE_AGENT); }
  {
    int s = 3;
#pragma unroll 1
    for (int blk = 0; blk < 2; ++blk) {
      const int e1 = blk ? 15 : nX - 2, e2 = blk ? 16 : (nX + 2 < 16 ? nX + 2 : 16);
#pragma unroll 1
      for (; s < e1; ++s) A_FAST(false);
#pragma unroll 1
      for (; s < e2; ++s) A_SLOW(s);
    }
  }
  TS(t4);
  {
    L_SUM(); l_pre(16);
    const unsigned mp = rfl(lds0 + (g ? 98304u : 32768u) + (unsigned)wq * 2048u), op = rfl((unsigned)(WS_PQ - WS_QF) + (unsigned)(b * 16384 + w * 2048));
    L_SOFTMAX(true, nX == 16, dma16o<0>(mp, voff, sW, op), dma16n<1024>(voff, sW, op), (void)0, (void)0, (void)0, (void)0, (void)0, (void)0);
    if (16 == nX || 16 == nX + 1) wait_vm<0>();
    l_post(16);
    asm volatile("" : "+v"(p[0]), "+v"(p[1]), "+v"(l), "+v"(S[1]));
    SBAR(); wait_bar<2>(); SBAR();
  }
  a_mphase<false, true>(kbase, vbase, q, p, S, O, negm, l, pm0 STP_ARG);
  NOP16(); SBAR();
  L_SUM();
  if (g == 0 && nX == 16) {
    ST_LOAD(w + 4); wait_vm<0>(); asm volatile("" : "+v"(ps));
    a_finish(keep0, keep1, mk, lk, xr, ps[0], ps[1], gate[0], sW, voff, CF_SO(X, 0));
    LWAIT();
  }
  LAS f32x2* stl = (LAS f32x2*)(L + 81920) + r32;
  auto stage2 = [&]() {
    f32x16 kv = {};
#pragma unroll
    for (int k = 0; k < 8; ++k)
#pragma unroll
      for (int qd = 0; qd < 4; ++qd) { const f32x4 v = *(LAS const f32x4*)(L + (k < 4 ? 49152 : 114688) + (k & 3) * 4096 + qd * 1024 + lane * 16);
#pragma unroll
        for (int e = 0; e < 4; ++e) kv[4 * qd + e] += v[e]; }
    const bf16x8 p0 = ldsfrag(kbase + wq * 2048), p1 = ldsfrag(kbase + wq * 2048 + 1024);
    f32x16 o = MFMA(asbf(pack8<0>(kv)), p0, (f32x16){});
    o = MFMA(asbf(pack8<1>(kv)), p1, o);
    const float sc = (1.0f - (g ? gate[1] : gate[0])) / (swap32_lo(o[8]) + 1e-6f);
    o = o * sc;
    st128(pack8<0>(o), sW, voff, (unsigned)(WS_CF - WS_QF) + (unsigned)(((4 * (g ? Y : X) + wq) * 288 + 256 + h) * 1024));
  };
  const float lt = swap32_add(l);
  if (g == 0) {
    a_send(O[2], O[3], xr);
    stl[w * 32] = (f32x2){m, lt};
    wait_bar<0>();
    stage2();
    LBAR();
    const f32x2 po = stl[(w + 4) * 32];
    a_finish(O[0], O[1], m, lt, xr, po[0], po[1], gate[1], sW, voff, CF_SO(Y, 0));
  } else {
    const f32x2 po = stl[(w - 4) * 32];
    a_finish(O[2], O[3], m, lt, xr, po[0], po[1], gate[1], sW, voff, CF_SO(Y, 1));
    LWAIT();
    a_send(O[0], O[1], xr);
    stl[w * 32] = (f32x2){m, lt};
    wait_bar<4>();
    stage2();
  }
#undef TILE
#undef CF_SO
#undef ST_SO
#undef ST_LOAD
#undef L_SOFTMAX
#undef SM_GRP
#undef L_SUM
#undef A_TAIL
#undef A_FAST
#undef A_SLOW
}

#define OD 8
constexpr int O_D = OD;
struct OCtx {
  srd_t sC, sWa, sWb; unsigned voff, voffB, soA, soW, mW, bB;
  LAS char* L; bf16x8 a[O_D][2]; f32x16 acc[2][2];
};
template <int i> DI void o_issue(OCtx& c) {
  if constexpr (i < 36) {
    constexpr unsigned sl = (unsigned)(i % O_D) * 2048u;
    if constexpr (i < 32) { dma16c<sl, i * 131072u>(c.mW, c.voffB, c.sWa, c.soW); dma16c<sl + 1024u, i * 131072u + 8192u>(c.mW, c.voffB, c.sWa, c.soW); }
    else                  { dma16c<sl, (i - 32) * 131072u>(c.mW, c.voffB, c.sWb, c.soW); dma16c<sl + 1024u, (i - 32) * 131072u + 8192u>(c.mW, c.voffB, c.sWb, c.soW); }
    unsigned t;
    asm volatile("s_add_u32 %2, %5, %6\n\tbuffer_load_dwordx4 %0, %3, %4, %2 offen\n\ts_add_u32 %2, %5, %7\n\tbuffer_load_dwordx4 %1, %3, %4, %2 offen"
                 : "=&v"(c.a[i % O_D][0]), "=&v"(c.a[i % O_D][1]), "=&s"(t) : "v"(c.voff), "s"(c.sC), "s"(c.soA), "i"(i * 8192u), "i"(i * 8192u + 294912u) : "memory", "scc");
  }
}
template <int i> DI void o_step(OCtx& c) {
  constexpr int NY = (35 - i < O_D - 1 ? 35 - i : O_D - 1) * 4;
  asm volatile("s_waitcnt vmcnt(%2)" : "+v"(c.a[i % O_D][0]), "+v"(c.a[i % O_D][1]) : "i"(NY) : "memory");
  LAS const char* sp = c.L + c.bB + (i % O_D) * 2048;
  const bf16x8 f0 = trfrag(sp, sp + 1024), f1 = trfrag(sp + 512, sp + 1536);
  const bf16x8 a0 = c.a[i % O_D][0], a1 = c.a[i % O_D][1];
  c.acc[0][0] = MFMA(f0, a0, c.acc[0][0]); c.acc[1][0] = MFMA(f0, a1, c.acc[1][0]);
  c.acc[0][1] = MFMA(f1, a0, c.acc[0][1]); c.acc[1][1] = MFMA(f1, a1, c.acc[1][1]);
  SBAR();
  o_issue<i + O_D>(c);
  SBAR();
}
template <int i> DI void o_prologue(OCtx& c) { if constexpr (i < O_D) { o_issue<i>(c); o_prologue<i + 1>(c); } }
template <int i> DI void o_steps(OCtx& c) { if constexpr (i < 36) { o_step<i>(c); o_steps<i + 1>(c); } }
__global__ __launch_bounds__(512, 2) void kO(const u16* __restrict__ wo_a, const u16* __restrict__ wo_b, char* __restrict__ ws, u16* __restrict__ out) {
  extern __shared__ __attribute__((aligned(16))) char lds[];
  LAS char* L = (LAS char*)lds; const unsigned lds0 = (unsigned)(uintptr_t)L;
  const int tid = threadIdx.x, lane = tid & 63, w = __builtin_amdgcn_readfirstlane(tid >> 6);
  const int b = blockIdx.x, idx = b >> 3, mi = (b & 7) * 4 + (idx & 3), ni = idx >> 2;
  const int hh = lane >> 5, r32 = lane & 31, i16 = lane & 15, p4 = i16 & 3;
  TS(z0);
  OCtx c;
  c.L = L;
  c.sC = mksrd(ws + WS_CF, 64u * 288u * 1024u); c.sWa = mksrd(wo_a, 4096u * 1024u); c.sWb = mksrd(wo_b, 512u * 1024u);
  c.voff = (unsigned)lane * 16u;
  c.voffB = (unsigned)(((lane >> 2) & 7) * 1024 + (lane >> 5) * 64 + (lane & 3) * 16);
  c.soA = rfl((unsigned)(2 * mi * 288 + w) * 1024u);
  c.soW = rfl((unsigned)(16 * w) * 1024u + (unsigned)ni * 128u);
  c.mW = rfl(lds0 + (unsigned)(w * O_D) * 2048u);
  c.bB = (unsigned)(w * O_D * 2048 + (4 * hh + (i16 >> 2)) * 64 + ((lane >> 4) & 1) * 32 + (((p4 & 1) << 1) | (p4 >> 1)) * 8);
  o_prologue<0>(c);
#pragma unroll
  for (int mt = 0; mt < 2; ++mt)
#pragma unroll
    for (int nt = 0; nt < 2; ++nt) c.acc[mt][nt] = (f32x16){};
  o_steps<0>(c);
  TS(z1);
  LBAR();
#pragma unroll
  for (int t = 0; t < 4; ++t)
#pragma unroll
    for (int qd = 0; qd < 4; ++qd) { const f32x16& v = c.acc[t >> 1][t & 1];
      *(LAS f32x4*)(L + w * 16384 + (t * 4 + qd) * 1024 + lane * 16) = (f32x4){v[4 * qd], v[4 * qd + 1], v[4 * qd + 2], v[4 * qd + 3]}; }
  LBAR();
  f32x4 s0 = {0.f, 0.f, 0.f, 0.f}, s1 = s0;
#pragma unroll
  for (int k = 0; k < 8; ++k) {
    s0 += *(LAS const f32x4*)(L + k * 16384 + w * 2048 + lane * 16);
    s1 += *(LAS const f32x4*)(L + k * 16384 + w * 2048 + 1024 + lane * 16);
  }
  const int t = w >> 1, h8 = w & 1;
  u16* op = out + (size_t)(64 * mi + 32 * (t >> 1) + r32) * 512 + 64 * ni + 32 * (t & 1) + 16 * h8 + 8 * hh;
  *(u32x4*)op = (u32x4){cvtpk(s0[0], s0[1]), cvtpk(s0[2], s0[3]), cvtpk(s1[0], s1[1]), cvtpk(s1[2], s1[3])};
}

extern "C" void kernel_launch(void* const* d_in, const int* in_sizes, int n_in, void* d_out, int out_size, void* d_ws, size_t ws_size, hipStream_t stream) {
  const u16* x = (const u16*)d_in[0]; const u16* ckv = (const u16*)d_in[1]; const u16* wq = (const u16*)d_in[2];
  const u16* wuk = (const u16*)d_in[3]; const u16* wuv = (const u16*)d_in[4]; const u16* wo_a = (const u16*)d_in[5];
  const u16* wqkv = (const u16*)d_in[7]; const u16* wphi = (const u16*)d_in[8]; const u16* wo_b = (const u16*)d_in[9]; const u16* gatew = (const u16*)d_in[10];
  (void)hipFuncSetAttribute((const void*)kF, hipFuncAttributeMaxDynamicSharedMemorySize, 163840);
  (void)hipFuncSetAttribute((const void*)kO, hipFuncAttributeMaxDynamicSharedMemorySize, 163840);
  kF<<<256, 512, 163840, stream>>>(x, ckv, wq, wuk, wuv, wqkv, wphi, gatew, (char*)d_ws);
  kO<<<256, 512, O_D > 8 ? O_D * 16384 : 131072, stream>>>(wo_a, wo_b, (char*)d_ws, (u16*)d_out);
}
```

```cpp
#define PRIO_M
#include <hip/hip_runtime.h>
#include <stdint.h>

typedef unsigned short u16;
typedef unsigned long long u64;
typedef __bf16 bf16x8 __attribute__((ext_vector_type(8)));
typedef __bf16 bf16x2 __attribute__((ext_vector_type(2)));
typedef float f32x2 __attribute__((ext_vector_type(2)));
typedef float f32x4 __attribute__((ext_vector_type(4)));
typedef float f32x16 __attribute__((ext_vector_type(16)));
typedef unsigned u32x2 __attribute__((ext_vector_type(2)));
typedef unsigned u32x4 __attribute__((ext_vector_type(4)));
typedef short s16x4 __attribute__((ext_vector_type(4)));
typedef short s16x8 __attribute__((ext_vector_type(8)));
typedef __amdgpu_buffer_rsrc_t srd_t;
#define LAS __attribute__((address_space(3)))
#define DI __device__ __forceinline__
#define SBAR() __builtin_amdgcn_sched_barrier(0)
#define MFMA(a, b, c) __builtin_amdgcn_mfma_f32_32x32x16_bf16((a), (b), (c), 0, 0, 0)

constexpr size_t MB = 1024 * 1024;
constexpr size_t WS_KF = 0;
constexpr size_t WS_VF = 16 * MB;
constexpr size_t WS_QF = 32 * MB;
constexpr size_t WS_CF = 48 * MB;
constexpr size_t WS_PQ = 68 * MB;
constexpr size_t WS_KVP = 72 * MB;
constexpr size_t WS_GW = 73 * MB;
constexpr size_t WS_ST = 74 * MB;
constexpr size_t WS_SLAB = 76 * MB;
constexpr size_t WS_CNT = 92 * MB;
constexpr size_t WS_FLG = 93 * MB;
constexpr size_t WS_DBG = 100 * MB;

constexpr float QSCALE = 0.08838834764831845f * 1.4426950408889634f;
constexpr float THR = 8.0f;

DI unsigned cvtpk(float lo, float hi) { f32x2 v = {lo, hi}; bf16x2 b = __builtin_convertvector(v, bf16x2); return __builtin_bit_cast(unsigned, b); }
template <int S> DI u32x4 pack8(const f32x16& x) {
  return (u32x4){cvtpk(x[8 * S + 0], x[8 * S + 1]), cvtpk(x[8 * S + 2], x[8 * S + 3]), cvtpk(x[8 * S + 4], x[8 * S + 5]), cvtpk(x[8 * S + 6], x[8 * S + 7])};
}
DI bf16x8 asbf(u32x4 v) { return __builtin_bit_cast(bf16x8, v); }
DI float swap32_max(float v) { auto rr = __builtin_amdgcn_permlane32_swap(__float_as_uint(v), __float_as_uint(v), false, false); return fmaxf(__uint_as_float(rr[0]), __uint_as_float(rr[1])); }
DI float swap32_add(float v) { auto rr = __builtin_amdgcn_permlane32_swap(__float_as_uint(v), __float_as_uint(v), false, false); return __uint_as_float(rr[0]) + __uint_as_float(rr[1]); }
DI float swap32_lo(float v) { auto rr = __builtin_amdgcn_permlane32_swap(__float_as_uint(v), __float_as_uint(v), false, false); return __uint_as_float(rr[0]); }
DI float bf16round(float x) { return __uint_as_float(cvtpk(x, 0.f) << 16); }
DI srd_t mksrd(const void* p, unsigned bytes) { return __builtin_amdgcn_make_buffer_rsrc((void*)p, (short)0, (int)bytes, 0x00020000); }
DI unsigned rfl(unsigned x) { return (unsigned)__builtin_amdgcn_readfirstlane((int)x); }
template <int OFF> DI void dma16o(unsigned m0v, unsigned voff, srd_t srd, unsigned soff) {
  asm volatile("s_mov_b32 m0, %0\n\ts_nop 0\n\tbuffer_load_dwordx4 %1, %2, %3 offen offset:%4 sc1 lds" :: "s"(m0v), "v"(voff), "s"(srd), "s"(soff), "i"(OFF) : "memory");
}
template <unsigned MOFF, unsigned SOFF> DI void dma16s(unsigned mb, unsigned voff, srd_t srd, unsigned sb) {
  unsigned t;
  asm volatile("s_add_u32 m0, %1, %5\n\ts_add_u32 %0, %4, %6\n\tbuffer_load_dwordx4 %2, %3, %0 offen sc1 lds" : "=&s"(t) : "s"(mb), "v"(voff), "s"(srd), "s"(sb), "i"(MOFF), "i"(SOFF) : "memory", "scc");
}
template <int OFF> DI void dma16n(unsigned voff, srd_t srd, unsigned soff) {
  asm volatile("buffer_load_dwordx4 %0, %1, %2 offen offset:%3 sc1 lds" :: "v"(voff), "s"(srd), "s"(soff), "i"(OFF) : "memory");
}
template <unsigned MOFF, unsigned SOFF> DI void dma16c(unsigned mb, unsigned voff, srd_t srd, unsigned sb) {
  unsigned t;
  asm volatile("s_add_u32 m0, %1, %5\n\ts_add_u32 %0, %4, %6\n\tbuffer_load_dwordx4 %2, %3, %0 offen lds" : "=&s"(t) : "s"(mb), "v"(voff), "s"(srd), "s"(sb), "i"(MOFF), "i"(SOFF) : "memory", "scc");
}
template <unsigned MOFF, unsigned SOFF> DI void dma16m(unsigned mb, unsigned voff, srd_t srd, unsigned sb) {
  unsigned t;
  asm volatile("s_add_u32 m0, %1, %5\n\ts_add_u32 %0, %4, %6\n\ts_mov_b32 exec_hi, 0x33333333\n\tbuffer_load_dwordx4 %2, %3, %0 offen lds\n\ts_mov_b32 exec_hi, -1"
               : "=&s"(t) : "s"(mb), "v"(voff), "s"(srd), "s"(sb), "i"(MOFF), "i"(SOFF) : "memory", "scc");
}
template <int N> DI void wait_bar() { asm volatile("s_waitcnt vmcnt(%0) lgkmcnt(0)\n\ts_barrier" :: "i"(N) : "memory"); }
template <int N> DI void wait_vm() { asm volatile("s_waitcnt vmcnt(%0)" :: "i"(N) : "memory"); }
#define LBAR() asm volatile("s_waitcnt lgkmcnt(0)\n\ts_barrier" ::: "memory")
#define LWAIT() asm volatile("s_waitcnt lgkmcnt(0)" ::: "memory")
#define NOP16() asm volatile("s_nop 7\n\ts_nop 7" ::: "memory")
DI bf16x8 ldsfrag(LAS const char* p) { return *(LAS const bf16x8*)p; }
DI bf16x8 trfrag(LAS const char* p0, LAS const char* p1) {
  s16x4 a = __builtin_amdgcn_ds_read_tr16_b64_v4i16((LAS s16x4*)p0);
  s16x4 b = __builtin_amdgcn_ds_read_tr16_b64_v4i16((LAS s16x4*)p1);
  return __builtin_bit_cast(bf16x8, __builtin_shufflevector(a, b, 0, 1, 2, 3, 4, 5, 6, 7));
}
DI void st128(u32x4 v, srd_t s, unsigned vo, unsigned so) { __builtin_amdgcn_raw_buffer_store_b128(v, s, vo + so, 0, 0); }
DI void st128_wt(u32x4 v, srd_t s, unsigned vo, unsigned so) { __builtin_amdgcn_raw_buffer_store_b128(v, s, vo + so, 0, 16); }
#define TS(x)

#define KVAUX 16
struct PCtx {
  srd_t sA1, sA2, sUK, sUV, sWQ, sWB, sWP, sK, sV;
  unsigned voffA[2], voffB, voffS, voff;
  unsigned soA1, soA2, soB, soS, soKa, so2, mA, mB2, m2;
  unsigned gv[8];
  u32x2 fl; u64 tag; const u64* flg;
  u32x4 wp;
  unsigned soPW, soPS, soPX;
  u32x4 kvp[8]; unsigned kvo;
  int w, lane, h;
  LAS char* L;
};
#define TS3(x) do { SBAR(); asm volatile("s_memtime %0\n\ts_waitcnt lgkmcnt(0)" : "=s"(x) :: "memory"); SBAR(); } while (0)
template <int T, int I> DI void p_pieceA(const PCtx& c) {
  if constexpr (T < 24) {
    constexpr unsigned st = (unsigned)(T % 3) * 32768u, kt = (unsigned)(T & 7);
    if constexpr (T < 16) dma16c<st + I * 1024u, I * 8192u + kt * 128u>(c.mA, c.voffA[I & 1], c.sA1, c.soA1);
    else                  dma16c<st + I * 1024u, I * 8192u + kt * 128u>(c.mA, c.voffA[I & 1], c.sA2, c.soA2);
  }
}
template <int T, int I> DI void p_pieceB(const PCtx& c) {
  constexpr unsigned st = 98304u + (unsigned)(T & 1) * 32768u, kt = (unsigned)(T & 7);
  if constexpr (T < 16) {
    if constexpr (I < 2) dma16c<st + I * 1024u, kt * 524288u + I * 128u>(c.m2, c.voffB, T < 8 ? c.sUK : c.sUV, c.soB);
  } else if constexpr (T < 24) {
    if constexpr (I < 2) dma16c<st + I * 1024u, kt * 524288u + I * 128u>(c.mB2, c.voffB, c.sWQ, c.soB);
    else if constexpr (I == 2) dma16m<st + 2048u, kt * 196608u>(c.mB2, c.voffS, c.sWB, c.soS);
    else if (c.w == 7) *(LAS u16*)(c.L + st + ((c.lane >> 3) * 6 + 5) * 512 + (c.lane & 7) * 64 + 32) = (u16)c.gv[kt];
  }
}
template <int I, bool ISV> DI void p_kvstore(const PCtx& c) {
  if constexpr (I >= 0 && I < 8) {
    constexpr int mt = I >> 2, nt = (I >> 1) & 1, sh = I & 1;
    constexpr unsigned off = (unsigned)((ISV ? nt * 4 + 2 * mt + sh : mt * 8 + 2 * nt + sh) * 1024);
    __builtin_amdgcn_raw_buffer_store_b128(c.kvp[I], ISV ? c.sV : c.sK, c.kvo + off, 0, KVAUX);
  }
}
#define WARM1(VO, SRD, SO, OFF) dma16c<114688u, (OFF)>(c.m2, VO, SRD, SO)
template <int I> DI void p_warm(PCtx& c) {
  if constexpr (I == 0) { WARM1(c.voff, c.sA1, c.soPX, 0); WARM1(c.voffB, c.sUK, c.soPW, 0); WARM1(c.voffB, c.sUK, c.soPW, 128); }
  if constexpr (I == 1) { WARM1(c.voffB, c.sUV, c.soPW, 0); WARM1(c.voffB, c.sUV, c.soPW, 128); }
  if constexpr (I == 2) { WARM1(c.voff, c.sA2, c.soPX, 0); WARM1(c.voffB, c.sWQ, c.soPW, 0); }
  if constexpr (I == 3) { WARM1(c.voffB, c.sWQ, c.soPW, 128); WARM1(c.voffS, c.sWB, c.soPS, 0); }
}
#undef WARM1
DI void poll_spin(const u64* f, u64 tag, int lane) {
  while (!__all(__hip_atomic_load(f + (lane & 7), __ATOMIC_RELAXED, __HIP_MEMORY_SCOPE_AGENT) == tag)) __builtin_amdgcn_s_sleep(2);
}
DI void poll_issue(u32x2& fl, const u64* f, int lane) { asm volatile("global_load_dwordx2 %0, %1, off sc1" : "+v"(fl) : "v"(f + (lane & 7)) : "memory"); }
DI void poll_check(u32x2& fl, const u64* f, u64 tag, int lane) {
  asm volatile("" : "+v"(fl));
  if (!__all(fl[0] == (unsigned)tag && fl[1] == (unsigned)(tag >> 32))) poll_spin(f, tag, lane);
  asm volatile("" ::: "memory");
}
template <int B> DI void p_hook(PCtx& c) {
  if constexpr (B == 17) { if (c.w == 0) dma16c<155648u, 0u>(c.mA, c.voff, c.sWP, (unsigned)c.h * 512u); }
  if constexpr (B == 21) {
    if (c.w == 0) { if (c.lane == 0) __hip_atomic_store((u64*)c.flg + (blockIdx.x >> 5), c.tag, __ATOMIC_RELAXED, __HIP_MEMORY_SCOPE_AGENT); }
    if (c.w >= 4) {
      const int r32 = c.lane & 31, hh = c.lane >> 5; LAS const u16* wb = (LAS const u16*)(c.L + 155648) + (r32 & 15); const unsigned sg = r32 >= 16 ? 0x80008000u : 0u;
#pragma unroll
      for (int jj = 0; jj < 4; ++jj) { const int d0 = 8 * (jj >> 1) + 4 * hh + 2 * (jj & 1); c.wp[jj] = ((unsigned)wb[d0 * 16] | ((unsigned)wb[(d0 + 1) * 16] << 16)) ^ sg; }
    }
  }
  if constexpr (B == 22) { if (c.w == 0) poll_issue(c.fl, c.flg, c.lane); }
  if constexpr (B == 23) { if (c.w == 0) poll_check(c.fl, c.flg, c.tag, c.lane); }
  if constexpr (B == 24) {
    dma16s<32768u, 0u>(c.mA, c.voff, c.sK, c.soKa); dma16s<32768u + 1024u, 1024u>(c.mA, c.voff, c.sK, c.soKa);
    dma16s<32768u + 2048u, 2048u>(c.mA, c.voff, c.sK, c.soKa); dma16s<32768u + 3072u, 3072u>(c.mA, c.voff, c.sK, c.soKa);
    dma16s<98304u, 16384u>(c.mA, c.voff, c.sK, c.soKa); dma16s<98304u + 1024u, 16384u + 1024u>(c.mA, c.voff, c.sK, c.soKa);
    dma16s<98304u + 2048u, 16384u + 2048u>(c.mA, c.voff, c.sK, c.soKa); dma16s<98304u + 3072u, 16384u + 3072u>(c.mA, c.voff, c.sK, c.soKa);
    dma16s<65536u, 0u>(c.m2, c.voff, c.sV, c.so2); dma16s<65536u + 1024u, 1024u>(c.m2, c.voff, c.sV, c.so2);
    dma16s<0u, 16384u>(c.m2, c.voff, c.sV, c.so2); dma16s<1024u, 16384u + 1024u>(c.m2, c.voff, c.sV, c.so2);
  }
}

template <int PASS, int MT, int NT, int NSUB, bool TRANS>
struct PPass {
  PCtx& c; f32x16 (&acc)[MT][NT];
  unsigned aB[2][4], bB;
  bf16x8 fa[2][MT], fb[2][NT];
  template <int SET, int T, int KS> DI void load() {
    constexpr int bo = (T & 1) * 32768 + KS * (2 * NSUB * 512), sl = T % 3;
#pragma unroll
    for (int nt = 0; nt < NT; ++nt) fb[SET][nt] = trfrag(c.L + bB + bo + nt * 512, c.L + bB + bo + nt * 512 + 256);
#pragma unroll
    for (int mt = 0; mt < MT; ++mt) fa[SET][mt] = ldsfrag(c.L + aB[sl >> 1][KS] + (sl & 1) * 32768 + mt * 4096);
  }
  template <int SET, int I> DI void mma1() {
    if constexpr (I < MT * NT) {
      constexpr int mt = I / NT, nt = I % NT;
      acc[mt][nt] = TRANS ? MFMA(fb[SET][nt], fa[SET][mt], acc[mt][nt]) : MFMA(fa[SET][mt], fb[SET][nt], acc[mt][nt]);
    }
  }
  template <int SET, int T, int I0, int S0> DI void mmaA() {
    mma1<SET, 0>(); SBAR(); p_pieceA<T, I0>(c); SBAR(); mma1<SET, 1>(); SBAR(); p_kvstore<S0, PASS == 2>(c); SBAR(); mma1<SET, 2>(); SBAR(); p_pieceA<T, I0 + 1>(c); SBAR();
    mma1<SET, 3>(); mma1<SET, 4>(); mma1<SET, 5>();
  }
  template <int t> DI void tile() {
    constexpr int T = PASS * 8 + t;
    constexpr int SA = PASS == 1 ? t : (PASS == 2 && t < 4 ? 2 * t : -1), SB = PASS == 2 && t < 4 ? 2 * t + 1 : -1;
    constexpr int NW = T == 0 ? 5 : (T < 3 ? 2 : 0);
    constexpr int NST = (SA >= 0 ? 1 : 0) + (SB >= 0 ? 1 : 0) + NW;
    SBAR(); load<1, T, 1>(); SBAR(); mmaA<0, T + 2, 0, SA>();
    SBAR(); load<0, T, 2>(); SBAR(); mmaA<1, T + 2, 2, -1>();
    SBAR(); load<1, T, 3>(); SBAR(); if constexpr (T < 3) p_warm<T + 1>(c); SBAR(); mmaA<0, 24, 0, SB>();
    SBAR();
    if constexpr (T == 23) LBAR(); else if constexpr (T == 22) wait_bar<0>(); else wait_bar<4 + NST>();
    SBAR();
    if constexpr (t < 7) load<0, T + 1, 0>();
    SBAR();
    mma1<1, 0>(); p_pieceB<T + 2, 0>(c); SBAR();
    mma1<1, 1>(); p_pieceB<T + 2, 1>(c); SBAR();
    mma1<1, 2>(); p_pieceB<T + 2, 2>(c); SBAR();
    mma1<1, 3>(); p_pieceB<T + 2, 3>(c); SBAR();
    mma1<1, 4>(); mma1<1, 5>(); SBAR();
    p_hook<T + 1>(c); SBAR();
  }
  DI void run(unsigned arow0, unsigned nt0) {
    const int lane = c.lane, r = lane & 31, hh = lane >> 5, i16 = lane & 15;
#pragma unroll
    for (int ks = 0; ks < 4; ++ks) { aB[0][ks] = (arow0 + r) * 128u + (unsigned)(((2 * ks + hh) ^ ((r >> 1) & 7)) * 16); aB[1][ks] = aB[0][ks] + 65536u; asm volatile("" : "+v"(aB[1][ks])); }
    bB = 98304u + (unsigned)(hh * NSUB + nt0) * 512u + (unsigned)((i16 >> 2) * 64 + ((lane >> 4) & 1) * 32 + (i16 & 3) * 8); asm volatile("" : "+v"(bB));
#pragma unroll
    for (int mt = 0; mt < MT; ++mt)
#pragma unroll
      for (int nt = 0; nt < NT; ++nt) acc[mt][nt] = (f32x16){};
    load<0, PASS * 8, 0>();
    tile<0>(); tile<1>(); tile<2>(); tile<3>(); tile<4>(); tile<5>(); tile<6>(); tile<7>();
  }
};
template <int PASS, int MT, int NT, int NSUB, bool TRANS>
DI void p_pass(PCtx& c, f32x16 (&acc)[MT][NT], unsigned arow0, unsigned nt0) { PPass<PASS, MT, NT, NSUB, TRANS> p{c, acc}; p.run(arow0, nt0); }

DI void softmax2(f32x16& x) {
#pragma unroll
  for (int s = 0; s < 2; ++s) {
    float v[8];
#pragma unroll
    for (int j = 0; j < 8; ++j) v[j] = bf16round(x[8 * s + j]);
    float mx = v[0];
#pragma unroll
    for (int j = 1; j < 8; ++j) mx = fmaxf(mx, v[j]);
    mx = swap32_max(mx);
    float sum = 0.f;
#pragma unroll
    for (int j = 0; j < 8; ++j) { v[j] = __builtin_amdgcn_exp2f((v[j] - mx) * 1.4426950408889634f); sum += v[j]; }
    sum = swap32_add(sum);
    const float inv = 1.0f / sum;
#pragma unroll
    for (int j = 0; j < 8; ++j) x[8 * s + j] = v[j] * inv;
  }
}

DI void mfma_acc(f32x16& c, bf16x8 a, bf16x8 b) { asm volatile("v_mfma_f32_32x32x16_bf16 %0, %1, %2, %0" : "+v"(c) : "v"(a), "v"(b)); }
DI void mfma_acc2(f32x16& c, bf16x8 a, bf16x8 b, bf16x8 nx) { asm volatile("v_mfma_f32_32x32x16_bf16 %0, %1, %2, %0" : "+v"(c) : "v"(a), "v"(b), "v"(nx)); }
DI void mfma_init2(f32x16& c, bf16x8 a, bf16x8 b, const f32x16& ci, bf16x8 nx) { asm volatile("v_mfma_f32_32x32x16_bf16 %0, %1, %2, %3" : "=&v"(c) : "v"(a), "v"(b), "v"(ci), "v"(nx)); }
DI float sum2(unsigned pk, float acc) { return __builtin_amdgcn_fdot2_f32_bf16(__builtin_bit_cast(bf16x2, pk), __builtin_bit_cast(bf16x2, 0x3f803f80u), acc, false); }
DI float sum8(bf16x8 p, float acc) { const u32x4 u = __builtin_bit_cast(u32x4, p); return sum2(u[3], sum2(u[2], sum2(u[1], sum2(u[0], acc)))); }
DI float max3f(float a, float b, float c) { float r; asm("v_max3_f32 %0, %1, %2, %3" : "=v"(r) : "v"(a), "v"(b), "v"(c)); return r; }
DI float max2f(float a, float b) { float r; asm("v_max_f32_e32 %0, %1, %2" : "=v"(r) : "v"(a), "v"(b)); return r; }
#define TS2X(a)
#define STP_PARAM
#define STP_ARG
#define TS2(acc)
template <bool DOQK, bool DOPV>
DI void a_mphase(LAS const char* kp, LAS const char* vp, const bf16x8 (&q)[8], bf16x8 (&p)[4], f32x16 (&S)[2], f32x16 (&O)[4], const f32x16& negm, float& l, float& pm0 STP_PARAM) {
  constexpr int N = (DOQK ? 16 : 0) + (DOPV ? 16 : 0), D = 6;
  bf16x8 f[N];
  unsigned pw[8]; float ls = 0.f, e0 = 0.f;
#define A_ISQK(i) (DOQK && (!DOPV || (((i) >> 3) & 1) == 0))
#define A_IX(i) ((DOPV && DOQK) ? (((i) >> 4) * 8 + ((i) & 7)) : (i))
#define A_LD(i) do { if constexpr ((i) < N) { if constexpr (A_ISQK(i)) f[i] = ldsfrag(kp + A_IX(i) * 1024); \
                     else f[i] = ldsfrag(vp + ((A_IX(i) & 3) * 4 + (A_IX(i) >> 2)) * 1024); } } while (0)
#define A_MM(i) do { if constexpr (A_ISQK(i)) { if constexpr ((A_IX(i) & 7) == 0) mfma_init2(S[A_IX(i) >> 3], f[i], q[0], negm, f[(i) + 1]); \
                       else if constexpr ((i) & 1) mfma_acc(S[A_IX(i) >> 3], f[i], q[A_IX(i) & 7]); else mfma_acc2(S[A_IX(i) >> 3], f[i], q[A_IX(i) & 7], f[((i) + 1) % N]); } \
                     else if constexpr ((i) & 1) mfma_acc(O[A_IX(i) & 3], f[i], p[A_IX(i) >> 2]); else mfma_acc2(O[A_IX(i) & 3], f[i], p[A_IX(i) >> 2], f[((i) + 1) % N]); } while (0)
#define A_SM(e) do { const float x_ = __builtin_amdgcn_exp2f(S[1][(e) & 15]); ls += x_; \
                     if constexpr ((e) & 1) { pw[((e) & 15) >> 1] = cvtpk(e0, x_); asm volatile("" : "+v"(pw[((e) & 15) >> 1]), "+v"(ls)); } else { e0 = x_; asm volatile("" : "+v"(e0), "+v"(ls)); } \
                     if constexpr ((e) == 7) p[2] = asbf((u32x4){pw[0], pw[1], pw[2], pw[3]}); if constexpr ((e) == 15) p[3] = asbf((u32x4){pw[4], pw[5], pw[6], pw[7]}); } while (0)
#define A_MX(j) do { if constexpr ((j) == 0) pm0 = max2f(S[0][0], S[0][1]); else pm0 = max3f(pm0, S[0][2 * ((j) & 7)], S[0][2 * ((j) & 7) + 1]); asm volatile("" : "+v"(pm0)); } while (0)
#define A_STEP(i) do { if constexpr ((i) < N) { A_LD((i) + D); if constexpr ((i) == N - 4) { SBAR(); TS2(a); LBAR(); TS2(b); SBAR(); } A_MM(i); \
                     if constexpr (DOQK && DOPV && (i) < 16) A_SM(i); \
                     if constexpr (DOQK && DOPV && (i) >= 16 && (i) < 24) A_MX((i) - 16); \
                     if constexpr (!DOQK && (i) < 8) { A_SM(2 * (i)); A_SM(2 * (i) + 1); } SBAR(); } } while (0)
  SBAR();
  __builtin_amdgcn_s_setprio(1);
  A_LD(0); A_LD(1); A_LD(2); A_LD(3); A_LD(4); A_LD(5); SBAR();
  A_STEP(0); A_STEP(1); A_STEP(2); A_STEP(3); A_STEP(4); A_STEP(5); A_STEP(6); A_STEP(7);
  A_STEP(8); A_STEP(9); A_STEP(10); A_STEP(11); A_STEP(12); A_STEP(13); A_STEP(14); A_STEP(15);
  A_STEP(16); A_STEP(17); A_STEP(18); A_STEP(19); A_STEP(20); A_STEP(21); A_STEP(22); A_STEP(23);
  A_STEP(24); A_STEP(25); A_STEP(26); A_STEP(27); A_STEP(28); A_STEP(29); A_STEP(30); A_STEP(31);
  if constexpr (DOPV) l += ls;
  if constexpr (!DOPV) { NOP16(); SBAR(); A_MX(0); A_MX(1); A_MX(2); A_MX(3); A_MX(4); A_MX(5); A_MX(6); A_MX(7); }
  __builtin_amdgcn_s_setprio(0);
  TS2X(c);
#undef A_ISQK
#undef A_IX
#undef A_LD
#undef A_MM
#undef A_SM
#undef A_MX
#undef A_STEP
}

DI void a_finish(const f32x16& a0, const f32x16& a1, float ma, float la, LAS const char* xr, float mb, float lb, float gate, srd_t sC, unsigned voff, unsigned soff) {
  const float mt = fmaxf(ma, mb);
  const float fa = __builtin_amdgcn_exp2f(ma - mt), fb = __builtin_amdgcn_exp2f(mb - mt);
  const float sc = gate / (la * fa + lb * fb);
  const float wa = fa * sc, wb = fb * sc;
#pragma unroll
  for (int t = 0; t < 2; ++t) {
    const f32x16& a = t ? a1 : a0;
#pragma unroll
    for (int s = 0; s < 2; ++s) {
      const f32x4 b0 = *(LAS const f32x4*)(xr + t * 4096 + s * 2048), b1 = *(LAS const f32x4*)(xr + t * 4096 + s * 2048 + 1024);
      const u32x4 o = {cvtpk(a[8 * s + 0] * wa + b0[0] * wb, a[8 * s + 1] * wa + b0[1] * wb), cvtpk(a[8 * s + 2] * wa + b0[2] * wb, a[8 * s + 3] * wa + b0[3] * wb),
                       cvtpk(a[8 * s + 4] * wa + b1[0] * wb, a[8 * s + 5] * wa + b1[1] * wb), cvtpk(a[8 * s + 6] * wa + b1[2] * wb, a[8 * s + 7] * wa + b1[3] * wb)};
      st128(o, sC, voff + (unsigned)((2 * t + s) * 1024), soff);
      SBAR();
    }
  }
}
DI void a_send(const f32x16& a0, const f32x16& a1, LAS char* xr) {
#pragma unroll
  for (int g = 0; g < 4; ++g) {
    *(LAS f32x4*)(xr + g * 1024) = (f32x4){a0[4 * g], a0[4 * g + 1], a0[4 * g + 2], a0[4 * g + 3]};
    *(LAS f32x4*)(xr + 4096 + g * 1024) = (f32x4){a1[4 * g], a1[4 * g + 1], a1[4 * g + 2], a1[4 * g + 3]};
  }
}

__global__ __launch_bounds__(512, 2) void kF(const u16* __restrict__ x, const u16* __restrict__ ckv, const u16* __restrict__ wq_, const u16* __restrict__ wuk,
                                             const u16* __restrict__ wuv, const u16* __restrict__ wqkv, const u16* __restrict__ wphi, const u16* __restrict__ gatew,
                                             const u16* __restrict__ wo_a, const u16* __restrict__ wo_b, char* __restrict__ ws) {
  extern __shared__ __attribute__((aligned(16))) char lds[];
  LAS char* L = (LAS char*)lds; const unsigned lds0 = (unsigned)(uintptr_t)L;
  const int tid = threadIdx.x, lane = tid & 63, w = __builtin_amdgcn_readfirstlane(tid >> 6);
  const int b = blockIdx.x, h = (b & 7) * 4 + ((b >> 3) & 3), r = b >> 5;
  const int g = w >> 2, wq = w & 3, hh = lane >> 5, r32 = lane & 31;
  const int X = 15 - r, Y = r, nX = 16 - r;
  constexpr int NS = 17;
  const unsigned voff = (unsigned)lane * 16u;
  TS(t0);
  bf16x8 q[8];
  u64 tag; const u64* flg = (const u64*)(ws + WS_FLG + (size_t)h * 256);
  const srd_t sK = mksrd(ws + WS_KF + (size_t)h * 32 * 16384, 32u * 16384u), sV = mksrd(ws + WS_VF + (size_t)h * 32 * 16384, 32u * 16384u);
  const srd_t sW = mksrd(ws + WS_QF, (unsigned)(WS_DBG - WS_QF));
  {
    PCtx c;
    c.fl = (u32x2){0u, 0u}; c.wp = (u32x4){0u, 0u, 0u, 0u};
    c.L = L; c.w = w; c.lane = lane; c.h = h; c.voff = voff; c.flg = flg; c.sK = sK; c.sV = sV;
    c.soA1 = rfl((unsigned)(256 * r + 32 * w) * 1024u);
    c.soA2 = rfl((unsigned)(w < 4 ? 128 * X + 32 * w : 128 * Y + 32 * (w - 4)) * 1024u);
    c.soB = rfl((unsigned)(8 * w) * 8192u + (unsigned)h * 256u);
    c.soS = rfl((unsigned)(8 * w) * 3072u + (unsigned)h * 32u);
    c.soPW = rfl((unsigned)(64 * r + 8 * w) * 8192u + (unsigned)h * 256u); c.soPS = rfl((unsigned)(64 * r + 8 * w) * 3072u + (unsigned)h * 32u);
    c.soPX = rfl((unsigned)(256 * r + 8 * h + w) * 1024u);
    c.soKa = rfl((unsigned)(g * 32768 + wq * 4096)); c.so2 = rfl((unsigned)w * 2048u);
    c.mA = rfl(lds0 + (unsigned)w * 4096u); c.mB2 = rfl(lds0 + (unsigned)w * 3072u); c.m2 = rfl(lds0 + (unsigned)w * 2048u);
    c.sA1 = mksrd(ckv, 2048u * 1024u); c.sA2 = mksrd(x, 2048u * 1024u);
    c.sUK = mksrd(wuk, 512u * 8192u); c.sUV = mksrd(wuv, 512u * 8192u); c.sWQ = mksrd(wq_, 512u * 8192u); c.sWB = mksrd(wqkv, 512u * 3072u); c.sWP = mksrd(wphi, 32u * 512u);
#pragma unroll
    for (int k = 0; k < 2; ++k) c.voffA[k] = (unsigned)((lane >> 3) * 1024 + (((lane & 7) ^ ((4 * k + (lane >> 4)) & 7)) * 16));
    c.voffB = (unsigned)(((lane >> 2) & 7) * 8192 + (lane >> 5) * 64 + (lane & 3) * 16);
    { const int ch = lane & 3, sub = lane >> 5;
      c.voffS = (unsigned)(((lane >> 2) & 7) * 3072 + (sub == 0 ? (ch < 2 ? ch * 16 : 1024 + (ch - 2) * 16) : 2048 + (ch & 1) * 16)); }
    u32x2 ep;
    asm volatile("global_load_dwordx2 %0, %1, off sc1" : "=v"(ep) : "v"(flg + 16) : "memory");
#pragma unroll
    for (int k = 0; k < 8; ++k) asm volatile("global_load_ushort %0, %1, off offset:%2" : "=v"(c.gv[k]) : "v"(gatew + lane), "i"(k * 128) : "memory");
    p_pieceA<0, 0>(c); p_pieceA<0, 1>(c); p_pieceA<0, 2>(c); p_pieceA<0, 3>(c);
    p_pieceB<0, 0>(c); p_pieceB<0, 1>(c);
    p_pieceB<1, 0>(c); p_pieceB<1, 1>(c);
    p_pieceA<1, 0>(c); p_pieceA<1, 1>(c); p_pieceA<1, 2>(c); p_pieceA<1, 3>(c);
    p_warm<0>(c);
    wait_bar<9>();
    asm volatile("" : "+v"(ep), "+v"(c.gv[0]), "+v"(c.gv[1]), "+v"(c.gv[2]), "+v"(c.gv[3]), "+v"(c.gv[4]), "+v"(c.gv[5]), "+v"(c.gv[6]), "+v"(c.gv[7]));
    tag = (((u64)rfl(ep[1]) << 32) | rfl(ep[0])) + 1ull; c.tag = tag;
    {
      f32x16 acc[2][2];
      const int wm = w >> 1, wn = w & 1;
      p_pass<0, 2, 2, 4, true>(c, acc, 64u * wm, 2u * wn);
#pragma unroll
      for (int i = 0; i < 4; ++i) { c.kvp[2 * i] = pack8<0>(acc[i >> 1][i & 1]); c.kvp[2 * i + 1] = pack8<1>(acc[i >> 1][i & 1]); }
      c.kvo = voff + (unsigned)((4 * r + wm) * 16384 + 4 * wn * 1024);
      p_pass<1, 2, 2, 4, false>(c, acc, 64u * wm, 2u * wn);
#pragma unroll
      for (int i = 0; i < 4; ++i) { c.kvp[2 * i] = pack8<0>(acc[i >> 1][i & 1]); c.kvp[2 * i + 1] = pack8<1>(acc[i >> 1][i & 1]); }
      c.kvo = voff + (unsigned)((4 * r + wm) * 16384 + 8 * wn * 1024);
    }
    TS(t1);
    f32x16 acc[2][3];
    p_pass<2, 2, 3, 6, true>(c, acc, 64u * wq, 3u * g);
    TS(t2);
    if (wq < 2) {
#pragma unroll
      for (int mt = 0; mt < 2; ++mt)
#pragma unroll
        for (int nt = 0; nt < 3; ++nt)
          if (g == 0 || nt == 0) {
            const f32x16 v = acc[mt][nt] * QSCALE; LAS char* qd = L + 131072 + ((2 * wq + mt) * 8 + 2 * (3 * g + nt)) * 1024 + lane * 16;
            *(LAS u32x4*)qd = pack8<0>(v); *(LAS u32x4*)(qd + 1024) = pack8<1>(v);
          }
    }
    wait_bar<0>();
#pragma unroll
    for (int ks = 0; ks < 8; ++ks) q[ks] = ldsfrag(L + 131072 + wq * 8192 + ks * 1024 + lane * 16);
    if (wq >= 2) {
#pragma unroll
      for (int mt = 0; mt < 2; ++mt)
#pragma unroll
        for (int nt = 0; nt < 3; ++nt)
          if (g == 0 || nt == 0) {
            const f32x16 v = acc[mt][nt] * QSCALE; const unsigned so = (unsigned)(b * 32768 + ((2 * (wq - 2) + mt) * 8 + 2 * (3 * g + nt)) * 1024);
            st128(pack8<0>(v), sW, voff, so); st128(pack8<1>(v), sW, voff + 1024u, so);
          }
    }
    if (g == 1) {
      LAS char* Ik = L + 16384 + wq * 4096; LAS char* Iv = Ik + 2048;
      const int i16 = lane & 15;
      const unsigned tb = (unsigned)((8 * hh + (i16 >> 2)) * 64 + ((lane >> 4) & 1) * 32 + (i16 & 3) * 8);
      f32x16 kv = {};
#pragma unroll
      for (int mt = 0; mt < 2; ++mt) {
        f32x16 pq = MFMA(asbf(c.wp), asbf(pack8<0>(acc[mt][1])), (f32x16){});
        f32x16 pk = MFMA(asbf(c.wp), asbf(pack8<1>(acc[mt][1])), (f32x16){});
        softmax2(pq); softmax2(pk);
        const unsigned so = (unsigned)(WS_PQ - WS_QF) + (unsigned)(b * 16384 + (2 * wq + mt) * 2048);
        st128(pack8<0>(pq), sW, voff, so); st128(pack8<1>(pq), sW, voff + 1024u, so);
#pragma unroll
        for (int k = 0; k < 4; ++k) *(LAS u32x2*)(Ik + r32 * 64 + 16 * k + 8 * hh) = (u32x2){cvtpk(pk[4 * k], pk[4 * k + 1]), cvtpk(pk[4 * k + 2], pk[4 * k + 3])};
        const f32x16& tv = acc[mt][2];
        *(LAS u32x2*)(Iv + r32 * 64 + 8 * hh) = (u32x2){cvtpk(tv[0], tv[1]), cvtpk(tv[2], tv[3])};
        *(LAS u32x2*)(Iv + r32 * 64 + 16 + 8 * hh) = (u32x2){cvtpk(tv[4], tv[5]), cvtpk(tv[6], tv[7])};
        *(LAS u32x2*)(Iv + r32 * 64 + 32 + 8 * hh) = (u32x2){hh == 0 ? 0x00003f80u : 0u, 0u};
        *(LAS u32x2*)(Iv + r32 * 64 + 48 + 8 * hh) = (u32x2){0u, 0u};
        const float gt = 1.0f / (1.0f + __expf(-swap32_lo(tv[8])));
        __builtin_amdgcn_raw_buffer_store_b32(__float_as_uint(gt), sW, (unsigned)r32 * 4u, (unsigned)(WS_GW - WS_QF) + (unsigned)(b * 1024 + (64 * wq + 32 * mt) * 4), 0);
        LWAIT();
#pragma unroll
        for (int ks = 0; ks < 2; ++ks) kv = MFMA(trfrag(Ik + tb + ks * 1024, Ik + tb + ks * 1024 + 256), trfrag(Iv + tb + ks * 1024, Iv + tb + ks * 1024 + 256), kv);
        LWAIT();
      }
#pragma unroll
      for (int k = 0; k < 4; ++k) *(LAS f32x4*)(Ik + k * 1024 + lane * 16) = (f32x4){kv[4 * k], kv[4 * k + 1], kv[4 * k + 2], kv[4 * k + 3]};
      LBAR();
      f32x4 s4 = *(LAS const f32x4*)(L + 16384 + wq * 1024 + lane * 16);
#pragma unroll
      for (int k = 1; k < 4; ++k) s4 += *(LAS const f32x4*)(L + 16384 + k * 4096 + wq * 1024 + lane * 16);
      st128_wt(__builtin_bit_cast(u32x4, s4), sW, voff, (unsigned)(WS_KVP - WS_QF) + (unsigned)((h * 8 + r) * 4096 + wq * 1024));
    }
  }
  TS(t3);
  const unsigned ringK = rfl(lds0 + (g ? 98304u : 32768u) + (unsigned)wq * 4096u), ringV = rfl(lds0 + (g ? 0u : 65536u) + (unsigned)wq * 4096u);
  f32x16 O[4], S[2], keep0 = {}, keep1 = {}, negm = {};
#pragma unroll
  for (int d = 0; d < 4; ++d) O[d] = (f32x16){};
  bf16x8 p[4];
  float m = 0.f, l = 0.f, mk = 0.f, lk = 0.f, pm0 = 0.f;
  float gate[2] = {0.f, 0.f}; f32x2 ps = {0.f, 0.f};
  u32x2 fl2 = {0u, 0u};
  u32x4 warm_dummy; unsigned warm_dm1;
  LAS const char* kbase = L + (g ? 98304 : 32768) + lane * 16;
  LAS const char* vbase = L + (g ? 0 : 65536) + lane * 16;
  LAS char* xr = L + 131072 + wq * 8192 + lane * 16;
#define TILE(s) ((s) < nX ? 2 * (s) + g : 2 * ((s) - nX) + g)
#define CF_SO(blkrow, half) ((unsigned)(WS_CF - WS_QF) + (unsigned)(((4 * (blkrow) + wq) * 288 + 8 * h + 4 * (half)) * 1024))
#define ST_SO(wv) ((unsigned)(WS_ST - WS_QF) + (unsigned)(b * 2048 + (wv) * 256))
#define ST_LOAD(wv) asm volatile("buffer_load_dwordx2 %0, %1, %2, %3 offen" : "+v"(ps) : "v"((unsigned)r32 * 8u), "s"(sW), "s"(rfl(ST_SO(wv))) : "memory")
#define L_SOFTMAX(DIAG, FIRST, D0, D1, D2, D3, D4, D5, D6, D7) do { \
    if (DIAG) { int rel = 32 * wq + r32 - 64 * g - 4 * hh; \
      _Pragma("unroll") for (int e = 0; e < 16; ++e) { const int cc = (e & 3) + 8 * (e >> 2); if ((e & 3) == 0) { asm volatile("" : "+v"(rel)); SBAR(); } \
        if (cc > rel) S[0][e] = -__builtin_inff(); if (cc + 32 > rel) S[1][e] = -__builtin_inff(); } \
      pm0 = max2f(S[0][0], S[0][1]); _Pragma("unroll") for (int e = 2; e < 16; e += 2) pm0 = max3f(pm0, S[0][e], S[0][e + 1]); } \
    float pm = max3f(pm0, S[1][0], S[1][1]), pn = max3f(S[1][2], S[1][3], S[1][4]); \
    pm = max3f(pm, S[1][5], S[1][6]); pn = max3f(pn, S[1][7], S[1][8]); pm = max3f(pm, S[1][9], S[1][10]); pn = max3f(pn, S[1][11], S[1][12]); \
    pm = max3f(pm, S[1][13], S[1][14]); pm = max3f(pm, pn, S[1][15]); \
    { auto rr = __builtin_amdgcn_permlane32_swap(__float_as_uint(pm), __float_as_uint(pm), false, false); pm = max2f(__uint_as_float(rr[0]), __uint_as_float(rr[1])); } \
    SBAR(); TS2X(f); D0; SBAR(); \
    const bool first_ = (FIRST); \
    if (first_ || __builtin_expect(__any(pm > THR), 0)) { \
      NOP16(); SBAR(); \
      const float dl = fmaxf(pm, first_ ? -1e30f : 0.f); \
      m += dl; \
      if (!first_) { const float f = __builtin_amdgcn_exp2f(-dl); l *= f; _Pragma("unroll") for (int d = 0; d < 4; ++d) O[d] = O[d] * f; } \
      _Pragma("unroll") for (int e = 0; e < 16; ++e) { S[0][e] -= dl; S[1][e] -= dl; negm[e] = -m; } \
    } \
    TS2X(g); float ls0 = l, ls1 = 0.f; unsigned pw[8]; \
    SM_GRP(0, D1); SM_GRP(2, D2); SM_GRP(4, D3); SM_GRP(6, D4); SM_GRP(8, D5); SM_GRP(10, D6); SM_GRP(12, D7); SM_GRP(14, (void)0); \
    l = ls0 + ls1; \
    p[0] = asbf((u32x4){pw[0], pw[1], pw[2], pw[3]}); p[1] = asbf((u32x4){pw[4], pw[5], pw[6], pw[7]}); } while (0)
#define SM_GRP(e0, DMA) do { const float x0 = __builtin_amdgcn_exp2f(S[0][e0]), x1 = __builtin_amdgcn_exp2f(S[0][(e0) + 1]); \
      ls0 += x0; ls1 += x1; pw[(e0) >> 1] = cvtpk(x0, x1); asm volatile("" : "+v"(pw[(e0) >> 1]), "+v"(ls0), "+v"(ls1)); DMA; SBAR(); } while (0)
#define L_SUM() do { } while (0)
  float mo = 0.f, lo = 0.f;
  auto l_pre = [&](int s) {
    if (s == nX) {
      NOP16(); SBAR();
      mo = m; lo = swap32_add(l);
      if (g == 1) ST_LOAD(w - 4);
      __builtin_amdgcn_raw_buffer_store_b64(__builtin_bit_cast(u32x2, (f32x2){mo, lo}), sW, (unsigned)r32 * 8u, ST_SO(w), 0);
      if (g == 0) { a_send(O[2], O[3], xr); keep0 = O[0]; keep1 = O[1]; mk = mo; lk = lo; }
      m = 0.f; l = 0.f;
    }
    if (g == 0 && s == nX + 1) ST_LOAD(w + 4);
  };
  auto l_post = [&](int s) {
    if (s == nX) {
      if (g == 1) {
        wait_vm<8>(); asm volatile("" : "+v"(ps));
        a_finish(O[2], O[3], mo, lo, xr, ps[0], ps[1], gate[0], sW, voff, CF_SO(X, 1));
        LWAIT();
        a_send(O[0], O[1], xr);
      }
#pragma unroll
      for (int d = 0; d < 4; ++d) O[d] = (f32x16){};
    }
    if (g == 0 && s == nX + 1) {
      wait_vm<8>(); asm volatile("" : "+v"(ps));
      a_finish(keep0, keep1, mk, lk, xr, ps[0], ps[1], gate[0], sW, voff, CF_SO(X, 0));
    }
  };
  unsigned mK = ringK, mV = ringV + 16384u, oK = (unsigned)(4 + g) * 16384u + (unsigned)wq * 4096u, oV = (unsigned)(2 + g) * 16384u + (unsigned)wq * 4096u;
  LAS const char* kp = kbase + 16384; LAS const char* vp = vbase;
#define A_TAIL() do { \
    asm volatile("" : "+v"(p[0]), "+v"(p[1]), "+v"(l), "+v"(S[1])); \
    SBAR(); TS2(d); wait_bar<8>(); TS2(e); SBAR(); \
    a_mphase<true, true>(kp, vp, q, p, S, O, negm, l, pm0 STP_ARG); \
    mK ^= 16384u; mV ^= 16384u; kp = (LAS const char*)((unsigned)(uintptr_t)kp ^ 16384u); vp = (LAS const char*)((unsigned)(uintptr_t)vp ^ 16384u); } while (0)
#define A_FAST(FIRST) do { \
    if (!(FIRST)) L_SUM(); \
    L_SOFTMAX(false, FIRST, dma16o<0>(mK, voff, sK, oK), dma16n<1024>(voff, sK, oK), dma16n<2048>(voff, sK, oK), dma16n<3072>(voff, sK, oK), \
              dma16o<0>(mV, voff, sV, oV), dma16n<1024>(voff, sV, oV), dma16n<2048>(voff, sV, oV), dma16n<3072>(voff, sV, oV)); \
    oK += 32768u; oV += 32768u; TS2X(h); A_TAIL(); } while (0)
#define A_SLOW(s) do { \
    L_SUM(); l_pre(s); \
    oK = (s) < 15 ? (unsigned)TILE((s) + 2) * 16384u + (unsigned)wq * 4096u : (unsigned)(WS_KVP - WS_QF) + (unsigned)(h * 32768 + w * 4096); \
    oV = (unsigned)TILE((s) + 1) * 16384u + (unsigned)wq * 4096u; \
    const srd_t sKx = (s) < 15 ? sK : sW; \
    L_SOFTMAX((s) == nX - 1, (s) == nX, dma16o<0>(mK, voff, sKx, oK), dma16n<1024>(voff, sKx, oK), dma16n<2048>(voff, sKx, oK), dma16n<3072>(voff, sKx, oK), \
              dma16o<0>(mV, voff, sV, oV), dma16n<1024>(voff, sV, oV), dma16n<2048>(voff, sV, oV), dma16n<3072>(voff, sV, oV)); \
    oK += 32768u; oV += 32768u; TS2X(h); \
    l_post(s); \
    if ((s) == nX - 1) { _Pragma("unroll") for (int ks = 0; ks < 8; ++ks) q[ks] = ldsfrag(L + 131072 + wq * 8192 + ks * 1024 + lane * 16); negm = (f32x16){}; } \
    A_TAIL(); } while (0)
  a_mphase<true, false>(kbase, vbase, q, p, S, O, negm, l, pm0 STP_ARG);
  A_FAST(true);
  if (w == 0 && lane == 0) __hip_atomic_store((u64*)flg + 8 + r, tag, __ATOMIC_RELAXED, __HIP_MEMORY_SCOPE_AGENT);
  A_FAST(false);
  {
    const unsigned mq = rfl(lds0 + 131072u + (unsigned)w * 4096u), oq = rfl((unsigned)(b * 32768 + w * 4096));
    dma16o<0>(mq, voff, sW, oq); dma16n<1024>(voff, sW, oq); dma16n<2048>(voff, sW, oq); dma16n<3072>(voff, sW, oq);
    const unsigned go = rfl((unsigned)(WS_GW - WS_QF) + (unsigned)(b * 1024 + wq * 128));
    asm volatile("buffer_load_dword %0, %2, %3, %4 offen\n\tbuffer_load_dword %1, %2, %3, %4 offen offset:512" : "+v"(gate[0]), "+v"(gate[1]) : "v"((unsigned)r32 * 4u), "s"(sW), "s"(go) : "memory");
    poll_issue(fl2, flg + 8, lane);
    const srd_t sWa = mksrd(wo_a, 4096u * 1024u), sWb = mksrd(wo_b, 512u * 1024u);
    u32x4 dmy; unsigned dm1;
    asm volatile("buffer_load_dwordx4 %0, %2, %3, 0 offen\n\tbuffer_load_dwordx4 %0, %2, %3, 0 offen offset:1024\n\t"
                 "buffer_load_dwordx4 %0, %4, %5, 0 offen\n\tbuffer_load_dword %1, %6, %3, 0 offen"
                 : "=&v"(dmy), "=&v"(dm1) : "v"(voff + (unsigned)(b * 16384 + w * 2048)), "s"(sWa), "v"((unsigned)(b * 2048 + w * 256) + (voff & 255u)), "s"(sWb),
                   "v"((unsigned)((((b >> 3) * 8 + w) * 4 + (lane & 3)) * 4096 + (lane >> 2) * 0)) : "memory");
    warm_dummy = dmy; warm_dm1 = dm1;
  }
  A_FAST(false);
  asm volatile("" : "+v"(gate[0]), "+v"(gate[1]), "+v"(fl2));
  asm volatile("" :: "v"(warm_dummy), "v"(warm_dm1));
  if (w == 0) { poll_check(fl2, flg + 8, tag, lane); if (r == 0 && lane == 0) __hip_atomic_store((u64*)flg + 16, tag, __ATOMIC_RELAXED, __HIP_MEMORY_SCOPE_AGENT); }
  {
    int s = 3;
#pragma unroll 1
    for (int blk = 0; blk < 2; ++blk) {
      const int e1 = blk ? 15 : nX - 2, e2 = blk ? 16 : (nX + 2 < 16 ? nX + 2 : 16);
#pragma unroll 1
      for (; s < e1; ++s) A_FAST(false);
#pragma unroll 1
      for (; s < e2; ++s) A_SLOW(s);
    }
  }
  TS(t4);
  {
    L_SUM(); l_pre(16);
    const unsigned mp = rfl(lds0 + (g ? 98304u : 32768u) + (unsigned)wq * 2048u), op = rfl((unsigned)(WS_PQ - WS_QF) + (unsigned)(b * 16384 + w * 2048));
    L_SOFTMAX(true, nX == 16, dma16o<0>(mp, voff, sW, op), dma16n<1024>(voff, sW, op), (void)0, (void)0, (void)0, (void)0, (void)0, (void)0);
    if (16 == nX || 16 == nX + 1) wait_vm<0>();
    l_post(16);
    asm volatile("" : "+v"(p[0]), "+v"(p[1]), "+v"(l), "+v"(S[1]));
    SBAR(); wait_bar<2>(); SBAR();
  }
  a_mphase<false, true>(kbase, vbase, q, p, S, O, negm, l, pm0 STP_ARG);
  NOP16(); SBAR();
  L_SUM();
  if (g == 0 && nX == 16) {
    ST_LOAD(w + 4); wait_vm<0>(); asm volatile("" : "+v"(ps));
    a_finish(keep0, keep1, mk, lk, xr, ps[0], ps[1], gate[0], sW, voff, CF_SO(X, 0));
    LWAIT();
  }
  LAS f32x2* stl = (LAS f32x2*)(L + 81920) + r32;
  auto stage2 = [&]() {
    f32x16 kv = {};
#pragma unroll
    for (int k = 0; k < 8; ++k)
#pragma unroll
      for (int qd = 0; qd < 4; ++qd) { const f32x4 v = *(LAS const f32x4*)(L + (k < 4 ? 49152 : 114688) + (k & 3) * 4096 + qd * 1024 + lane * 16);
#pragma unroll
        for (int e = 0; e < 4; ++e) kv[4 * qd + e] += v[e]; }
    const bf16x8 p0 = ldsfrag(kbase + wq * 2048), p1 = ldsfrag(kbase + wq * 2048 + 1024);
    f32x16 o = MFMA(asbf(pack8<0>(kv)), p0, (f32x16){});
    o = MFMA(asbf(pack8<1>(kv)), p1, o);
    const float sc = (1.0f - (g ? gate[1] : gate[0])) / (swap32_lo(o[8]) + 1e-6f);
    o = o * sc;
    st128(pack8<0>(o), sW, voff, (unsigned)(WS_CF - WS_QF) + (unsigned)(((4 * (g ? Y : X) + wq) * 288 + 256 + h) * 1024));
  };
  const float lt = swap32_add(l);
  if (g == 0) {
    a_send(O[2], O[3], xr);
    stl[w * 32] = (f32x2){m, lt};
    wait_bar<0>();
    stage2();
    LBAR();
    const f32x2 po = stl[(w + 4) * 32];
    a_finish(O[0], O[1], m, lt, xr, po[0], po[1], gate[1], sW, voff, CF_SO(Y, 0));
  } else {
    const f32x2 po = stl[(w - 4) * 32];
    a_finish(O[2], O[3], m, lt, xr, po[0], po[1], gate[1], sW, voff, CF_SO(Y, 1));
    LWAIT();
    a_send(O[0], O[1], xr);
    stl[w * 32] = (f32x2){m, lt};
    wait_bar<4>();
    stage2();
  }
#undef TILE
#undef CF_SO
#undef ST_SO
#undef ST_LOAD
#undef L_SOFTMAX
#undef SM_GRP
#undef L_SUM
#undef A_TAIL
#undef A_FAST
#undef A_SLOW
}

#define OD 8
constexpr int O_D = OD;
struct OCtx {
  srd_t sC, sWa, sWb; unsigned voff, voffB, soA, soW, mW, bB;
  LAS char* L; bf16x8 a[O_D][2]; f32x16 acc[2][2];
};
template <int i> DI void o_issue(OCtx& c) {
  if constexpr (i < 36) {
    constexpr unsigned sl = (unsigned)(i % O_D) * 2048u;
    if constexpr (i < 32) { dma16c<sl, i * 131072u>(c.mW, c.voffB, c.sWa, c.soW); dma16c<sl + 1024u, i * 131072u + 8192u>(c.mW, c.voffB, c.sWa, c.soW); }
    else                  { dma16c<sl, (i - 32) * 131072u>(c.mW, c.voffB, c.sWb, c.soW); dma16c<sl + 1024u, (i - 32) * 131072u + 8192u>(c.mW, c.voffB, c.sWb, c.soW); }
    unsigned t;
    asm volatile("s_add_u32 %2, %5, %6\n\tbuffer_load_dwordx4 %0, %3, %4, %2 offen\n\ts_add_u32 %2, %5, %7\n\tbuffer_load_dwordx4 %1, %3, %4, %2 offen"
                 : "=&v"(c.a[i % O_D][0]), "=&v"(c.a[i % O_D][1]), "=&s"(t) : "v"(c.voff), "s"(c.sC), "s"(c.soA), "i"(i * 8192u), "i"(i * 8192u + 294912u) : "memory", "scc");
  }
}
template <int i> DI void o_step(OCtx& c) {
  constexpr int NY = (35 - i < O_D - 1 ? 35 - i : O_D - 1) * 4;
  asm volatile("s_waitcnt vmcnt(%2)" : "+v"(c.a[i % O_D][0]), "+v"(c.a[i % O_D][1]) : "i"(NY) : "memory");
  LAS const char* sp = c.L + c.bB + (i % O_D) * 2048;
  const bf16x8 f0 = trfrag(sp, sp + 1024), f1 = trfrag(sp + 512, sp + 1536);
  const bf16x8 a0 = c.a[i % O_D][0], a1 = c.a[i % O_D][1];
  c.acc[0][0] = MFMA(f0, a0, c.acc[0][0]); c.acc[1][0] = MFMA(f0, a1, c.acc[1][0]);
  c.acc[0][1] = MFMA(f1, a0, c.acc[0][1]); c.acc[1][1] = MFMA(f1, a1, c.acc[1][1]);
  SBAR();
  o_issue<i + O_D>(c);
  SBAR();
}
template <int i> DI void o_prologue(OCtx& c) { if constexpr (i < O_D) { o_issue<i>(c); o_prologue<i + 1>(c); } }
template <int i> DI void o_steps(OCtx& c) { if constexpr (i < 36) { o_step<i>(c); o_steps<i + 1>(c); } }
__global__ __launch_bounds__(512, 2) void kO(const u16* __restrict__ wo_a, const u16* __restrict__ wo_b, char* __restrict__ ws, u16* __restrict__ out) {
  extern __shared__ __attribute__((aligned(16))) char lds[];
  LAS char* L = (LAS char*)lds; const unsigned lds0 = (unsigned)(uintptr_t)L;
  const int tid = threadIdx.x, lane = tid & 63, w = __builtin_amdgcn_readfirstlane(tid >> 6);
  const int b = blockIdx.x, idx = b >> 3, mi = (b & 7) * 4 + (idx & 3), ni = idx >> 2;
  const int hh = lane >> 5, r32 = lane & 31, i16 = lane & 15, p4 = i16 & 3;
  TS(z0);
  OCtx c;
  c.L = L;
  c.sC = mksrd(ws + WS_CF, 64u * 288u * 1024u); c.sWa = mksrd(wo_a, 4096u * 1024u); c.sWb = mksrd(wo_b, 512u * 1024u);
  c.voff = (unsigned)lane * 16u;
  c.voffB = (unsigned)(((lane >> 2) & 7) * 1024 + (lane >> 5) * 64 + (lane & 3) * 16);
  c.soA = rfl((unsigned)(2 * mi * 288 + w) * 1024u);
  c.soW = rfl((unsigned)(16 * w) * 1024u + (unsigned)ni * 128u);
  c.mW = rfl(lds0 + (unsigned)(w * O_D) * 2048u);
  c.bB = (unsigned)(w * O_D * 2048 + (4 * hh + (i16 >> 2)) * 64 + ((lane >> 4) & 1) * 32 + (((p4 & 1) << 1) | (p4 >> 1)) * 8);
  o_prologue<0>(c);
#pragma unroll
  for (int mt = 0; mt < 2; ++mt)
#pragma unroll
    for (int nt = 0; nt < 2; ++nt) c.acc[mt][nt] = (f32x16){};
  o_steps<0>(c);
  TS(z1);
  LBAR();
#pragma unroll
  for (int t = 0; t < 4; ++t)
#pragma unroll
    for (int qd = 0; qd < 4; ++qd) { const f32x16& v = c.acc[t >> 1][t & 1];
      *(LAS f32x4*)(L + w * 16384 + (t * 4 + qd) * 1024 + lane * 16) = (f32x4){v[4 * qd], v[4 * qd + 1], v[4 * qd + 2], v[4 * qd + 3]}; }
  LBAR();
  f32x4 s0 = {0.f, 0.f, 0.f, 0.f}, s1 = s0;
#pragma unroll
  for (int k = 0; k < 8; ++k) {
    s0 += *(LAS const f32x4*)(L + k * 16384 + w * 2048 + lane * 16);
    s1 += *(LAS const f32x4*)(L + k * 16384 + w * 2048 + 1024 + lane * 16);
  }
  const int t = w >> 1, h8 = w & 1;
  u16* op = out + (size_t)(64 * mi + 32 * (t >> 1) + r32) * 512 + 64 * ni + 32 * (t & 1) + 16 * h8 + 8 * hh;
  *(u32x4*)op = (u32x4){cvtpk(s0[0], s0[1]), cvtpk(s0[2], s0[3]), cvtpk(s1[0], s1[1]), cvtpk(s1[2], s1[3])};
}

extern "C" void kernel_launch(void* const* d_in, const int* in_sizes, int n_in, void* d_out, int out_size, void* d_ws, size_t ws_size, hipStream_t stream) {
  const u16* x = (const u16*)d_in[0]; const u16* ckv = (const u16*)d_in[1]; const u16* wq = (const u16*)d_in[2];
  const u16* wuk = (const u16*)d_in[3]; const u16* wuv = (const u16*)d_in[4]; const u16* wo_a = (const u16*)d_in[5];
  const u16* wqkv = (const u16*)d_in[7]; const u16* wphi = (const u16*)d_in[8]; const u16* wo_b = (const u16*)d_in[9]; const u16* gatew = (const u16*)d_in[10];
  (void)hipFuncSetAttribute((const void*)kF, hipFuncAttributeMaxDynamicSharedMemorySize, 163840);
  (void)hipFuncSetAttribute((const void*)kO, hipFuncAttributeMaxDynamicSharedMemorySize, 163840);
  kF<<<256, 512, 163840, stream>>>(x, ckv, wq, wuk, wuv, wqkv, wphi, gatew, wo_a, wo_b, (char*)d_ws);
  kO<<<256, 512, O_D > 8 ? O_D * 16384 : 131072, stream>>>(wo_a, wo_b, (char*)d_ws, (u16*)d_out);
}
```

```cpp
#define PRIO_M
#include <hip/hip_runtime.h>
#include <stdint.h>

typedef unsigned short u16;
typedef unsigned long long u64;
typedef __bf16 bf16x8 __attribute__((ext_vector_type(8)));
typedef __bf16 bf16x2 __attribute__((ext_vector_type(2)));
typedef float f32x2 __attribute__((ext_vector_type(2)));
typedef float f32x4 __attribute__((ext_vector_type(4)));
typedef float f32x16 __attribute__((ext_vector_type(16)));
typedef unsigned u32x2 __attribute__((ext_vector_type(2)));
typedef unsigned u32x4 __attribute__((ext_vector_type(4)));
typedef short s16x4 __attribute__((ext_vector_type(4)));
typedef short s16x8 __attribute__((ext_vector_type(8)));
typedef __amdgpu_buffer_rsrc_t srd_t;
#define LAS __attribute__((address_space(3)))
#define DI __device__ __forceinline__
#define SBAR() __builtin_amdgcn_sched_barrier(0)
#define MFMA(a, b, c) __builtin_amdgcn_mfma_f32_32x32x16_bf16((a), (b), (c), 0, 0, 0)

constexpr size_t MB = 1024 * 1024;
constexpr size_t WS_KF = 0;
constexpr size_t WS_VF = 16 * MB;
constexpr size_t WS_QF = 32 * MB;
constexpr size_t WS_CF = 48 * MB;
constexpr size_t WS_PQ = 68 * MB;
constexpr size_t WS_KVP = 72 * MB;
constexpr size_t WS_GW = 73 * MB;
constexpr size_t WS_ST = 74 * MB;
constexpr size_t WS_SLAB = 104 * MB;
constexpr size_t WS_CNT = 92 * MB;
constexpr size_t WS_FLG = 93 * MB;
constexpr size_t WS_DBG = 100 * MB;

constexpr float QSCALE = 0.08838834764831845f * 1.4426950408889634f;
constexpr float THR = 8.0f;

DI unsigned cvtpk(float lo, float hi) { f32x2 v = {lo, hi}; bf16x2 b = __builtin_convertvector(v, bf16x2); return __builtin_bit_cast(unsigned, b); }
template <int S> DI u32x4 pack8(const f32x16& x) {
  return (u32x4){cvtpk(x[8 * S + 0], x[8 * S + 1]), cvtpk(x[8 * S + 2], x[8 * S + 3]), cvtpk(x[8 * S + 4], x[8 * S + 5]), cvtpk(x[8 * S + 6], x[8 * S + 7])};
}
DI bf16x8 asbf(u32x4 v) { return __builtin_bit_cast(bf16x8, v); }
DI float swap32_max(float v) { auto rr = __builtin_amdgcn_permlane32_swap(__float_as_uint(v), __float_as_uint(v), false, false); return fmaxf(__uint_as_float(rr[0]), __uint_as_float(rr[1])); }
DI float swap32_add(float v) { auto rr = __builtin_amdgcn_permlane32_swap(__float_as_uint(v), __float_as_uint(v), false, false); return __uint_as_float(rr[0]) + __uint_as_float(rr[1]); }
DI float swap32_lo(float v) { auto rr = __builtin_amdgcn_permlane32_swap(__float_as_uint(v), __float_as_uint(v), false, false); return __uint_as_float(rr[0]); }
DI float bf16round(float x) { return __uint_as_float(cvtpk(x, 0.f) << 16); }
DI srd_t mksrd(const void* p, unsigned bytes) { return __builtin_amdgcn_make_buffer_rsrc((void*)p, (short)0, (int)bytes, 0x00020000); }
DI unsigned rfl(unsigned x) { return (unsigned)__builtin_amdgcn_readfirstlane((int)x); }
template <int OFF> DI void dma16o(unsigned m0v, unsigned voff, srd_t srd, unsigned soff) {
  asm volatile("s_mov_b32 m0, %0\n\ts_nop 0\n\tbuffer_load_dwordx4 %1, %2, %3 offen offset:%4 sc1 lds" :: "s"(m0v), "v"(voff), "s"(srd), "s"(soff), "i"(OFF) : "memory");
}
template <unsigned MOFF, unsigned SOFF> DI void dma16s(unsigned mb, unsigned voff, srd_t srd, unsigned sb) {
  unsigned t;
  asm volatile("s_add_u32 m0, %1, %5\n\ts_add_u32 %0, %4, %6\n\tbuffer_load_dwordx4 %2, %3, %0 offen sc1 lds" : "=&s"(t) : "s"(mb), "v"(voff), "s"(srd), "s"(sb), "i"(MOFF), "i"(SOFF) : "memory", "scc");
}
template <int OFF> DI void dma16n(unsigned voff, srd_t srd, unsigned soff) {
  asm volatile("buffer_load_dwordx4 %0, %1, %2 offen offset:%3 sc1 lds" :: "v"(voff), "s"(srd), "s"(soff), "i"(OFF) : "memory");
}
template <unsigned MOFF, unsigned SOFF> DI void dma16c(unsigned mb, unsigned voff, srd_t srd, unsigned sb) {
  unsigned t;
  asm volatile("s_add_u32 m0, %1, %5\n\ts_add_u32 %0, %4, %6\n\tbuffer_load_dwordx4 %2, %3, %0 offen lds" : "=&s"(t) : "s"(mb), "v"(voff), "s"(srd), "s"(sb), "i"(MOFF), "i"(SOFF) : "memory", "scc");
}
template <unsigned MOFF, unsigned SOFF> DI void dma16m(unsigned mb, unsigned voff, srd_t srd, unsigned sb) {
  unsigned t;
  asm volatile("s_add_u32 m0, %1, %5\n\ts_add_u32 %0, %4, %6\n\ts_mov_b32 exec_hi, 0x33333333\n\tbuffer_load_dwordx4 %2, %3, %0 offen lds\n\ts_mov_b32 exec_hi, -1"
               : "=&s"(t) : "s"(mb), "v"(voff), "s"(srd), "s"(sb), "i"(MOFF), "i"(SOFF) : "memory", "scc");
}
template <int N> DI void wait_bar() { asm volatile("s_waitcnt vmcnt(%0) lgkmcnt(0)\n\ts_barrier" :: "i"(N) : "memory"); }
template <int N> DI void wait_vm() { asm volatile("s_waitcnt vmcnt(%0)" :: "i"(N) : "memory"); }
#define LBAR() asm volatile("s_waitcnt lgkmcnt(0)\n\ts_barrier" ::: "memory")
#define LWAIT() asm volatile("s_waitcnt lgkmcnt(0)" ::: "memory")
#define NOP16() asm volatile("s_nop 7\n\ts_nop 7" ::: "memory")
DI bf16x8 ldsfrag(LAS const char* p) { return *(LAS const bf16x8*)p; }
DI bf16x8 trfrag(LAS const char* p0, LAS const char* p1) {
  s16x4 a = __builtin_amdgcn_ds_read_tr16_b64_v4i16((LAS s16x4*)p0);
  s16x4 b = __builtin_amdgcn_ds_read_tr16_b64_v4i16((LAS s16x4*)p1);
  return __builtin_bit_cast(bf16x8, __builtin_shufflevector(a, b, 0, 1, 2, 3, 4, 5, 6, 7));
}
DI void st128(u32x4 v, srd_t s, unsigned vo, unsigned so) { __builtin_amdgcn_raw_buffer_store_b128(v, s, vo + so, 0, 0); }
DI void st128_wt(u32x4 v, srd_t s, unsigned vo, unsigned so) { __builtin_amdgcn_raw_buffer_store_b128(v, s, vo + so, 0, 16); }
#define TS(x)

#define KVAUX 16
struct PCtx {
  srd_t sA1, sA2, sUK, sUV, sWQ, sWB, sWP, sK, sV;
  unsigned voffA[2], voffB, voffS, voff;
  unsigned soA1, soA2, soB, soS, soKa, mK0, mA, mB2, m2;
  unsigned gv[8];
  u32x2 fl; u64 tag; const u64* flg;
  u32x4 wp;
  unsigned soPW, soPS, soPX;
  u32x4 kvp[8]; unsigned kvo;
  int w, lane, h;
  LAS char* L;
};
#define TS3(x) do { SBAR(); asm volatile("s_memtime %0\n\ts_waitcnt lgkmcnt(0)" : "=s"(x) :: "memory"); SBAR(); } while (0)
template <int T, int I> DI void p_pieceA(const PCtx& c) {
  if constexpr (T < 24) {
    constexpr unsigned st = (unsigned)(T % 3) * 32768u, kt = (unsigned)(T & 7);
    if constexpr (T < 16) dma16c<st + I * 1024u, I * 8192u + kt * 128u>(c.mA, c.voffA[I & 1], c.sA1, c.soA1);
    else                  dma16c<st + I * 1024u, I * 8192u + kt * 128u>(c.mA, c.voffA[I & 1], c.sA2, c.soA2);
  }
}
template <int T, int I> DI void p_pieceB(const PCtx& c) {
  constexpr unsigned st = 98304u + (unsigned)(T & 1) * 32768u, kt = (unsigned)(T & 7);
  if constexpr (T < 16) {
    if constexpr (I < 2) dma16c<st + I * 1024u, kt * 524288u + I * 128u>(c.m2, c.voffB, T < 8 ? c.sUK : c.sUV, c.soB);
  } else if constexpr (T < 24) {
    if constexpr (I < 2) dma16c<st + I * 1024u, kt * 524288u + I * 128u>(c.mB2, c.voffB, c.sWQ, c.soB);
    else if constexpr (I == 2) dma16m<st + 2048u, kt * 196608u>(c.mB2, c.voffS, c.sWB, c.soS);
    else if (c.w == 7) *(LAS u16*)(c.L + st + ((c.lane >> 3) * 6 + 5) * 512 + (c.lane & 7) * 64 + 32) = (u16)c.gv[kt];
  }
}
template <int I, bool ISV> DI void p_kvstore(const PCtx& c) {
  if constexpr (I >= 0 && I < 8) {
    constexpr int mt = I >> 2, nt = (I >> 1) & 1, sh = I & 1;
    constexpr unsigned off = (unsigned)((ISV ? nt * 4 + 2 * mt + sh : mt * 8 + 2 * nt + sh) * 1024);
    __builtin_amdgcn_raw_buffer_store_b128(c.kvp[I], ISV ? c.sV : c.sK, c.kvo + off, 0, KVAUX);
  }
}
#define WARM1(VO, SRD, SO, OFF) dma16c<114688u, (OFF)>(c.m2, VO, SRD, SO)
template <int I> DI void p_warm(PCtx& c) {
  if constexpr (I == 0) { WARM1(c.voff, c.sA1, c.soPX, 0); WARM1(c.voffB, c.sUK, c.soPW, 0); WARM1(c.voffB, c.sUK, c.soPW, 128); }
  if constexpr (I == 1) { WARM1(c.voffB, c.sUV, c.soPW, 0); WARM1(c.voffB, c.sUV, c.soPW, 128); }
  if constexpr (I == 2) { WARM1(c.voff, c.sA2, c.soPX, 0); WARM1(c.voffB, c.sWQ, c.soPW, 0); }
  if constexpr (I == 3) { WARM1(c.voffB, c.sWQ, c.soPW, 128); WARM1(c.voffS, c.sWB, c.soPS, 0); }
}
#undef WARM1
DI void poll_spin(const u64* f, u64 tag, int lane) {
  while (!__all(__hip_atomic_load(f + (lane & 7), __ATOMIC_RELAXED, __HIP_MEMORY_SCOPE_AGENT) == tag)) __builtin_amdgcn_s_sleep(2);
}
DI void poll_issue(u32x2& fl, const u64* f, int lane) { asm volatile("global_load_dwordx2 %0, %1, off sc1" : "+v"(fl) : "v"(f + (lane & 7)) : "memory"); }
DI void poll_check(u32x2& fl, const u64* f, u64 tag, int lane) {
  asm volatile("" : "+v"(fl));
  if (!__all(fl[0] == (unsigned)tag && fl[1] == (unsigned)(tag >> 32))) poll_spin(f, tag, lane);
  asm volatile("" ::: "memory");
}
template <int B> DI void p_hook(PCtx& c) {
  if constexpr (B == 17) { if (c.w == 0) dma16c<155648u, 0u>(c.mA, c.voff, c.sWP, (unsigned)c.h * 512u); }
  if constexpr (B == 21) {
    if (c.w == 0) { if (c.lane == 0) __hip_atomic_store((u64*)c.flg + (blockIdx.x >> 5), c.tag, __ATOMIC_RELAXED, __HIP_MEMORY_SCOPE_AGENT); }
    {
      const int r32 = c.lane & 31, hh = c.lane >> 5; LAS const u16* wb = (LAS const u16*)(c.L + 155648) + (r32 & 15); const unsigned sg = r32 >= 16 ? 0x80008000u : 0u;
#pragma unroll
      for (int jj = 0; jj < 4; ++jj) { const int d0 = 8 * (jj >> 1) + 4 * hh + 2 * (jj & 1); c.wp[jj] = ((unsigned)wb[d0 * 16] | ((unsigned)wb[(d0 + 1) * 16] << 16)) ^ sg; }
    }
  }
  if constexpr (B == 22) { if (c.w == 0) poll_issue(c.fl, c.flg, c.lane); }
  if constexpr (B == 23) { if (c.w == 0) poll_check(c.fl, c.flg, c.tag, c.lane); }
  if constexpr (B == 24) {
    dma16s<0u, 0u>(c.mK0, c.voff, c.sK, c.soKa); dma16s<1024u, 1024u>(c.mK0, c.voff, c.sK, c.soKa);
    dma16s<2048u, 2048u>(c.mK0, c.voff, c.sK, c.soKa); dma16s<3072u, 3072u>(c.mK0, c.voff, c.sK, c.soKa);
  }
}

template <int PASS, int MT, int NT, int NSUB, bool TRANS>
struct PPass {
  PCtx& c; f32x16 (&acc)[MT][NT];
  unsigned aB[2][4], bB;
  bf16x8 fa[2][MT], fb[2][NT];
  template <int SET, int T, int KS> DI void load() {
    constexpr int bo = (T & 1) * 32768 + KS * (2 * NSUB * 512), sl = T % 3;
#pragma unroll
    for (int nt = 0; nt < NT; ++nt) fb[SET][nt] = trfrag(c.L + bB + bo + nt * 512, c.L + bB + bo + nt * 512 + 256);
#pragma unroll
    for (int mt = 0; mt < MT; ++mt) fa[SET][mt] = ldsfrag(c.L + aB[sl >> 1][KS] + (sl & 1) * 32768 + mt * 4096);
  }
  template <int SET, int I> DI void mma1() {
    if constexpr (I < MT * NT) {
      constexpr int mt = I / NT, nt = I % NT;
      acc[mt][nt] = TRANS ? MFMA(fb[SET][nt], fa[SET][mt], acc[mt][nt]) : MFMA(fa[SET][mt], fb[SET][nt], acc[mt][nt]);
    }
  }
  template <int SET, int T, int I0, int S0> DI void mmaA() {
    mma1<SET, 0>(); SBAR(); p_pieceA<T, I0>(c); SBAR(); mma1<SET, 1>(); SBAR(); p_kvstore<S0, PASS == 2>(c); SBAR(); mma1<SET, 2>(); SBAR(); p_pieceA<T, I0 + 1>(c); SBAR();
    mma1<SET, 3>(); mma1<SET, 4>(); mma1<SET, 5>();
  }
  template <int t> DI void tile() {
    constexpr int T = PASS * 8 + t;
    constexpr int SA = PASS == 1 ? t : (PASS == 2 && t < 4 ? 2 * t : -1), SB = PASS == 2 && t < 4 ? 2 * t + 1 : -1;
    constexpr int NW = T == 0 ? 5 : (T < 3 ? 2 : 0);
    constexpr int NST = (SA >= 0 ? 1 : 0) + (SB >= 0 ? 1 : 0) + NW;
    SBAR(); load<1, T, 1>(); SBAR(); mmaA<0, T + 2, 0, SA>();
    SBAR(); load<0, T, 2>(); SBAR(); mmaA<1, T + 2, 2, -1>();
    SBAR(); load<1, T, 3>(); SBAR(); if constexpr (T < 3) p_warm<T + 1>(c); SBAR(); mmaA<0, 24, 0, SB>();
    SBAR();
    if constexpr (T == 23) LBAR(); else if constexpr (T == 22) wait_bar<0>(); else wait_bar<4 + NST>();
    SBAR();
    if constexpr (t < 7) load<0, T + 1, 0>();
    SBAR();
    mma1<1, 0>(); p_pieceB<T + 2, 0>(c); SBAR();
    mma1<1, 1>(); p_pieceB<T + 2, 1>(c); SBAR();
    mma1<1, 2>(); p_pieceB<T + 2, 2>(c); SBAR();
    mma1<1, 3>(); p_pieceB<T + 2, 3>(c); SBAR();
    mma1<1, 4>(); mma1<1, 5>(); SBAR();
    p_hook<T + 1>(c); SBAR();
  }
  DI void run(unsigned arow0, unsigned nt0) {
    const int lane = c.lane, r = lane & 31, hh = lane >> 5, i16 = lane & 15;
#pragma unroll
    for (int ks = 0; ks < 4; ++ks) { aB[0][ks] = (arow0 + r) * 128u + (unsigned)(((2 * ks + hh) ^ ((r >> 1) & 7)) * 16); aB[1][ks] = aB[0][ks] + 65536u; asm volatile("" : "+v"(aB[1][ks])); }
    bB = 98304u + (unsigned)(hh * NSUB + nt0) * 512u + (unsigned)((i16 >> 2) * 64 + ((lane >> 4) & 1) * 32 + (i16 & 3) * 8); asm volatile("" : "+v"(bB));
#pragma unroll
    for (int mt = 0; mt < MT; ++mt)
#pragma unroll
      for (int nt = 0; nt < NT; ++nt) acc[mt][nt] = (f32x16){};
    load<0, PASS * 8, 0>();
    tile<0>(); tile<1>(); tile<2>(); tile<3>(); tile<4>(); tile<5>(); tile<6>(); tile<7>();
  }
};
template <int PASS, int MT, int NT, int NSUB, bool TRANS>
DI void p_pass(PCtx& c, f32x16 (&acc)[MT][NT], unsigned arow0, unsigned nt0) { PPass<PASS, MT, NT, NSUB, TRANS> p{c, acc}; p.run(arow0, nt0); }

DI void softmax2(f32x16& x) {
#pragma unroll
  for (int s = 0; s < 2; ++s) {
    float mx = fmaxf(fmaxf(fmaxf(x[8 * s], x[8 * s + 1]), fmaxf(x[8 * s + 2], x[8 * s + 3])), fmaxf(fmaxf(x[8 * s + 4], x[8 * s + 5]), fmaxf(x[8 * s + 6], x[8 * s + 7])));
    mx = swap32_max(mx);
    const float cc = -mx * 1.4426950408889634f;
    float sum = 0.f;
#pragma unroll
    for (int j = 0; j < 8; ++j) { x[8 * s + j] = __builtin_amdgcn_exp2f(__builtin_fmaf(x[8 * s + j], 1.4426950408889634f, cc)); sum += x[8 * s + j]; }
    const float inv = __builtin_amdgcn_rcpf(swap32_add(sum));
#pragma unroll
    for (int j = 0; j < 8; ++j) x[8 * s + j] *= inv;
  }
}

DI void mfma_acc(f32x16& c, bf16x8 a, bf16x8 b) { asm volatile("v_mfma_f32_32x32x16_bf16 %0, %1, %2, %0" : "+v"(c) : "v"(a), "v"(b)); }
DI void mfma_acc2(f32x16& c, bf16x8 a, bf16x8 b, bf16x8 nx) { asm volatile("v_mfma_f32_32x32x16_bf16 %0, %1, %2, %0" : "+v"(c) : "v"(a), "v"(b), "v"(nx)); }
DI void mfma_init2(f32x16& c, bf16x8 a, bf16x8 b, const f32x16& ci, bf16x8 nx) { asm volatile("v_mfma_f32_32x32x16_bf16 %0, %1, %2, %3" : "=&v"(c) : "v"(a), "v"(b), "v"(ci), "v"(nx)); }
DI float sum2(unsigned pk, float acc) { return __builtin_amdgcn_fdot2_f32_bf16(__builtin_bit_cast(bf16x2, pk), __builtin_bit_cast(bf16x2, 0x3f803f80u), acc, false); }
DI float sum8(bf16x8 p, float acc) { const u32x4 u = __builtin_bit_cast(u32x4, p); return sum2(u[3], sum2(u[2], sum2(u[1], sum2(u[0], acc)))); }
DI float max3f(float a, float b, float c) { float r; asm("v_max3_f32 %0, %1, %2, %3" : "=v"(r) : "v"(a), "v"(b), "v"(c)); return r; }
DI float max2f(float a, float b) { float r; asm("v_max_f32_e32 %0, %1, %2" : "=v"(r) : "v"(a), "v"(b)); return r; }
#define TS2X(a)
#define TS2V()
#define STP_PARAM
#define STP_ARG
#define TS2(acc)
#define NMV 4
#define TRAIL 4
#define ADIST 6
template <bool DOQK, bool DOPV>
DI void a_mphase(LAS const char* kp, LAS const char* vp, const bf16x8 (&q)[8], bf16x8 (&p)[4], f32x16 (&S)[2], f32x16 (&O)[4], const f32x16& negm, float& l, float& pm0,
                 unsigned mV, unsigned voff, srd_t sV, unsigned oV STP_PARAM) {
  constexpr int N = (DOQK ? 16 : 0) + (DOPV ? 16 : 0), D = ADIST;
  bf16x8 f[N];
  unsigned pw[8]; float ls = 0.f, e0 = 0.f;
#define A_ISQK(i) (DOQK && (!DOPV || (((i) >> 3) & 1) == 0))
#define A_IX(i) ((DOPV && DOQK) ? (((i) >> 4) * 8 + ((i) & 7)) : (i))
#define A_LD(i) do { if constexpr ((i) < N) { if constexpr (A_ISQK(i)) f[i] = ldsfrag(kp + A_IX(i) * 1024); \
                     else f[i] = ldsfrag(vp + ((A_IX(i) & 3) * 4 + (A_IX(i) >> 2)) * 1024); } } while (0)
#define A_MM(i) do { if constexpr (A_ISQK(i)) { if constexpr ((A_IX(i) & 7) == 0) mfma_init2(S[A_IX(i) >> 3], f[i], q[0], negm, f[(i) + 1]); \
                       else if constexpr ((i) & 1) mfma_acc(S[A_IX(i) >> 3], f[i], q[A_IX(i) & 7]); else mfma_acc2(S[A_IX(i) >> 3], f[i], q[A_IX(i) & 7], f[((i) + 1) % N]); } \
                     else if constexpr ((i) & 1) mfma_acc(O[A_IX(i) & 3], f[i], p[A_IX(i) >> 2]); else mfma_acc2(O[A_IX(i) & 3], f[i], p[A_IX(i) >> 2], f[((i) + 1) % N]); } while (0)
#define A_SM(e) do { const float x_ = __builtin_amdgcn_exp2f(S[1][(e) & 15]); ls += x_; \
                     if constexpr ((e) & 1) { pw[((e) & 15) >> 1] = cvtpk(e0, x_); asm volatile("" : "+v"(pw[((e) & 15) >> 1]), "+v"(ls)); } else { e0 = x_; asm volatile("" : "+v"(e0), "+v"(ls)); } \
                     if constexpr ((e) == 7) p[2] = asbf((u32x4){pw[0], pw[1], pw[2], pw[3]}); if constexpr ((e) == 15) p[3] = asbf((u32x4){pw[4], pw[5], pw[6], pw[7]}); } while (0)
#define A_MX(j) do { if constexpr ((j) == 0) pm0 = max2f(S[0][0], S[0][1]); else pm0 = max3f(pm0, S[0][2 * ((j) & 7)], S[0][2 * ((j) & 7) + 1]); asm volatile("" : "+v"(pm0)); } while (0)
#define A_STEP(i) do { if constexpr ((i) < N) { A_LD((i) + D); if constexpr ((i) == N - TRAIL) { SBAR(); TS2(a); LBAR(); TS2(b); SBAR(); } A_MM(i); \
                     if constexpr (DOQK && DOPV && (i) < 16) A_SM(i); \
                     if constexpr (DOQK && DOPV && (i) >= 16 && (i) < 24) A_MX((i) - 16); \
                     if constexpr (DOQK && DOPV && NMV >= 4 && (i) == 17) dma16o<0>(mV, voff, sV, oV); \
                     if constexpr (DOQK && DOPV && NMV >= 4 && (i) == 20) dma16n<1024>(voff, sV, oV); \
                     if constexpr (DOQK && DOPV && NMV >= 4 && (i) == 23) dma16n<2048>(voff, sV, oV); \
                     if constexpr (DOQK && DOPV && NMV >= 4 && (i) == 25) dma16n<3072>(voff, sV, oV); \
                     if constexpr (!DOQK && (i) < 8) { A_SM(2 * (i)); A_SM(2 * (i) + 1); } SBAR(); } } while (0)
  SBAR();
  __builtin_amdgcn_s_setprio(1);
  A_LD(0); A_LD(1); A_LD(2); A_LD(3); if constexpr (D > 4) { A_LD(4); A_LD(5); } SBAR();
  A_STEP(0); A_STEP(1); A_STEP(2); A_STEP(3); A_STEP(4); A_STEP(5); A_STEP(6); A_STEP(7);
  A_STEP(8); A_STEP(9); A_STEP(10); A_STEP(11); A_STEP(12); A_STEP(13); A_STEP(14); A_STEP(15);
  A_STEP(16); A_STEP(17); A_STEP(18); A_STEP(19); A_STEP(20); A_STEP(21); A_STEP(22); A_STEP(23);
  A_STEP(24); A_STEP(25); A_STEP(26); A_STEP(27); A_STEP(28); A_STEP(29); A_STEP(30); A_STEP(31);
  if constexpr (DOPV) l += ls;
  if constexpr (!DOPV) { NOP16(); SBAR(); A_MX(0); A_MX(1); A_MX(2); A_MX(3); A_MX(4); A_MX(5); A_MX(6); A_MX(7); }
  __builtin_amdgcn_s_setprio(0);
  TS2X(c);
#undef A_ISQK
#undef A_IX
#undef A_LD
#undef A_MM
#undef A_SM
#undef A_MX
#undef A_STEP
}

DI void a_finish(const f32x16& a0, const f32x16& a1, float ma, float la, LAS const char* xr, float mb, float lb, float gate, srd_t sC, unsigned voff, unsigned soff) {
  const float mt = fmaxf(ma, mb);
  const float fa = __builtin_amdgcn_exp2f(ma - mt), fb = __builtin_amdgcn_exp2f(mb - mt);
  const float sc = gate / (la * fa + lb * fb);
  const float wa = fa * sc, wb = fb * sc;
#pragma unroll
  for (int t = 0; t < 2; ++t) {
    const f32x16& a = t ? a1 : a0;
#pragma unroll
    for (int s = 0; s < 2; ++s) {
      const f32x4 b0 = *(LAS const f32x4*)(xr + t * 4096 + s * 2048), b1 = *(LAS const f32x4*)(xr + t * 4096 + s * 2048 + 1024);
      const u32x4 o = {cvtpk(a[8 * s + 0] * wa + b0[0] * wb, a[8 * s + 1] * wa + b0[1] * wb), cvtpk(a[8 * s + 2] * wa + b0[2] * wb, a[8 * s + 3] * wa + b0[3] * wb),
                       cvtpk(a[8 * s + 4] * wa + b1[0] * wb, a[8 * s + 5] * wa + b1[1] * wb), cvtpk(a[8 * s + 6] * wa + b1[2] * wb, a[8 * s + 7] * wa + b1[3] * wb)};
      st128(o, sC, voff + (unsigned)((2 * t + s) * 1024), soff);
      SBAR();
    }
  }
}
DI void a_send(const f32x16& a0, const f32x16& a1, LAS char* xr) {
#pragma unroll
  for (int g = 0; g < 4; ++g) {
    *(LAS f32x4*)(xr + g * 1024) = (f32x4){a0[4 * g], a0[4 * g + 1], a0[4 * g + 2], a0[4 * g + 3]};
    *(LAS f32x4*)(xr + 4096 + g * 1024) = (f32x4){a1[4 * g], a1[4 * g + 1], a1[4 * g + 2], a1[4 * g + 3]};
  }
}

__global__ __launch_bounds__(512, 2) void kF(const u16* __restrict__ x, const u16* __restrict__ ckv, const u16* __restrict__ wq_, const u16* __restrict__ wuk,
                                             const u16* __restrict__ wuv, const u16* __restrict__ wqkv, const u16* __restrict__ wphi, const u16* __restrict__ gatew,
                                             const u16* __restrict__ wo_a, const u16* __restrict__ wo_b, char* __restrict__ ws) {
  extern __shared__ __attribute__((aligned(16))) char lds[];
  LAS char* L = (LAS char*)lds; const unsigned lds0 = (unsigned)(uintptr_t)L;
  const int tid = threadIdx.x, lane = tid & 63, w = __builtin_amdgcn_readfirstlane(tid >> 6);
  const int b = blockIdx.x, h = (b & 7) * 4 + ((b >> 3) & 3), r = b >> 5;
  const int g = w >> 2, wq = w & 3, hh = lane >> 5, r32 = lane & 31;
  const int X = 15 - r, Y = r, nX = 16 - r;
  constexpr int NS = 17;
  const unsigned voff = (unsigned)lane * 16u;
  TS(t0);
  bf16x8 q[8];
  u64 tag; const u64* flg = (const u64*)(ws + WS_FLG + (size_t)h * 256);
  const srd_t sK = mksrd(ws + WS_KF + (size_t)h * 32 * 16384, 32u * 16384u), sV = mksrd(ws + WS_VF + (size_t)h * 32 * 16384, 32u * 16384u);
  const srd_t sW = mksrd(ws + WS_QF, (unsigned)(WS_DBG - WS_QF));
  const unsigned ringK = rfl(lds0 + (g ? 98304u : 32768u) + (unsigned)wq * 4096u), ringV = rfl(lds0 + (g ? 0u : 65536u) + (unsigned)wq * 4096u);
  {
    PCtx c;
    c.fl = (u32x2){0u, 0u}; c.wp = (u32x4){0u, 0u, 0u, 0u};
    c.L = L; c.w = w; c.lane = lane; c.h = h; c.voff = voff; c.flg = flg; c.sK = sK; c.sV = sV;
    c.soA1 = rfl((unsigned)(256 * r + 32 * w) * 1024u);
    c.soA2 = rfl((unsigned)(w < 4 ? 128 * X + 32 * w : 128 * Y + 32 * (w - 4)) * 1024u);
    c.soB = rfl((unsigned)(8 * w) * 8192u + (unsigned)h * 256u);
    c.soS = rfl((unsigned)(8 * w) * 3072u + (unsigned)h * 32u);
    c.soPW = rfl((unsigned)(64 * r + 8 * w) * 8192u + (unsigned)h * 256u); c.soPS = rfl((unsigned)(64 * r + 8 * w) * 3072u + (unsigned)h * 32u);
    c.soPX = rfl((unsigned)(256 * r + 8 * h + w) * 1024u);
    c.soKa = rfl((unsigned)(g * 16384 + wq * 4096)); c.mK0 = ringK;
    c.mA = rfl(lds0 + (unsigned)w * 4096u); c.mB2 = rfl(lds0 + (unsigned)w * 3072u); c.m2 = rfl(lds0 + (unsigned)w * 2048u);
    c.sA1 = mksrd(ckv, 2048u * 1024u); c.sA2 = mksrd(x, 2048u * 1024u);
    c.sUK = mksrd(wuk, 512u * 8192u); c.sUV = mksrd(wuv, 512u * 8192u); c.sWQ = mksrd(wq_, 512u * 8192u); c.sWB = mksrd(wqkv, 512u * 3072u); c.sWP = mksrd(wphi, 32u * 512u);
#pragma unroll
    for (int k = 0; k < 2; ++k) c.voffA[k] = (unsigned)((lane >> 3) * 1024 + (((lane & 7) ^ ((4 * k + (lane >> 4)) & 7)) * 16));
    c.voffB = (unsigned)(((lane >> 2) & 7) * 8192 + (lane >> 5) * 64 + (lane & 3) * 16);
    { const int ch = lane & 3, sub = lane >> 5;
      c.voffS = (unsigned)(((lane >> 2) & 7) * 3072 + (sub == 0 ? (ch < 2 ? ch * 16 : 1024 + (ch - 2) * 16) : 2048 + (ch & 1) * 16)); }
    if (b == 0 && tid < 256) *(unsigned*)(ws + WS_CNT + tid * 64) = 0u;
    u32x2 ep;
    asm volatile("global_load_dwordx2 %0, %1, off sc1" : "=v"(ep) : "v"(flg + 16) : "memory");
#pragma unroll
    for (int k = 0; k < 8; ++k) asm volatile("global_load_ushort %0, %1, off offset:%2" : "=v"(c.gv[k]) : "v"(gatew + lane), "i"(k * 128) : "memory");
    p_pieceA<0, 0>(c); p_pieceA<0, 1>(c); p_pieceA<0, 2>(c); p_pieceA<0, 3>(c);
    p_pieceB<0, 0>(c); p_pieceB<0, 1>(c);
    p_pieceB<1, 0>(c); p_pieceB<1, 1>(c);
    p_pieceA<1, 0>(c); p_pieceA<1, 1>(c); p_pieceA<1, 2>(c); p_pieceA<1, 3>(c);
    p_warm<0>(c);
    wait_bar<9>();
    asm volatile("" : "+v"(ep), "+v"(c.gv[0]), "+v"(c.gv[1]), "+v"(c.gv[2]), "+v"(c.gv[3]), "+v"(c.gv[4]), "+v"(c.gv[5]), "+v"(c.gv[6]), "+v"(c.gv[7]));
    tag = (((u64)rfl(ep[1]) << 32) | rfl(ep[0])) + 1ull; c.tag = tag;
    {
      f32x16 acc[2][2];
      const int wm = w >> 1, wn = w & 1;
      p_pass<0, 2, 2, 4, true>(c, acc, 64u * wm, 2u * wn);
#pragma unroll
      for (int i = 0; i < 4; ++i) { c.kvp[2 * i] = pack8<0>(acc[i >> 1][i & 1]); c.kvp[2 * i + 1] = pack8<1>(acc[i >> 1][i & 1]); }
      c.kvo = voff + (unsigned)((4 * r + wm) * 16384 + 4 * wn * 1024);
      p_pass<1, 2, 2, 4, false>(c, acc, 64u * wm, 2u * wn);
#pragma unroll
      for (int i = 0; i < 4; ++i) { c.kvp[2 * i] = pack8<0>(acc[i >> 1][i & 1]); c.kvp[2 * i + 1] = pack8<1>(acc[i >> 1][i & 1]); }
      c.kvo = voff + (unsigned)((4 * r + wm) * 16384 + 8 * wn * 1024);
    }
    TS(t1);
    f32x16 acc[2][3];
    p_pass<2, 2, 3, 6, true>(c, acc, 64u * wq, 3u * g);
    TS(t2);
    if (wq < 2) {
#pragma unroll
      for (int mt = 0; mt < 2; ++mt)
#pragma unroll
        for (int nt = 0; nt < 3; ++nt)
          if (g == 0 || nt == 0) {
            const f32x16 v = acc[mt][nt] * QSCALE; LAS char* qd = L + 131072 + ((2 * wq + mt) * 8 + 2 * (3 * g + nt)) * 1024 + lane * 16;
            *(LAS u32x4*)qd = pack8<0>(v); *(LAS u32x4*)(qd + 1024) = pack8<1>(v);
          }
    }
    LAS char* Ik = L + (g ? 16384 : 81920) + wq * 4096; LAS char* Iv = Ik + 2048;
    u32x4 hq, hk, hv; float gl;
    if (g == 1) {
      LAS char* ho = L + 81920 + wq * 4096 + lane * 16;
      *(LAS u32x4*)ho = pack8<0>(acc[1][1]); *(LAS u32x4*)(ho + 1024) = pack8<1>(acc[1][1]); *(LAS u32x4*)(ho + 2048) = pack8<0>(acc[1][2]);
      *(LAS float*)(L + 81920 + wq * 4096 + 3072 + lane * 4) = acc[1][2][8];
      hq = pack8<0>(acc[0][1]); hk = pack8<1>(acc[0][1]); hv = pack8<0>(acc[0][2]); gl = acc[0][2][8];
    }
    TS(ts_a);
    LBAR();
    TS(ts_b);
#pragma unroll
    for (int ks = 0; ks < 8; ++ks) q[ks] = ldsfrag(L + 131072 + wq * 8192 + ks * 1024 + lane * 16);
    if (g == 0) {
      hq = *(LAS const u32x4*)(Ik + lane * 16); hk = *(LAS const u32x4*)(Ik + 1024 + lane * 16); hv = *(LAS const u32x4*)(Ik + 2048 + lane * 16); gl = *(LAS const float*)(Ik + 3072 + lane * 4);
    }
    if (wq >= 2) {
#pragma unroll
      for (int mt = 0; mt < 2; ++mt)
#pragma unroll
        for (int nt = 0; nt < 3; ++nt)
          if (g == 0 || nt == 0) {
            const f32x16 v = acc[mt][nt] * QSCALE; const unsigned so = (unsigned)(b * 32768 + ((2 * (wq - 2) + mt) * 8 + 2 * (3 * g + nt)) * 1024);
            st128(pack8<0>(v), sW, voff, so); st128(pack8<1>(v), sW, voff + 1024u, so);
          }
    }
    {
      const int i16 = lane & 15, rt = 2 * wq + (g ^ 1);
      const int sw = (r32 >> 1) & 7; LAS char* wr = Ik + r32 * 64;
      const int gr = ((lane >> 4) & 1) * 4 + (i16 & 3), q4 = i16 >> 2;
      unsigned tb[2];
#pragma unroll
      for (int u = 0; u < 2; ++u) tb[u] = (unsigned)((8 * hh + 4 * u + q4) * 64 + ((gr ^ (4 * hh + 2 * u + (q4 >> 1))) * 8));
      f32x16 pq = MFMA(asbf(c.wp), asbf(hq), (f32x16){});
      f32x16 pk = MFMA(asbf(c.wp), asbf(hk), (f32x16){});
      LWAIT();
      *(LAS u32x2*)(wr + 2048 + ((0 + hh) ^ sw) * 8) = (u32x2){hv[0], hv[1]};
      *(LAS u32x2*)(wr + 2048 + ((2 + hh) ^ sw) * 8) = (u32x2){hv[2], hv[3]};
      *(LAS u32x2*)(wr + 2048 + ((4 + hh) ^ sw) * 8) = (u32x2){hh == 0 ? 0x00003f80u : 0u, 0u};
      *(LAS u32x2*)(wr + 2048 + ((6 + hh) ^ sw) * 8) = (u32x2){0u, 0u};
      softmax2(pk);
#pragma unroll
      for (int k = 0; k < 4; ++k) *(LAS u32x2*)(wr + ((2 * k + hh) ^ sw) * 8) = (u32x2){cvtpk(pk[4 * k], pk[4 * k + 1]), cvtpk(pk[4 * k + 2], pk[4 * k + 3])};
      LWAIT();
      f32x16 kv = {};
#pragma unroll
      for (int ks = 0; ks < 2; ++ks) kv = MFMA(trfrag(Ik + tb[0] + ks * 1024, Ik + tb[1] + ks * 1024), trfrag(Iv + tb[0] + ks * 1024, Iv + tb[1] + ks * 1024), kv);
      softmax2(pq);
      const unsigned so = (unsigned)(WS_PQ - WS_QF) + (unsigned)(b * 16384 + rt * 2048);
      st128(pack8<0>(pq), sW, voff, so); st128(pack8<1>(pq), sW, voff + 1024u, so);
      const float gt = __builtin_amdgcn_rcpf(1.0f + __builtin_amdgcn_exp2f(-1.4426950408889634f * swap32_lo(gl)));
      __builtin_amdgcn_raw_buffer_store_b32(__float_as_uint(gt), sW, (unsigned)r32 * 4u + (unsigned)(WS_GW - WS_QF) + (unsigned)(b * 1024 + rt * 128), 0, 0);
      LWAIT();
#pragma unroll
      for (int k = 0; k < 4; ++k) *(LAS f32x4*)(Ik + k * 1024 + lane * 16) = (f32x4){kv[4 * k], kv[4 * k + 1], kv[4 * k + 2], kv[4 * k + 3]};
    }
    dma16s<16384u, 32768u>(ringK, voff, sK, c.soKa); dma16s<16384u + 1024u, 32768u + 1024u>(ringK, voff, sK, c.soKa);
    dma16s<16384u + 2048u, 32768u + 2048u>(ringK, voff, sK, c.soKa); dma16s<16384u + 3072u, 32768u + 3072u>(ringK, voff, sK, c.soKa);
    dma16s<0u, 0u>(ringV, voff, sV, c.soKa); dma16s<1024u, 1024u>(ringV, voff, sV, c.soKa);
    dma16s<2048u, 2048u>(ringV, voff, sV, c.soKa); dma16s<3072u, 3072u>(ringV, voff, sV, c.soKa);
    TS(ts_c);
    wait_bar<11>();
    if (g == 1) {
      f32x4 s4 = *(LAS const f32x4*)(L + 16384 + wq * 1024 + lane * 16);
#pragma unroll
      for (int k = 1; k < 8; ++k) s4 += *(LAS const f32x4*)(L + (k < 4 ? 16384 : 81920) + (k & 3) * 4096 + wq * 1024 + lane * 16);
      st128_wt(__builtin_bit_cast(u32x4, s4), sW, voff, (unsigned)(WS_KVP - WS_QF) + (unsigned)((h * 8 + r) * 4096 + wq * 1024));
      LBAR();
    }
  }
  TS(t3);
  f32x16 O[4], S[2], keep0 = {}, keep1 = {}, negm = {};
#pragma unroll
  for (int d = 0; d < 4; ++d) O[d] = (f32x16){};
  bf16x8 p[4];
  float m = 0.f, l = 0.f, mk = 0.f, lk = 0.f, pm0 = 0.f;
  float gate[2] = {0.f, 0.f}; f32x2 ps = {0.f, 0.f};
  u32x2 fl2 = {0u, 0u};
  u32x4 warm_dummy; unsigned warm_dm1;
  LAS const char* kbase = L + (g ? 98304 : 32768) + lane * 16;
  LAS const char* vbase = L + (g ? 0 : 65536) + lane * 16;
  LAS char* xr = L + 131072 + wq * 8192 + lane * 16;
#define TILE(s) ((s) < nX ? 2 * (s) + g : 2 * ((s) - nX) + g)
#define CF_SO(blkrow, half) ((unsigned)(WS_CF - WS_QF) + (unsigned)(((4 * (blkrow) + wq) * 288 + 8 * h + 4 * (half)) * 1024))
#define ST_SO(wv) ((unsigned)(WS_ST - WS_QF) + (unsigned)(b * 2048 + (wv) * 256))
#define ST_LOAD(wv) asm volatile("buffer_load_dwordx2 %0, %1, %2, %3 offen" : "+v"(ps) : "v"((unsigned)r32 * 8u), "s"(sW), "s"(rfl(ST_SO(wv))) : "memory")
#define L_SOFTMAX(DIAG, FIRST, D0, D1, D2, D3, D4, D5, D6, D7) do { \
    if (DIAG) { int rel = 32 * wq + r32 - 64 * g - 4 * hh; \
      _Pragma("unroll") for (int e = 0; e < 16; ++e) { const int cc = (e & 3) + 8 * (e >> 2); if ((e & 3) == 0) { asm volatile("" : "+v"(rel)); SBAR(); } \
        if (cc > rel) S[0][e] = -__builtin_inff(); if (cc + 32 > rel) S[1][e] = -__builtin_inff(); } \
      pm0 = max2f(S[0][0], S[0][1]); _Pragma("unroll") for (int e = 2; e < 16; e += 2) pm0 = max3f(pm0, S[0][e], S[0][e + 1]); } \
    float pm = max3f(pm0, S[1][0], S[1][1]), pn = max3f(S[1][2], S[1][3], S[1][4]); \
    pm = max3f(pm, S[1][5], S[1][6]); pn = max3f(pn, S[1][7], S[1][8]); pm = max3f(pm, S[1][9], S[1][10]); pn = max3f(pn, S[1][11], S[1][12]); \
    pm = max3f(pm, S[1][13], S[1][14]); pm = max3f(pm, pn, S[1][15]); \
    { auto rr = __builtin_amdgcn_permlane32_swap(__float_as_uint(pm), __float_as_uint(pm), false, false); pm = max2f(__uint_as_float(rr[0]), __uint_as_float(rr[1])); } \
    SBAR(); TS2X(f); D0; SBAR(); \
    const bool first_ = (FIRST); \
    if (first_ || __builtin_expect(__any(pm > THR), 0)) { \
      NOP16(); SBAR(); \
      const float dl = fmaxf(pm, first_ ? -1e30f : 0.f); \
      m += dl; \
      if (!first_) { const float f = __builtin_amdgcn_exp2f(-dl); l *= f; _Pragma("unroll") for (int d = 0; d < 4; ++d) O[d] = O[d] * f; } \
      _Pragma("unroll") for (int e = 0; e < 16; ++e) { S[0][e] -= dl; S[1][e] -= dl; negm[e] = -m; } \
    } \
    TS2X(g); float ls0 = l, ls1 = 0.f; unsigned pw[8]; \
    SM_GRP(0, D1); SM_GRP(2, D2); SM_GRP(4, D3); SM_GRP(6, D4); SM_GRP(8, D5); SM_GRP(10, D6); SM_GRP(12, D7); SM_GRP(14, (void)0); \
    l = ls0 + ls1; \
    p[0] = asbf((u32x4){pw[0], pw[1], pw[2], pw[3]}); p[1] = asbf((u32x4){pw[4], pw[5], pw[6], pw[7]}); } while (0)
#define SM_GRP(e0, DMA) do { const float x0 = __builtin_amdgcn_exp2f(S[0][e0]), x1 = __builtin_amdgcn_exp2f(S[0][(e0) + 1]); \
      ls0 += x0; ls1 += x1; pw[(e0) >> 1] = cvtpk(x0, x1); asm volatile("" : "+v"(pw[(e0) >> 1]), "+v"(ls0), "+v"(ls1)); DMA; SBAR(); } while (0)
#define L_SUM() do { } while (0)
  float mo = 0.f, lo = 0.f;
  auto l_pre = [&](int s) {
    if (s == nX) {
      NOP16(); SBAR();
      mo = m; lo = swap32_add(l);
      if (g == 1) ST_LOAD(w - 4);
      __builtin_amdgcn_raw_buffer_store_b64(__builtin_bit_cast(u32x2, (f32x2){mo, lo}), sW, (unsigned)r32 * 8u, ST_SO(w), 0);
      if (g == 0) { a_send(O[2], O[3], xr); keep0 = O[0]; keep1 = O[1]; mk = mo; lk = lo; }
      m = 0.f; l = 0.f;
    }
    if (g == 0 && s == nX + 1) ST_LOAD(w + 4);
  };
  auto l_post = [&](int s) {
    if (s == nX) {
      if (g == 1) {
        wait_vm<8 - NMV>(); asm volatile("" : "+v"(ps));
        a_finish(O[2], O[3], mo, lo, xr, ps[0], ps[1], gate[0], sW, voff, CF_SO(X, 1));
        LWAIT();
        a_send(O[0], O[1], xr);
      }
#pragma unroll
      for (int d = 0; d < 4; ++d) O[d] = (f32x16){};
    }
    if (g == 0 && s == nX + 1) {
      wait_vm<8 - NMV>(); asm volatile("" : "+v"(ps));
      a_finish(keep0, keep1, mk, lk, xr, ps[0], ps[1], gate[0], sW, voff, CF_SO(X, 0));
    }
  };
  unsigned mK = ringK, mV = ringV + 16384u, oK = (unsigned)(4 + g) * 16384u + (unsigned)wq * 4096u, oV = (unsigned)(2 + g) * 16384u + (unsigned)wq * 4096u;
  LAS const char* kp = kbase + 16384; LAS const char* vp = vbase;
#define LV(x) do { if constexpr (NMV == 0) { x; } } while (0)
#define A_TAIL() do { \
    asm volatile("" : "+v"(p[0]), "+v"(p[1]), "+v"(l), "+v"(S[1])); \
    SBAR(); TS2(d); TS2V(); wait_bar<8 - NMV>(); TS2(e); SBAR(); \
    a_mphase<true, true>(kp, vp, q, p, S, O, negm, l, pm0, mV, voff, sV, oV STP_ARG); oV += 32768u; \
    mK ^= 16384u; mV ^= 16384u; kp = (LAS const char*)((unsigned)(uintptr_t)kp ^ 16384u); vp = (LAS const char*)((unsigned)(uintptr_t)vp ^ 16384u); } while (0)
#define A_FAST(FIRST) do { \
    if (!(FIRST)) L_SUM(); \
    L_SOFTMAX(false, FIRST, dma16o<0>(mK, voff, sK, oK), dma16n<1024>(voff, sK, oK), dma16n<2048>(voff, sK, oK), dma16n<3072>(voff, sK, oK), \
              LV(dma16o<0>(mV, voff, sV, oV)), LV(dma16n<1024>(voff, sV, oV)), LV(dma16n<2048>(voff, sV, oV)), LV(dma16n<3072>(voff, sV, oV))); \
    oK += 32768u; TS2X(h); A_TAIL(); } while (0)
#define A_SLOW(s) do { \
    L_SUM(); l_pre(s); \
    oK = (s) < 15 ? (unsigned)TILE((s) + 2) * 16384u + (unsigned)wq * 4096u : (unsigned)(WS_KVP - WS_QF) + (unsigned)(h * 32768 + w * 4096); \
    oV = (unsigned)TILE((s) + 1) * 16384u + (unsigned)wq * 4096u; \
    const srd_t sKx = (s) < 15 ? sK : sW; \
    L_SOFTMAX((s) == nX - 1, (s) == nX, dma16o<0>(mK, voff, sKx, oK), dma16n<1024>(voff, sKx, oK), dma16n<2048>(voff, sKx, oK), dma16n<3072>(voff, sKx, oK), \
              LV(dma16o<0>(mV, voff, sV, oV)), LV(dma16n<1024>(voff, sV, oV)), LV(dma16n<2048>(voff, sV, oV)), LV(dma16n<3072>(voff, sV, oV))); \
    oK += 32768u; TS2X(h); \
    l_post(s); \
    if ((s) == nX - 1) { _Pragma("unroll") for (int ks = 0; ks < 8; ++ks) q[ks] = ldsfrag(L + 131072 + wq * 8192 + ks * 1024 + lane * 16); negm = (f32x16){}; } \
    A_TAIL(); } while (0)
  a_mphase<true, false>(kbase, vbase, q, p, S, O, negm, l, pm0, 0u, voff, sV, 0u STP_ARG);
  A_FAST(true);
  if (w == 0 && lane == 0) __hip_atomic_store((u64*)flg + 8 + r, tag, __ATOMIC_RELAXED, __HIP_MEMORY_SCOPE_AGENT);
  A_FAST(false);
  {
    const unsigned mq = rfl(lds0 + 131072u + (unsigned)w * 4096u), oq = rfl((unsigned)(b * 32768 + w * 4096));
    dma16o<0>(mq, voff, sW, oq); dma16n<1024>(voff, sW, oq); dma16n<2048>(voff, sW, oq); dma16n<3072>(voff, sW, oq);
    const unsigned go = rfl((unsigned)(WS_GW - WS_QF) + (unsigned)(b * 1024 + wq * 128));
    asm volatile("buffer_load_dword %0, %2, %3, %4 offen\n\tbuffer_load_dword %1, %2, %3, %4 offen offset:512" : "+v"(gate[0]), "+v"(gate[1]) : "v"((unsigned)r32 * 4u), "s"(sW), "s"(go) : "memory");
    poll_issue(fl2, flg + 8, lane);
    const srd_t sWa = mksrd(wo_a, 4096u * 1024u), sWb = mksrd(wo_b, 512u * 1024u);
    u32x4 dmy; unsigned dm1;
    asm volatile("buffer_load_dwordx4 %0, %2, %3, 0 offen\n\tbuffer_load_dwordx4 %0, %2, %3, 0 offen offset:1024\n\t"
                 "buffer_load_dwordx4 %0, %4, %5, 0 offen\n\tbuffer_load_dword %1, %6, %3, 0 offen"
                 : "=&v"(dmy), "=&v"(dm1) : "v"(voff + (unsigned)(b * 16384 + w * 2048)), "s"(sWa), "v"((unsigned)(b * 2048 + w * 256) + (voff & 255u)), "s"(sWb),
                   "v"((unsigned)((((b >> 3) * 8 + w) * 4 + (lane & 3)) * 4096 + (lane >> 2) * 0)) : "memory");
    warm_dummy = dmy; warm_dm1 = dm1;
  }
  A_FAST(false);
  asm volatile("" : "+v"(gate[0]), "+v"(gate[1]), "+v"(fl2));
  asm volatile("" :: "v"(warm_dummy), "v"(warm_dm1));
  if (w == 0) { poll_check(fl2, flg + 8, tag, lane); if (r == 0 && lane == 0) __hip_atomic_store((u64*)flg + 16, tag, __ATOMIC_RELAXED, __HIP_MEMORY_SCOPE_AGENT); }
  {
    int s = 3;
#pragma unroll 1
    for (int blk = 0; blk < 2; ++blk) {
      const int e1 = blk ? 15 : nX - 2, e2 = blk ? 16 : (nX + 2 < 16 ? nX + 2 : 16);
#pragma unroll 1
      for (; s < e1; ++s) A_FAST(false);
#pragma unroll 1
      for (; s < e2; ++s) A_SLOW(s);
    }
  }
  TS(t4);
  {
    L_SUM(); l_pre(16);
    const unsigned mp = rfl(lds0 + (g ? 98304u : 32768u) + (unsigned)wq * 2048u), op = rfl((unsigned)(WS_PQ - WS_QF) + (unsigned)(b * 16384 + w * 2048));
    L_SOFTMAX(true, nX == 16, dma16o<0>(mp, voff, sW, op), dma16n<1024>(voff, sW, op), (void)0, (void)0, (void)0, (void)0, (void)0, (void)0);
    if (16 == nX || 16 == nX + 1) wait_vm<0>();
    l_post(16);
    asm volatile("" : "+v"(p[0]), "+v"(p[1]), "+v"(l), "+v"(S[1]));
    SBAR(); wait_bar<2>(); SBAR();
  }
  a_mphase<false, true>(kbase, vbase, q, p, S, O, negm, l, pm0, 0u, voff, sV, 0u STP_ARG);
  NOP16(); SBAR();
  L_SUM();
  if (g == 0 && nX == 16) {
    ST_LOAD(w + 4); wait_vm<0>(); asm volatile("" : "+v"(ps));
    a_finish(keep0, keep1, mk, lk, xr, ps[0], ps[1], gate[0], sW, voff, CF_SO(X, 0));
    LWAIT();
  }
  LAS f32x2* stl = (LAS f32x2*)(L + 81920) + r32;
  auto stage2 = [&]() {
    f32x16 kv = {};
#pragma unroll
    for (int k = 0; k < 8; ++k)
#pragma unroll
      for (int qd = 0; qd < 4; ++qd) { const f32x4 v = *(LAS const f32x4*)(L + (k < 4 ? 49152 : 114688) + (k & 3) * 4096 + qd * 1024 + lane * 16);
#pragma unroll
        for (int e = 0; e < 4; ++e) kv[4 * qd + e] += v[e]; }
    const bf16x8 p0 = ldsfrag(kbase + wq * 2048), p1 = ldsfrag(kbase + wq * 2048 + 1024);
    f32x16 o = MFMA(asbf(pack8<0>(kv)), p0, (f32x16){});
    o = MFMA(asbf(pack8<1>(kv)), p1, o);
    const float sc = (1.0f - (g ? gate[1] : gate[0])) / (swap32_lo(o[8]) + 1e-6f);
    o = o * sc;
    st128(pack8<0>(o), sW, voff, (unsigned)(WS_CF - WS_QF) + (unsigned)(((4 * (g ? Y : X) + wq) * 288 + 256 + h) * 1024));
  };
  const float lt = swap32_add(l);
  if (g == 0) {
    a_send(O[2], O[3], xr);
    stl[w * 32] = (f32x2){m, lt};
    wait_bar<0>();
    stage2();
    LBAR();
    const f32x2 po = stl[(w + 4) * 32];
    a_finish(O[0], O[1], m, lt, xr, po[0], po[1], gate[1], sW, voff, CF_SO(Y, 0));
  } else {
    const f32x2 po = stl[(w - 4) * 32];
    a_finish(O[2], O[3], m, lt, xr, po[0], po[1], gate[1], sW, voff, CF_SO(Y, 1));
    LWAIT();
    a_send(O[0], O[1], xr);
    stl[w * 32] = (f32x2){m, lt};
    wait_bar<4>();
    stage2();
  }
#undef TILE
#undef CF_SO
#undef ST_SO
#undef ST_LOAD
#undef L_SOFTMAX
#undef SM_GRP
#undef L_SUM
#undef LV
#undef A_TAIL
#undef A_FAST
#undef A_SLOW
}

struct OCtx {
  srd_t sC, sWa, sWb, sS; unsigned voff, voffW, soA, soW[4], mW, bB, cut;
  LAS char* L; bf16x8 a[36];
};
constexpr int O_NS = 5;
template <int S, int I> DI void o_wpiece(const OCtx& c) {
  if constexpr (S < 12) {
    constexpr unsigned sl = (unsigned)(S % O_NS) * 24576u + I * 8192u, ks = 24u * (S % 3) + 8u * I;
    if (ks < c.cut) dma16c<sl, ks * 16384u>(c.mW, c.voffW, c.sWa, c.soW[S / 3]);
    else            dma16c<sl, ks * 16384u - 4194304u>(c.mW, c.voffW, c.sWb, c.soW[S / 3]);
  }
}
template <int i> DI void o_aload(OCtx& c) {
  unsigned t;
  asm volatile("s_add_u32 %1, %4, %5\n\tbuffer_load_dwordx4 %0, %2, %3, %1 offen" : "=v"(c.a[i]), "=&s"(t) : "v"(c.voff), "s"(c.sC), "s"(c.soA), "i"(i * 2048u) : "memory", "scc");
}
template <int i0, int n> DI void o_aloads(OCtx& c) { if constexpr (n > 0) { o_aload<i0>(c); o_aloads<i0 + 1, n - 1>(c); } }
template <int i0> DI void o_amark(OCtx& c) {
  asm volatile("" : "+v"(c.a[i0]), "+v"(c.a[i0 + 1]), "+v"(c.a[i0 + 2]), "+v"(c.a[i0 + 3]), "+v"(c.a[i0 + 4]), "+v"(c.a[i0 + 5]),
                    "+v"(c.a[i0 + 6]), "+v"(c.a[i0 + 7]), "+v"(c.a[i0 + 8]), "+v"(c.a[i0 + 9]), "+v"(c.a[i0 + 10]), "+v"(c.a[i0 + 11]));
}
template <int S> DI void o_stage(OCtx& c, f32x16& acc) {
  constexpr int sl = S % O_NS, i0 = 12 * (S % 3);
  LAS const char* sp = c.L + (sl < 2 ? c.bB : (sl < 4 ? c.bB + 49152u : c.bB + 98304u)) + (sl & 1) * 24576;
  bf16x8 f[12];
#define O_LD(k) f[k] = trfrag(sp + (k) * 2048, sp + (k) * 2048 + 512)
#define O_MM(k) acc = MFMA(f[k], c.a[i0 + (k)], acc)
  O_LD(0); O_LD(1); O_LD(2); O_LD(3); SBAR();
  O_LD(4); O_MM(0); SBAR(); O_LD(5); O_MM(1); o_wpiece<S + 3, 0>(c); SBAR(); O_LD(6); O_MM(2); SBAR(); O_LD(7); O_MM(3); SBAR();
  O_LD(8); O_MM(4); SBAR(); O_LD(9); O_MM(5); o_wpiece<S + 3, 1>(c); SBAR(); O_LD(10); O_MM(6); SBAR(); O_LD(11); O_MM(7); SBAR();
  O_MM(8); SBAR(); O_MM(9); o_wpiece<S + 3, 2>(c); SBAR(); O_MM(10); O_MM(11); SBAR();
#undef O_LD
#undef O_MM
}
DI void o_park(LAS char* p, const f32x16& v) {
#pragma unroll
  for (int qd = 0; qd < 4; ++qd) *(LAS f32x4*)(p + qd * 1024) = (f32x4){v[4 * qd], v[4 * qd + 1], v[4 * qd + 2], v[4 * qd + 3]};
}
DI void o_addlds(f32x16& v, LAS const char* p) {
#pragma unroll
  for (int qd = 0; qd < 4; ++qd) { const f32x4 u = *(LAS const f32x4*)(p + qd * 1024);
#pragma unroll
    for (int e = 0; e < 4; ++e) v[4 * qd + e] += u[e]; }
}
__global__ __launch_bounds__(512, 2) void kO(const u16* __restrict__ wo_a, const u16* __restrict__ wo_b, char* __restrict__ ws, u16* __restrict__ out) {
  extern __shared__ __attribute__((aligned(16))) char lds[];
  LAS char* L = (LAS char*)lds; const unsigned lds0 = (unsigned)(uintptr_t)L;
  const int tid = threadIdx.x, lane = tid & 63, w = __builtin_amdgcn_readfirstlane(tid >> 6);
  const int b = blockIdx.x, xcd = b & 7, j = xcd & 3, idx = b >> 3, mi = (xcd >> 2) * 8 + (idx >> 2), ni = idx & 3;
  const int g = w >> 2, rb = w & 3, hh = lane >> 5, r32 = lane & 31, i16 = lane & 15, p4 = i16 & 3;
  const int tg = mi * 4 + ni;
  TS(z0);
  OCtx c;
  c.L = L;
  c.sC = mksrd(ws + WS_CF, 64u * 288u * 1024u); c.sWa = mksrd(wo_a, 4096u * 1024u); c.sWb = mksrd(wo_b, 512u * 1024u);
  c.sS = mksrd(ws + WS_SLAB + (size_t)tg * 4 * 65536, 4u * 65536u);
  c.voff = (unsigned)lane * 16u;
  c.voffW = (unsigned)((lane >> 2) * 1024 + (lane & 3) * 16);
  c.soA = rfl((unsigned)((4 * mi + rb) * 288 + 72 * j + g) * 1024u);
#pragma unroll
  for (int p = 0; p < 4; ++p) c.soW[p] = rfl((unsigned)(72 * j + w) * 16384u + (unsigned)(ni * 256 + ((j + 1 + p) & 3) * 64));
  c.cut = rfl((unsigned)(256 - 72 * j));
  c.mW = rfl(lds0 + (unsigned)w * 1024u);
  c.bB = (unsigned)(g * 1024 + (4 * hh + (i16 >> 2)) * 64 + ((lane >> 4) & 1) * 32 + (((p4 & 1) << 1) | (p4 >> 1)) * 8);
  LAS char* red = L + 122880 + rb * 4096 + lane * 16;
  unsigned* cnt = (unsigned*)(ws + WS_CNT + (size_t)tg * 256);
  o_wpiece<0, 0>(c); o_wpiece<0, 1>(c); o_wpiece<0, 2>(c); o_aloads<0, 12>(c);
  o_wpiece<1, 0>(c); o_wpiece<1, 1>(c); o_wpiece<1, 2>(c); o_aloads<12, 12>(c);
  o_wpiece<2, 0>(c); o_wpiece<2, 1>(c); o_wpiece<2, 2>(c); o_aloads<24, 12>(c);
  f32x16 acc = {};
  wait_bar<30>(); o_amark<0>(c);  SBAR(); o_stage<0>(c, acc);
  wait_bar<18>(); o_amark<12>(c); SBAR(); o_stage<1>(c, acc);
  wait_bar<6>();  o_amark<24>(c); SBAR(); o_stage<2>(c, acc);
  TS(z1);
#define O_PUBLISH(P) do { o_addlds(prev, red); \
    _Pragma("unroll") for (int qd = 0; qd < 4; ++qd) st128_wt(__builtin_bit_cast(u32x4, (f32x4){prev[4 * qd], prev[4 * qd + 1], prev[4 * qd + 2], prev[4 * qd + 3]}), c.sS, \
        c.voff + (unsigned)(qd * 1024), (unsigned)(j * 65536 + ((j + 1 + (P)) & 3) * 16384 + rb * 4096)); } while (0)
#define O_SIGNAL(P) do { if (g == 0 && lane == 0) __hip_atomic_fetch_add(cnt + 16 * ((j + 1 + (P)) & 3), 1u, __ATOMIC_RELAXED, __HIP_MEMORY_SCOPE_AGENT); } while (0)
#define O_PASS(P) do { \
    if (g == 1) o_park(red, acc); \
    f32x16 prev = acc; acc = (f32x16){}; \
    wait_bar<6>(); SBAR(); if (g == 0) O_PUBLISH((P) - 1); SBAR(); o_stage<3 * (P)>(c, acc); \
    if (g == 0) wait_bar<(P) < 3 ? 10 : 7>(); else wait_bar<(P) < 3 ? 6 : 3>(); SBAR(); o_stage<3 * (P) + 1>(c, acc); \
    if constexpr ((P) < 3) wait_bar<6>(); else wait_bar<0>(); \
    SBAR(); O_SIGNAL((P) - 1); } while (0)
  O_PASS(1); SBAR(); o_stage<5>(c, acc);
  O_PASS(2); SBAR(); o_stage<8>(c, acc);
  O_PASS(3);
  TS(z2);
  unsigned pc = 0;
  asm volatile("global_load_dword %0, %1, off sc1" : "+v"(pc) : "v"(cnt + 16 * j) : "memory");
  SBAR(); o_stage<11>(c, acc);
  TS(z3);
  wait_vm<0>(); asm volatile("" : "+v"(pc));
  if (w == 0 && __builtin_amdgcn_readfirstlane(pc) < 12u) { while (__builtin_amdgcn_readfirstlane(__hip_atomic_load(cnt + 16 * j, __ATOMIC_RELAXED, __HIP_MEMORY_SCOPE_AGENT)) < 12u) __builtin_amdgcn_s_sleep(1); }
  LBAR();
  TS(z4);
  {
    const int n = g ? 2 : 1, j0 = g ? 2 : 1;
#pragma unroll
    for (int k = 0; k < 2; ++k) {
      if (k < n) {
#pragma unroll
        for (int qd = 0; qd < 4; ++qd) {
          const f32x4 v = __builtin_bit_cast(f32x4, __builtin_amdgcn_raw_buffer_load_b128(c.sS, c.voff + (unsigned)(qd * 1024), (unsigned)(((j + j0 + k) & 3) * 65536 + j * 16384 + rb * 4096), 16));
#pragma unroll
          for (int e = 0; e < 4; ++e) acc[4 * qd + e] += v[e];
        }
      }
    }
  }
  if (g == 1) o_park(red, acc);
  LBAR();
  if (g == 0) {
    o_addlds(acc, red);
    u16* op = out + (size_t)(128 * mi + 32 * rb + r32) * 512 + 128 * ni + 32 * j + 8 * hh;
    *(u32x4*)(op) = pack8<0>(acc);
    *(u32x4*)(op + 16) = pack8<1>(acc);
  }
#undef O_PUBLISH
#undef O_SIGNAL
#undef O_PASS
}

extern "C" void kernel_launch(void* const* d_in, const int* in_sizes, int n_in, void* d_out, int out_size, void* d_ws, size_t ws_size, hipStream_t stream) {
  const u16* x = (const u16*)d_in[0]; const u16* ckv = (const u16*)d_in[1]; const u16* wq = (const u16*)d_in[2];
  const u16* wuk = (const u16*)d_in[3]; const u16* wuv = (const u16*)d_in[4]; const u16* wo_a = (const u16*)d_in[5];
  const u16* wqkv = (const u16*)d_in[7]; const u16* wphi = (const u16*)d_in[8]; const u16* wo_b = (const u16*)d_in[9]; const u16* gatew = (const u16*)d_in[10];
  (void)hipFuncSetAttribute((const void*)kF, hipFuncAttributeMaxDynamicSharedMemorySize, 163840);
  (void)hipFuncSetAttribute((const void*)kO, hipFuncAttributeMaxDynamicSharedMemorySize, 139264);
  kF<<<256, 512, 163840, stream>>>(x, ckv, wq, wuk, wuv, wqkv, wphi, gatew, wo_a, wo_b, (char*)d_ws);
  kO<<<256, 512, 139264, stream>>>(wo_a, wo_b, (char*)d_ws, (u16*)d_out);
}
```

```cpp
#define PRIO_M
#include <hip/hip_runtime.h>
#include <stdint.h>

typedef unsigned short u16;
typedef unsigned long long u64;
typedef __bf16 bf16x8 __attribute__((ext_vector_type(8)));
typedef __bf16 bf16x2 __attribute__((ext_vector_type(2)));
typedef float f32x2 __attribute__((ext_vector_type(2)));
typedef float f32x4 __attribute__((ext_vector_type(4)));
typedef float f32x16 __attribute__((ext_vector_type(16)));
typedef unsigned u32x2 __attribute__((ext_vector_type(2)));
typedef unsigned u32x4 __attribute__((ext_vector_type(4)));
typedef short s16x4 __attribute__((ext_vector_type(4)));
typedef short s16x8 __attribute__((ext_vector_type(8)));
typedef __amdgpu_buffer_rsrc_t srd_t;
#define LAS __attribute__((address_space(3)))
#define DI __device__ __forceinline__
#define SBAR() __builtin_amdgcn_sched_barrier(0)
#define MFMA(a, b, c) __builtin_amdgcn_mfma_f32_32x32x16_bf16((a), (b), (c), 0, 0, 0)

constexpr size_t MB = 1024 * 1024;
constexpr size_t WS_KF = 0;
constexpr size_t WS_VF = 16 * MB;
constexpr size_t WS_QF = 32 * MB;
constexpr size_t WS_CF = 48 * MB;
constexpr size_t WS_PQ = 68 * MB;
constexpr size_t WS_KVP = 72 * MB;
constexpr size_t WS_GW = 73 * MB;
constexpr size_t WS_ST = 74 * MB;
constexpr size_t WS_SLAB = 0;
constexpr size_t WS_CNT = 92 * MB;
constexpr size_t WS_FLG = 93 * MB;
constexpr size_t WS_DBG = 100 * MB;

constexpr float QSCALE = 0.08838834764831845f * 1.4426950408889634f;
constexpr float THR = 8.0f;

DI unsigned cvtpk(float lo, float hi) { f32x2 v = {lo, hi}; bf16x2 b = __builtin_convertvector(v, bf16x2); return __builtin_bit_cast(unsigned, b); }
template <int S> DI u32x4 pack8(const f32x16& x) {
  return (u32x4){cvtpk(x[8 * S + 0], x[8 * S + 1]), cvtpk(x[8 * S + 2], x[8 * S + 3]), cvtpk(x[8 * S + 4], x[8 * S + 5]), cvtpk(x[8 * S + 6], x[8 * S + 7])};
}
DI bf16x8 asbf(u32x4 v) { return __builtin_bit_cast(bf16x8, v); }
DI float swap32_max(float v) { auto rr = __builtin_amdgcn_permlane32_swap(__float_as_uint(v), __float_as_uint(v), false, false); return fmaxf(__uint_as_float(rr[0]), __uint_as_float(rr[1])); }
DI float swap32_add(float v) { auto rr = __builtin_amdgcn_permlane32_swap(__float_as_uint(v), __float_as_uint(v), false, false); return __uint_as_float(rr[0]) + __uint_as_float(rr[1]); }
DI float swap32_lo(float v) { auto rr = __builtin_amdgcn_permlane32_swap(__float_as_uint(v), __float_as_uint(v), false, false); return __uint_as_float(rr[0]); }
DI float bf16round(float x) { return __uint_as_float(cvtpk(x, 0.f) << 16); }
DI srd_t mksrd(const void* p, unsigned bytes) { return __builtin_amdgcn_make_buffer_rsrc((void*)p, (short)0, (int)bytes, 0x00020000); }
DI unsigned rfl(unsigned x) { return (unsigned)__builtin_amdgcn_readfirstlane((int)x); }
template <int OFF> DI void dma16o(unsigned m0v, unsigned voff, srd_t srd, unsigned soff) {
  asm volatile("s_mov_b32 m0, %0\n\ts_nop 0\n\tbuffer_load_dwordx4 %1, %2, %3 offen offset:%4 sc1 lds" :: "s"(m0v), "v"(voff), "s"(srd), "s"(soff), "i"(OFF) : "memory");
}
template <unsigned MOFF, unsigned SOFF> DI void dma16s(unsigned mb, unsigned voff, srd_t srd, unsigned sb) {
  unsigned t;
  asm volatile("s_add_u32 m0, %1, %5\n\ts_add_u32 %0, %4, %6\n\tbuffer_load_dwordx4 %2, %3, %0 offen sc1 lds" : "=&s"(t) : "s"(mb), "v"(voff), "s"(srd), "s"(sb), "i"(MOFF), "i"(SOFF) : "memory", "scc");
}
template <int OFF> DI void dma16n(unsigned voff, srd_t srd, unsigned soff) {
  asm volatile("buffer_load_dwordx4 %0, %1, %2 offen offset:%3 sc1 lds" :: "v"(voff), "s"(srd), "s"(soff), "i"(OFF) : "memory");
}
template <unsigned MOFF, unsigned SOFF> DI void dma16c(unsigned mb, unsigned voff, srd_t srd, unsigned sb) {
  unsigned t;
  asm volatile("s_add_u32 m0, %1, %5\n\ts_add_u32 %0, %4, %6\n\tbuffer_load_dwordx4 %2, %3, %0 offen lds" : "=&s"(t) : "s"(mb), "v"(voff), "s"(srd), "s"(sb), "i"(MOFF), "i"(SOFF) : "memory", "scc");
}
template <unsigned MOFF, unsigned SOFF> DI void dma16m(unsigned mb, unsigned voff, srd_t srd, unsigned sb) {
  unsigned t;
  asm volatile("s_add_u32 m0, %1, %5\n\ts_add_u32 %0, %4, %6\n\ts_mov_b32 exec_hi, 0x33333333\n\tbuffer_load_dwordx4 %2, %3, %0 offen lds\n\ts_mov_b32 exec_hi, -1"
               : "=&s"(t) : "s"(mb), "v"(voff), "s"(srd), "s"(sb), "i"(MOFF), "i"(SOFF) : "memory", "scc");
}
template <int N> DI void wait_bar() { asm volatile("s_waitcnt vmcnt(%0) lgkmcnt(0)\n\ts_barrier" :: "i"(N) : "memory"); }
template <int N> DI void wait_vm() { asm volatile("s_waitcnt vmcnt(%0)" :: "i"(N) : "memory"); }
#define LBAR() asm volatile("s_waitcnt lgkmcnt(0)\n\ts_barrier" ::: "memory")
#define LWAIT() asm volatile("s_waitcnt lgkmcnt(0)" ::: "memory")
#define NOP16() asm volatile("s_nop 7\n\ts_nop 7" ::: "memory")
DI bf16x8 ldsfrag(LAS const char* p) { return *(LAS const bf16x8*)p; }
DI bf16x8 trfrag(LAS const char* p0, LAS const char* p1) {
  s16x4 a = __builtin_amdgcn_ds_read_tr16_b64_v4i16((LAS s16x4*)p0);
  s16x4 b = __builtin_amdgcn_ds_read_tr16_b64_v4i16((LAS s16x4*)p1);
  return __builtin_bit_cast(bf16x8, __builtin_shufflevector(a, b, 0, 1, 2, 3, 4, 5, 6, 7));
}
DI void st128(u32x4 v, srd_t s, unsigned vo, unsigned so) { __builtin_amdgcn_raw_buffer_store_b128(v, s, vo + so, 0, 0); }
DI void st128_wt(u32x4 v, srd_t s, unsigned vo, unsigned so) { __builtin_amdgcn_raw_buffer_store_b128(v, s, vo + so, 0, 16); }
#define TS(x)

#define KVAUX 16
struct PCtx {
  srd_t sA1, sA2, sUK, sUV, sWQ, sWB, sWP, sK, sV;
  unsigned voffA[2], voffB, voffS, voff;
  unsigned soA1, soA2, soB, soS, soKa, mK0, mA, mB2, m2;
  unsigned gv[8];
  u32x2 fl; u64 tag; const u64* flg;
  u32x4 wp;
  unsigned soPW, soPS, soPX;
  u32x4 kvp[8]; unsigned kvo;
  int w, lane, h;
  LAS char* L;
};
#define TS3(x) do { SBAR(); asm volatile("s_memtime %0\n\ts_waitcnt lgkmcnt(0)" : "=s"(x) :: "memory"); SBAR(); } while (0)
template <int T, int I> DI void p_pieceA(const PCtx& c) {
  if constexpr (T < 24) {
    constexpr unsigned st = (unsigned)(T % 3) * 32768u, kt = (unsigned)(T & 7);
    if constexpr (T < 16) dma16c<st + I * 1024u, I * 8192u + kt * 128u>(c.mA, c.voffA[I & 1], c.sA1, c.soA1);
    else                  dma16c<st + I * 1024u, I * 8192u + kt * 128u>(c.mA, c.voffA[I & 1], c.sA2, c.soA2);
  }
}
template <int T, int I> DI void p_pieceB(const PCtx& c) {
  constexpr unsigned st = 98304u + (unsigned)(T & 1) * 32768u, kt = (unsigned)(T & 7);
  if constexpr (T < 16) {
    if constexpr (I < 2) dma16c<st + I * 1024u, kt * 524288u + I * 128u>(c.m2, c.voffB, T < 8 ? c.sUK : c.sUV, c.soB);
  } else if constexpr (T < 24) {
    if constexpr (I < 2) dma16c<st + I * 1024u, kt * 524288u + I * 128u>(c.mB2, c.voffB, c.sWQ, c.soB);
    else if constexpr (I == 2) dma16m<st + 2048u, kt * 196608u>(c.mB2, c.voffS, c.sWB, c.soS);
    else if (c.w == 7) *(LAS u16*)(c.L + st + ((c.lane >> 3) * 6 + 5) * 512 + (c.lane & 7) * 64 + 32) = (u16)c.gv[kt];
  }
}
template <int I, bool ISV> DI void p_kvstore(const PCtx& c) {
  if constexpr (I >= 0 && I < 8) {
    constexpr int mt = I >> 2, nt = (I >> 1) & 1, sh = I & 1;
    constexpr unsigned off = (unsigned)((ISV ? nt * 4 + 2 * mt + sh : mt * 8 + 2 * nt + sh) * 1024);
    __builtin_amdgcn_raw_buffer_store_b128(c.kvp[I], ISV ? c.sV : c.sK, c.kvo + off, 0, KVAUX);
  }
}
#define WARM1(VO, SRD, SO, OFF) dma16c<114688u, (OFF)>(c.m2, VO, SRD, SO)
template <int I> DI void p_warm(PCtx& c) {
  if constexpr (I == 0) { WARM1(c.voff, c.sA1, c.soPX, 0); WARM1(c.voffB, c.sUK, c.soPW, 0); WARM1(c.voffB, c.sUK, c.soPW, 128); }
  if constexpr (I == 1) { WARM1(c.voffB, c.sUV, c.soPW, 0); WARM1(c.voffB, c.sUV, c.soPW, 128); }
  if constexpr (I == 2) { WARM1(c.voff, c.sA2, c.soPX, 0); WARM1(c.voffB, c.sWQ, c.soPW, 0); }
  if constexpr (I == 3) { WARM1(c.voffB, c.sWQ, c.soPW, 128); WARM1(c.voffS, c.sWB, c.soPS, 0); }
}
#undef WARM1
DI void poll_spin(const u64* f, u64 tag, int lane) {
  while (!__all(__hip_atomic_load(f + (lane & 7), __ATOMIC_RELAXED, __HIP_MEMORY_SCOPE_AGENT) == tag)) __builtin_amdgcn_s_sleep(2);
}
DI void poll_issue(u32x2& fl, const u64* f, int lane) { asm volatile("global_load_dwordx2 %0, %1, off sc1" : "+v"(fl) : "v"(f + (lane & 7)) : "memory"); }
DI void poll_check(u32x2& fl, const u64* f, u64 tag, int lane) {
  asm volatile("" : "+v"(fl));
  if (!__all(fl[0] == (unsigned)tag && fl[1] == (unsigned)(tag >> 32))) poll_spin(f, tag, lane);
  asm volatile("" ::: "memory");
}
template <int B> DI void p_hook(PCtx& c) {
  if constexpr (B == 17) { if (c.w == 0) dma16c<155648u, 0u>(c.mA, c.voff, c.sWP, (unsigned)c.h * 512u); }
  if constexpr (B == 21) {
    if (c.w == 0) { if (c.lane == 0) __hip_atomic_store((u64*)c.flg + (blockIdx.x >> 5), c.tag, __ATOMIC_RELAXED, __HIP_MEMORY_SCOPE_AGENT); }
    {
      const int r32 = c.lane & 31, hh = c.lane >> 5; LAS const u16* wb = (LAS const u16*)(c.L + 155648) + (r32 & 15); const unsigned sg = r32 >= 16 ? 0x80008000u : 0u;
#pragma unroll
      for (int jj = 0; jj < 4; ++jj) { const int d0 = 8 * (jj >> 1) + 4 * hh + 2 * (jj & 1); c.wp[jj] = ((unsigned)wb[d0 * 16] | ((unsigned)wb[(d0 + 1) * 16] << 16)) ^ sg; }
    }
  }
  if constexpr (B == 22) { if (c.w == 0) poll_issue(c.fl, c.flg, c.lane); }
  if constexpr (B == 23) { if (c.w == 0) poll_check(c.fl, c.flg, c.tag, c.lane); }
  if constexpr (B == 24) {
    dma16s<0u, 0u>(c.mK0, c.voff, c.sK, c.soKa); dma16s<1024u, 1024u>(c.mK0, c.voff, c.sK, c.soKa);
    dma16s<2048u, 2048u>(c.mK0, c.voff, c.sK, c.soKa); dma16s<3072u, 3072u>(c.mK0, c.voff, c.sK, c.soKa);
  }
}

template <int PASS, int MT, int NT, int NSUB, bool TRANS>
struct PPass {
  PCtx& c; f32x16 (&acc)[MT][NT];
  unsigned aB[2][4], bB;
  bf16x8 fa[2][MT], fb[2][NT];
  template <int SET, int T, int KS> DI void load() {
    constexpr int bo = (T & 1) * 32768 + KS * (2 * NSUB * 512), sl = T % 3;
#pragma unroll
    for (int nt = 0; nt < NT; ++nt) fb[SET][nt] = trfrag(c.L + bB + bo + nt * 512, c.L + bB + bo + nt * 512 + 256);
#pragma unroll
    for (int mt = 0; mt < MT; ++mt) fa[SET][mt] = ldsfrag(c.L + aB[sl >> 1][KS] + (sl & 1) * 32768 + mt * 4096);
  }
  template <int SET, int I> DI void mma1() {
    if constexpr (I < MT * NT) {
      constexpr int mt = I / NT, nt = I % NT;
      acc[mt][nt] = TRANS ? MFMA(fb[SET][nt], fa[SET][mt], acc[mt][nt]) : MFMA(fa[SET][mt], fb[SET][nt], acc[mt][nt]);
    }
  }
  template <int SET, int T, int I0, int S0> DI void mmaA() {
    mma1<SET, 0>(); SBAR(); p_pieceA<T, I0>(c); SBAR(); mma1<SET, 1>(); SBAR(); p_kvstore<S0, PASS == 2>(c); SBAR(); mma1<SET, 2>(); SBAR(); p_pieceA<T, I0 + 1>(c); SBAR();
    mma1<SET, 3>(); mma1<SET, 4>(); mma1<SET, 5>();
  }
  template <int t> DI void tile() {
    constexpr int T = PASS * 8 + t;
    constexpr int SA = PASS == 1 ? t : (PASS == 2 && t < 4 ? 2 * t : -1), SB = PASS == 2 && t < 4 ? 2 * t + 1 : -1;
    constexpr int NW = T == 0 ? 5 : (T < 3 ? 2 : 0);
    constexpr int NST = (SA >= 0 ? 1 : 0) + (SB >= 0 ? 1 : 0) + NW;
    SBAR(); load<1, T, 1>(); SBAR(); mmaA<0, T + 2, 0, SA>();
    SBAR(); load<0, T, 2>(); SBAR(); mmaA<1, T + 2, 2, -1>();
    SBAR(); load<1, T, 3>(); SBAR(); if constexpr (T < 3) p_warm<T + 1>(c); SBAR(); mmaA<0, 24, 0, SB>();
    SBAR();
    if constexpr (T == 23) LBAR(); else if constexpr (T == 22) wait_bar<0>(); else wait_bar<4 + NST>();
    SBAR();
    if constexpr (t < 7) load<0, T + 1, 0>();
    SBAR();
    mma1<1, 0>(); p_pieceB<T + 2, 0>(c); SBAR();
    mma1<1, 1>(); p_pieceB<T + 2, 1>(c); SBAR();
    mma1<1, 2>(); p_pieceB<T + 2, 2>(c); SBAR();
    mma1<1, 3>(); p_pieceB<T + 2, 3>(c); SBAR();
    mma1<1, 4>(); mma1<1, 5>(); SBAR();
    p_hook<T + 1>(c); SBAR();
  }
  DI void run(unsigned arow0, unsigned nt0) {
    const int lane = c.lane, r = lane & 31, hh = lane >> 5, i16 = lane & 15;
#pragma unroll
    for (int ks = 0; ks < 4; ++ks) { aB[0][ks] = (arow0 + r) * 128u + (unsigned)(((2 * ks + hh) ^ ((r >> 1) & 7)) * 16); aB[1][ks] = aB[0][ks] + 65536u; asm volatile("" : "+v"(aB[1][ks])); }
    bB = 98304u + (unsigned)(hh * NSUB + nt0) * 512u + (unsigned)((i16 >> 2) * 64 + ((lane >> 4) & 1) * 32 + (i16 & 3) * 8); asm volatile("" : "+v"(bB));
#pragma unroll
    for (int mt = 0; mt < MT; ++mt)
#pragma unroll
      for (int nt = 0; nt < NT; ++nt) acc[mt][nt] = (f32x16){};
    load<0, PASS * 8, 0>();
    tile<0>(); tile<1>(); tile<2>(); tile<3>(); tile<4>(); tile<5>(); tile<6>(); tile<7>();
  }
};
template <int PASS, int MT, int NT, int NSUB, bool TRANS>
DI void p_pass(PCtx& c, f32x16 (&acc)[MT][NT], unsigned arow0, unsigned nt0) { PPass<PASS, MT, NT, NSUB, TRANS> p{c, acc}; p.run(arow0, nt0); }

DI void softmax2(f32x16& x) {
#pragma unroll
  for (int s = 0; s < 2; ++s) {
    float mx = fmaxf(fmaxf(fmaxf(x[8 * s], x[8 * s + 1]), fmaxf(x[8 * s + 2], x[8 * s + 3])), fmaxf(fmaxf(x[8 * s + 4], x[8 * s + 5]), fmaxf(x[8 * s + 6], x[8 * s + 7])));
    mx = swap32_max(mx);
    const float cc = -mx * 1.4426950408889634f;
    float sum = 0.f;
#pragma unroll
    for (int j = 0; j < 8; ++j) { x[8 * s + j] = __builtin_amdgcn_exp2f(__builtin_fmaf(x[8 * s + j], 1.4426950408889634f, cc)); sum += x[8 * s + j]; }
    const float inv = __builtin_amdgcn_rcpf(swap32_add(sum));
#pragma unroll
    for (int j = 0; j < 8; ++j) x[8 * s + j] *= inv;
  }
}

DI void mfma_acc(f32x16& c, bf16x8 a, bf16x8 b) { asm volatile("v_mfma_f32_32x32x16_bf16 %0, %1, %2, %0" : "+v"(c) : "v"(a), "v"(b)); }
DI void mfma_acc2(f32x16& c, bf16x8 a, bf16x8 b, bf16x8 nx) { asm volatile("v_mfma_f32_32x32x16_bf16 %0, %1, %2, %0" : "+v"(c) : "v"(a), "v"(b), "v"(nx)); }
DI void mfma_init2(f32x16& c, bf16x8 a, bf16x8 b, const f32x16& ci, bf16x8 nx) { asm volatile("v_mfma_f32_32x32x16_bf16 %0, %1, %2, %3" : "=&v"(c) : "v"(a), "v"(b), "v"(ci), "v"(nx)); }
DI float sum2(unsigned pk, float acc) { return __builtin_amdgcn_fdot2_f32_bf16(__builtin_bit_cast(bf16x2, pk), __builtin_bit_cast(bf16x2, 0x3f803f80u), acc, false); }
DI float sum8(bf16x8 p, float acc) { const u32x4 u = __builtin_bit_cast(u32x4, p); return sum2(u[3], sum2(u[2], sum2(u[1], sum2(u[0], acc)))); }
DI float max3f(float a, float b, float c) { float r; asm("v_max3_f32 %0, %1, %2, %3" : "=v"(r) : "v"(a), "v"(b), "v"(c)); return r; }
DI float max2f(float a, float b) { float r; asm("v_max_f32_e32 %0, %1, %2" : "=v"(r) : "v"(a), "v"(b)); return r; }
#define TS2X(a)
#define TS2V()
#define STP_PARAM
#define STP_ARG
#define TS2(acc)
#define NMV 4
#define TRAIL 4
#define ADIST 6
template <bool DOQK, bool DOPV>
DI void a_mphase(LAS const char* kp, LAS const char* vp, const bf16x8 (&q)[8], bf16x8 (&p)[4], f32x16 (&S)[2], f32x16 (&O)[4], const f32x16& negm, float& l, float& pm0,
                 unsigned mV, unsigned voff, srd_t sV, unsigned oV STP_PARAM) {
  constexpr int N = (DOQK ? 16 : 0) + (DOPV ? 16 : 0), D = ADIST;
  bf16x8 f[N];
  unsigned pw[8]; float ls = 0.f, e0 = 0.f;
#define A_ISQK(i) (DOQK && (!DOPV || (((i) >> 3) & 1) == 0))
#define A_IX(i) ((DOPV && DOQK) ? (((i) >> 4) * 8 + ((i) & 7)) : (i))
#define A_LD(i) do { if constexpr ((i) < N) { if constexpr (A_ISQK(i)) f[i] = ldsfrag(kp + A_IX(i) * 1024); \
                     else f[i] = ldsfrag(vp + ((A_IX(i) & 3) * 4 + (A_IX(i) >> 2)) * 1024); } } while (0)
#define A_MM(i) do { if constexpr (A_ISQK(i)) { if constexpr ((A_IX(i) & 7) == 0) mfma_init2(S[A_IX(i) >> 3], f[i], q[0], negm, f[(i) + 1]); \
                       else if constexpr ((i) & 1) mfma_acc(S[A_IX(i) >> 3], f[i], q[A_IX(i) & 7]); else mfma_acc2(S[A_IX(i) >> 3], f[i], q[A_IX(i) & 7], f[((i) + 1) % N]); } \
                     else if constexpr ((i) & 1) mfma_acc(O[A_IX(i) & 3], f[i], p[A_IX(i) >> 2]); else mfma_acc2(O[A_IX(i) & 3], f[i], p[A_IX(i) >> 2], f[((i) + 1) % N]); } while (0)
#define A_SM(e) do { const float x_ = __builtin_amdgcn_exp2f(S[1][(e) & 15]); ls += x_; \
                     if constexpr ((e) & 1) { pw[((e) & 15) >> 1] = cvtpk(e0, x_); asm volatile("" : "+v"(pw[((e) & 15) >> 1]), "+v"(ls)); } else { e0 = x_; asm volatile("" : "+v"(e0), "+v"(ls)); } \
                     if constexpr ((e) == 7) p[2] = asbf((u32x4){pw[0], pw[1], pw[2], pw[3]}); if constexpr ((e) == 15) p[3] = asbf((u32x4){pw[4], pw[5], pw[6], pw[7]}); } while (0)
#define A_MX(j) do { if constexpr ((j) == 0) pm0 = max2f(S[0][0], S[0][1]); else pm0 = max3f(pm0, S[0][2 * ((j) & 7)], S[0][2 * ((j) & 7) + 1]); asm volatile("" : "+v"(pm0)); } while (0)
#define A_STEP(i) do { if constexpr ((i) < N) { A_LD((i) + D); if constexpr ((i) == N - TRAIL) { SBAR(); TS2(a); LBAR(); TS2(b); SBAR(); } A_MM(i); \
                     if constexpr (DOQK && DOPV && (i) < 16) A_SM(i); \
                     if constexpr (DOQK && DOPV && (i) >= 16 && (i) < 24) A_MX((i) - 16); \
                     if constexpr (DOQK && DOPV && NMV >= 4 && (i) == 17) dma16o<0>(mV, voff, sV, oV); \
                     if constexpr (DOQK && DOPV && NMV >= 4 && (i) == 20) dma16n<1024>(voff, sV, oV); \
                     if constexpr (DOQK && DOPV && NMV >= 4 && (i) == 23) dma16n<2048>(voff, sV, oV); \
                     if constexpr (DOQK && DOPV && NMV >= 4 && (i) == 25) dma16n<3072>(voff, sV, oV); \
                     if constexpr (!DOQK && (i) < 8) { A_SM(2 * (i)); A_SM(2 * (i) + 1); } SBAR(); } } while (0)
  SBAR();
  __builtin_amdgcn_s_setprio(1);
  A_LD(0); A_LD(1); A_LD(2); A_LD(3); if constexpr (D > 4) { A_LD(4); A_LD(5); } SBAR();
  A_STEP(0); A_STEP(1); A_STEP(2); A_STEP(3); A_STEP(4); A_STEP(5); A_STEP(6); A_STEP(7);
  A_STEP(8); A_STEP(9); A_STEP(10); A_STEP(11); A_STEP(12); A_STEP(13); A_STEP(14); A_STEP(15);
  A_STEP(16); A_STEP(17); A_STEP(18); A_STEP(19); A_STEP(20); A_STEP(21); A_STEP(22); A_STEP(23);
  A_STEP(24); A_STEP(25); A_STEP(26); A_STEP(27); A_STEP(28); A_STEP(29); A_STEP(30); A_STEP(31);
  if constexpr (DOPV) l += ls;
  if constexpr (!DOPV) { NOP16(); SBAR(); A_MX(0); A_MX(1); A_MX(2); A_MX(3); A_MX(4); A_MX(5); A_MX(6); A_MX(7); }
  __builtin_amdgcn_s_setprio(0);
  TS2X(c);
#undef A_ISQK
#undef A_IX
#undef A_LD
#undef A_MM
#undef A_SM
#undef A_MX
#undef A_STEP
}

DI void a_finish(const f32x16& a0, const f32x16& a1, float ma, float la, LAS const char* xr, float mb, float lb, float gate, srd_t sC, unsigned voff, unsigned soff) {
  const float mt = fmaxf(ma, mb);
  const float fa = __builtin_amdgcn_exp2f(ma - mt), fb = __builtin_amdgcn_exp2f(mb - mt);
  const float sc = gate / (la * fa + lb * fb);
  const float wa = fa * sc, wb = fb * sc;
#pragma unroll
  for (int t = 0; t < 2; ++t) {
    const f32x16& a = t ? a1 : a0;
#pragma unroll
    for (int s = 0; s < 2; ++s) {
      const f32x4 b0 = *(LAS const f32x4*)(xr + t * 4096 + s * 2048), b1 = *(LAS const f32x4*)(xr + t * 4096 + s * 2048 + 1024);
      const u32x4 o = {cvtpk(a[8 * s + 0] * wa + b0[0] * wb, a[8 * s + 1] * wa + b0[1] * wb), cvtpk(a[8 * s + 2] * wa + b0[2] * wb, a[8 * s + 3] * wa + b0[3] * wb),
                       cvtpk(a[8 * s + 4] * wa + b1[0] * wb, a[8 * s + 5] * wa + b1[1] * wb), cvtpk(a[8 * s + 6] * wa + b1[2] * wb, a[8 * s + 7] * wa + b1[3] * wb)};
      st128(o, sC, voff + (unsigned)((2 * t + s) * 1024), soff);
      SBAR();
    }
  }
}
DI void a_send(const f32x16& a0, const f32x16& a1, LAS char* xr) {
#pragma unroll
  for (int g = 0; g < 4; ++g) {
    *(LAS f32x4*)(xr + g * 1024) = (f32x4){a0[4 * g], a0[4 * g + 1], a0[4 * g + 2], a0[4 * g + 3]};
    *(LAS f32x4*)(xr + 4096 + g * 1024) = (f32x4){a1[4 * g], a1[4 * g + 1], a1[4 * g + 2], a1[4 * g + 3]};
  }
}

__global__ __launch_bounds__(512, 2) void kF(const u16* __restrict__ x, const u16* __restrict__ ckv, const u16* __restrict__ wq_, const u16* __restrict__ wuk,
                                             const u16* __restrict__ wuv, const u16* __restrict__ wqkv, const u16* __restrict__ wphi, const u16* __restrict__ gatew,
                                             const u16* __restrict__ wo_a, const u16* __restrict__ wo_b, char* __restrict__ ws) {
  extern __shared__ __attribute__((aligned(16))) char lds[];
  LAS char* L = (LAS char*)lds; const unsigned lds0 = (unsigned)(uintptr_t)L;
  const int tid = threadIdx.x, lane = tid & 63, w = __builtin_amdgcn_readfirstlane(tid >> 6);
  const int b = blockIdx.x, h = (b & 7) * 4 + ((b >> 3) & 3), r = b >> 5;
  const int g = w >> 2, wq = w & 3, hh = lane >> 5, r32 = lane & 31;
  const int X = 15 - r, Y = r, nX = 16 - r;
  constexpr int NS = 17;
  const unsigned voff = (unsigned)lane * 16u;
  TS(t0);
  bf16x8 q[8];
  u64 tag; const u64* flg = (const u64*)(ws + WS_FLG + (size_t)h * 256);
  const srd_t sK = mksrd(ws + WS_KF + (size_t)h * 32 * 16384, 32u * 16384u), sV = mksrd(ws + WS_VF + (size_t)h * 32 * 16384, 32u * 16384u);
  const srd_t sW = mksrd(ws + WS_QF, (unsigned)(WS_DBG - WS_QF));
  const unsigned ringK = rfl(lds0 + (g ? 98304u : 32768u) + (unsigned)wq * 4096u), ringV = rfl(lds0 + (g ? 0u : 65536u) + (unsigned)wq * 4096u);
  {
    PCtx c;
    c.fl = (u32x2){0u, 0u}; c.wp = (u32x4){0u, 0u, 0u, 0u};
    c.L = L; c.w = w; c.lane = lane; c.h = h; c.voff = voff; c.flg = flg; c.sK = sK; c.sV = sV;
    c.soA1 = rfl((unsigned)(256 * r + 32 * w) * 1024u);
    c.soA2 = rfl((unsigned)(w < 4 ? 128 * X + 32 * w : 128 * Y + 32 * (w - 4)) * 1024u);
    c.soB = rfl((unsigned)(8 * w) * 8192u + (unsigned)h * 256u);
    c.soS = rfl((unsigned)(8 * w) * 3072u + (unsigned)h * 32u);
    c.soPW = rfl((unsigned)(64 * r + 8 * w) * 8192u + (unsigned)h * 256u); c.soPS = rfl((unsigned)(64 * r + 8 * w) * 3072u + (unsigned)h * 32u);
    c.soPX = rfl((unsigned)(256 * r + 8 * h + w) * 1024u);
    c.soKa = rfl((unsigned)(g * 16384 + wq * 4096)); c.mK0 = ringK;
    c.mA = rfl(lds0 + (unsigned)w * 4096u); c.mB2 = rfl(lds0 + (unsigned)w * 3072u); c.m2 = rfl(lds0 + (unsigned)w * 2048u);
    c.sA1 = mksrd(ckv, 2048u * 1024u); c.sA2 = mksrd(x, 2048u * 1024u);
    c.sUK = mksrd(wuk, 512u * 8192u); c.sUV = mksrd(wuv, 512u * 8192u); c.sWQ = mksrd(wq_, 512u * 8192u); c.sWB = mksrd(wqkv, 512u * 3072u); c.sWP = mksrd(wphi, 32u * 512u);
#pragma unroll
    for (int k = 0; k < 2; ++k) c.voffA[k] = (unsigned)((lane >> 3) * 1024 + (((lane & 7) ^ ((4 * k + (lane >> 4)) & 7)) * 16));
    c.voffB = (unsigned)(((lane >> 2) & 7) * 8192 + (lane >> 5) * 64 + (lane & 3) * 16);
    { const int ch = lane & 3, sub = lane >> 5;
      c.voffS = (unsigned)(((lane >> 2) & 7) * 3072 + (sub == 0 ? (ch < 2 ? ch * 16 : 1024 + (ch - 2) * 16) : 2048 + (ch & 1) * 16)); }
    if (b == 0 && tid < 256) *(unsigned*)(ws + WS_CNT + tid * 64) = 0u;
    u32x2 ep;
    asm volatile("global_load_dwordx2 %0, %1, off sc1" : "=v"(ep) : "v"(flg + 16) : "memory");
#pragma unroll
    for (int k = 0; k < 8; ++k) asm volatile("global_load_ushort %0, %1, off offset:%2" : "=v"(c.gv[k]) : "v"(gatew + lane), "i"(k * 128) : "memory");
    p_pieceA<0, 0>(c); p_pieceA<0, 1>(c); p_pieceA<0, 2>(c); p_pieceA<0, 3>(c);
    p_pieceB<0, 0>(c); p_pieceB<0, 1>(c);
    p_pieceB<1, 0>(c); p_pieceB<1, 1>(c);
    p_pieceA<1, 0>(c); p_pieceA<1, 1>(c); p_pieceA<1, 2>(c); p_pieceA<1, 3>(c);
    p_warm<0>(c);
    wait_bar<9>();
    asm volatile("" : "+v"(ep), "+v"(c.gv[0]), "+v"(c.gv[1]), "+v"(c.gv[2]), "+v"(c.gv[3]), "+v"(c.gv[4]), "+v"(c.gv[5]), "+v"(c.gv[6]), "+v"(c.gv[7]));
    tag = (((u64)rfl(ep[1]) << 32) | rfl(ep[0])) + 1ull; c.tag = tag;
    {
      f32x16 acc[2][2];
      const int wm = w >> 1, wn = w & 1;
      p_pass<0, 2, 2, 4, true>(c, acc, 64u * wm, 2u * wn);
#pragma unroll
      for (int i = 0; i < 4; ++i) { c.kvp[2 * i] = pack8<0>(acc[i >> 1][i & 1]); c.kvp[2 * i + 1] = pack8<1>(acc[i >> 1][i & 1]); }
      c.kvo = voff + (unsigned)((4 * r + wm) * 16384 + 4 * wn * 1024);
      p_pass<1, 2, 2, 4, false>(c, acc, 64u * wm, 2u * wn);
#pragma unroll
      for (int i = 0; i < 4; ++i) { c.kvp[2 * i] = pack8<0>(acc[i >> 1][i & 1]); c.kvp[2 * i + 1] = pack8<1>(acc[i >> 1][i & 1]); }
      c.kvo = voff + (unsigned)((4 * r + wm) * 16384 + 8 * wn * 1024);
    }
    TS(t1);
    f32x16 acc[2][3];
    p_pass<2, 2, 3, 6, true>(c, acc, 64u * wq, 3u * g);
    TS(t2);
    if (wq < 2) {
#pragma unroll
      for (int mt = 0; mt < 2; ++mt)
#pragma unroll
        for (int nt = 0; nt < 3; ++nt)
          if (g == 0 || nt == 0) {
            const f32x16 v = acc[mt][nt] * QSCALE; LAS char* qd = L + 131072 + ((2 * wq + mt) * 8 + 2 * (3 * g + nt)) * 1024 + lane * 16;
            *(LAS u32x4*)qd = pack8<0>(v); *(LAS u32x4*)(qd + 1024) = pack8<1>(v);
          }
    }
    LAS char* Ik = L + (g ? 16384 : 81920) + wq * 4096; LAS char* Iv = Ik + 2048;
    u32x4 hq, hk, hv; float gl;
    if (g == 1) {
      LAS char* ho = L + 81920 + wq * 4096 + lane * 16;
      *(LAS u32x4*)ho = pack8<0>(acc[1][1]); *(LAS u32x4*)(ho + 1024) = pack8<1>(acc[1][1]); *(LAS u32x4*)(ho + 2048) = pack8<0>(acc[1][2]);
      *(LAS float*)(L + 81920 + wq * 4096 + 3072 + lane * 4) = acc[1][2][8];
      hq = pack8<0>(acc[0][1]); hk = pack8<1>(acc[0][1]); hv = pack8<0>(acc[0][2]); gl = acc[0][2][8];
    }
    TS(ts_a);
    LBAR();
    TS(ts_b);
#pragma unroll
    for (int ks = 0; ks < 8; ++ks) q[ks] = ldsfrag(L + 131072 + wq * 8192 + ks * 1024 + lane * 16);
    if (g == 0) {
      hq = *(LAS const u32x4*)(Ik + lane * 16); hk = *(LAS const u32x4*)(Ik + 1024 + lane * 16); hv = *(LAS const u32x4*)(Ik + 2048 + lane * 16); gl = *(LAS const float*)(Ik + 3072 + lane * 4);
    }
    if (wq >= 2) {
#pragma unroll
      for (int mt = 0; mt < 2; ++mt)
#pragma unroll
        for (int nt = 0; nt < 3; ++nt)
          if (g == 0 || nt == 0) {
            const f32x16 v = acc[mt][nt] * QSCALE; const unsigned so = (unsigned)(b * 32768 + ((2 * (wq - 2) + mt) * 8 + 2 * (3 * g + nt)) * 1024);
            st128(pack8<0>(v), sW, voff, so); st128(pack8<1>(v), sW, voff + 1024u, so);
          }
    }
    {
      const int i16 = lane & 15, rt = 2 * wq + (g ^ 1);
      const int sw = (r32 >> 1) & 7; LAS char* wr = Ik + r32 * 64;
      const int gr = ((lane >> 4) & 1) * 4 + (i16 & 3), q4 = i16 >> 2;
      unsigned tb[2];
#pragma unroll
      for (int u = 0; u < 2; ++u) tb[u] = (unsigned)((8 * hh + 4 * u + q4) * 64 + ((gr ^ (4 * hh + 2 * u + (q4 >> 1))) * 8));
      f32x16 pq = MFMA(asbf(c.wp), asbf(hq), (f32x16){});
      f32x16 pk = MFMA(asbf(c.wp), asbf(hk), (f32x16){});
      LWAIT();
      *(LAS u32x2*)(wr + 2048 + ((0 + hh) ^ sw) * 8) = (u32x2){hv[0], hv[1]};
      *(LAS u32x2*)(wr + 2048 + ((2 + hh) ^ sw) * 8) = (u32x2){hv[2], hv[3]};
      *(LAS u32x2*)(wr + 2048 + ((4 + hh) ^ sw) * 8) = (u32x2){hh == 0 ? 0x00003f80u : 0u, 0u};
      *(LAS u32x2*)(wr + 2048 + ((6 + hh) ^ sw) * 8) = (u32x2){0u, 0u};
      softmax2(pk);
#pragma unroll
      for (int k = 0; k < 4; ++k) *(LAS u32x2*)(wr + ((2 * k + hh) ^ sw) * 8) = (u32x2){cvtpk(pk[4 * k], pk[4 * k + 1]), cvtpk(pk[4 * k + 2], pk[4 * k + 3])};
      LWAIT();
      f32x16 kv = {};
#pragma unroll
      for (int ks = 0; ks < 2; ++ks) kv = MFMA(trfrag(Ik + tb[0] + ks * 1024, Ik + tb[1] + ks * 1024), trfrag(Iv + tb[0] + ks * 1024, Iv + tb[1] + ks * 1024), kv);
      softmax2(pq);
      const unsigned so = (unsigned)(WS_PQ - WS_QF) + (unsigned)(b * 16384 + rt * 2048);
      st128(pack8<0>(pq), sW, voff, so); st128(pack8<1>(pq), sW, voff + 1024u, so);
      const float gt = __builtin_amdgcn_rcpf(1.0f + __builtin_amdgcn_exp2f(-1.4426950408889634f * swap32_lo(gl)));
      __builtin_amdgcn_raw_buffer_store_b32(__float_as_uint(gt), sW, (unsigned)r32 * 4u + (unsigned)(WS_GW - WS_QF) + (unsigned)(b * 1024 + rt * 128), 0, 0);
      LWAIT();
#pragma unroll
      for (int k = 0; k < 4; ++k) *(LAS f32x4*)(Ik + k * 1024 + lane * 16) = (f32x4){kv[4 * k], kv[4 * k + 1], kv[4 * k + 2], kv[4 * k + 3]};
    }
    dma16s<16384u, 32768u>(ringK, voff, sK, c.soKa); dma16s<16384u + 1024u, 32768u + 1024u>(ringK, voff, sK, c.soKa);
    dma16s<16384u + 2048u, 32768u + 2048u>(ringK, voff, sK, c.soKa); dma16s<16384u + 3072u, 32768u + 3072u>(ringK, voff, sK, c.soKa);
    dma16s<0u, 0u>(ringV, voff, sV, c.soKa); dma16s<1024u, 1024u>(ringV, voff, sV, c.soKa);
    dma16s<2048u, 2048u>(ringV, voff, sV, c.soKa); dma16s<3072u, 3072u>(ringV, voff, sV, c.soKa);
    TS(ts_c);
    wait_bar<11>();
    if (g == 1) {
      f32x4 s4 = *(LAS const f32x4*)(L + 16384 + wq * 1024 + lane * 16);
#pragma unroll
      for (int k = 1; k < 8; ++k) s4 += *(LAS const f32x4*)(L + (k < 4 ? 16384 : 81920) + (k & 3) * 4096 + wq * 1024 + lane * 16);
      st128_wt(__builtin_bit_cast(u32x4, s4), sW, voff, (unsigned)(WS_KVP - WS_QF) + (unsigned)((h * 8 + r) * 4096 + wq * 1024));
      LBAR();
    }
  }
  TS(t3);
  f32x16 O[4], S[2], keep0 = {}, keep1 = {}, negm = {};
#pragma unroll
  for (int d = 0; d < 4; ++d) O[d] = (f32x16){};
  bf16x8 p[4];
  float m = 0.f, l = 0.f, mk = 0.f, lk = 0.f, pm0 = 0.f;
  float gate[2] = {0.f, 0.f}; f32x2 ps = {0.f, 0.f};
  u32x2 fl2 = {0u, 0u};
  u32x4 warm_dummy; unsigned warm_dm1;
  LAS const char* kbase = L + (g ? 98304 : 32768) + lane * 16;
  LAS const char* vbase = L + (g ? 0 : 65536) + lane * 16;
  LAS char* xr = L + 131072 + wq * 8192 + lane * 16;
#define TILE(s) ((s) < nX ? 2 * (s) + g : 2 * ((s) - nX) + g)
#define CF_SO(blkrow, half) ((unsigned)(WS_CF - WS_QF) + (unsigned)(((4 * (blkrow) + wq) * 288 + 8 * h + 4 * (half)) * 1024))
#define ST_SO(wv) ((unsigned)(WS_ST - WS_QF) + (unsigned)(b * 2048 + (wv) * 256))
#define ST_LOAD(wv) asm volatile("buffer_load_dwordx2 %0, %1, %2, %3 offen" : "+v"(ps) : "v"((unsigned)r32 * 8u), "s"(sW), "s"(rfl(ST_SO(wv))) : "memory")
#define L_SOFTMAX(DIAG, FIRST, D0, D1, D2, D3, D4, D5, D6, D7) do { \
    if (DIAG) { int rel = 32 * wq + r32 - 64 * g - 4 * hh; \
      _Pragma("unroll") for (int e = 0; e < 16; ++e) { const int cc = (e & 3) + 8 * (e >> 2); if ((e & 3) == 0) { asm volatile("" : "+v"(rel)); SBAR(); } \
        if (cc > rel) S[0][e] = -__builtin_inff(); if (cc + 32 > rel) S[1][e] = -__builtin_inff(); } \
      pm0 = max2f(S[0][0], S[0][1]); _Pragma("unroll") for (int e = 2; e < 16; e += 2) pm0 = max3f(pm0, S[0][e], S[0][e + 1]); } \
    float pm = max3f(pm0, S[1][0], S[1][1]), pn = max3f(S[1][2], S[1][3], S[1][4]); \
    pm = max3f(pm, S[1][5], S[1][6]); pn = max3f(pn, S[1][7], S[1][8]); pm = max3f(pm, S[1][9], S[1][10]); pn = max3f(pn, S[1][11], S[1][12]); \
    pm = max3f(pm, S[1][13], S[1][14]); pm = max3f(pm, pn, S[1][15]); \
    { auto rr = __builtin_amdgcn_permlane32_swap(__float_as_uint(pm), __float_as_uint(pm), false, false); pm = max2f(__uint_as_float(rr[0]), __uint_as_float(rr[1])); } \
    SBAR(); TS2X(f); D0; SBAR(); \
    const bool first_ = (FIRST); \
    if (first_ || __builtin_expect(__any(pm > THR), 0)) { \
      NOP16(); SBAR(); \
      const float dl = fmaxf(pm, first_ ? -1e30f : 0.f); \
      m += dl; \
      if (!first_) { const float f = __builtin_amdgcn_exp2f(-dl); l *= f; _Pragma("unroll") for (int d = 0; d < 4; ++d) O[d] = O[d] * f; } \
      _Pragma("unroll") for (int e = 0; e < 16; ++e) { S[0][e] -= dl; S[1][e] -= dl; negm[e] = -m; } \
    } \
    TS2X(g); float ls0 = l, ls1 = 0.f; unsigned pw[8]; \
    SM_GRP(0, D1); SM_GRP(2, D2); SM_GRP(4, D3); SM_GRP(6, D4); SM_GRP(8, D5); SM_GRP(10, D6); SM_GRP(12, D7); SM_GRP(14, (void)0); \
    l = ls0 + ls1; \
    p[0] = asbf((u32x4){pw[0], pw[1], pw[2], pw[3]}); p[1] = asbf((u32x4){pw[4], pw[5], pw[6], pw[7]}); } while (0)
#define SM_GRP(e0, DMA) do { const float x0 = __builtin_amdgcn_exp2f(S[0][e0]), x1 = __builtin_amdgcn_exp2f(S[0][(e0) + 1]); \
      ls0 += x0; ls1 += x1; pw[(e0) >> 1] = cvtpk(x0, x1); asm volatile("" : "+v"(pw[(e0) >> 1]), "+v"(ls0), "+v"(ls1)); DMA; SBAR(); } while (0)
#define L_SUM() do { } while (0)
  float mo = 0.f, lo = 0.f;
  auto l_pre = [&](int s) {
    if (s == nX) {
      NOP16(); SBAR();
      mo = m; lo = swap32_add(l);
      if (g == 1) ST_LOAD(w - 4);
      __builtin_amdgcn_raw_buffer_store_b64(__builtin_bit_cast(u32x2, (f32x2){mo, lo}), sW, (unsigned)r32 * 8u, ST_SO(w), 0);
      if (g == 0) { a_send(O[2], O[3], xr); keep0 = O[0]; keep1 = O[1]; mk = mo; lk = lo; }
      m = 0.f; l = 0.f;
    }
    if (g == 0 && s == nX + 1) ST_LOAD(w + 4);
  };
  auto l_post = [&](int s) {
    if (s == nX) {
      if (g == 1) {
        wait_vm<8 - NMV>(); asm volatile("" : "+v"(ps));
        a_finish(O[2], O[3], mo, lo, xr, ps[0], ps[1], gate[0], sW, voff, CF_SO(X, 1));
        LWAIT();
        a_send(O[0], O[1], xr);
      }
#pragma unroll
      for (int d = 0; d < 4; ++d) O[d] = (f32x16){};
    }
    if (g == 0 && s == nX + 1) {
      wait_vm<8 - NMV>(); asm volatile("" : "+v"(ps));
      a_finish(keep0, keep1, mk, lk, xr, ps[0], ps[1], gate[0], sW, voff, CF_SO(X, 0));
    }
  };
  unsigned mK = ringK, mV = ringV + 16384u, oK = (unsigned)(4 + g) * 16384u + (unsigned)wq * 4096u, oV = (unsigned)(2 + g) * 16384u + (unsigned)wq * 4096u;
  LAS const char* kp = kbase + 16384; LAS const char* vp = vbase;
#define LV(x) do { if constexpr (NMV == 0) { x; } } while (0)
#define A_TAIL() do { \
    asm volatile("" : "+v"(p[0]), "+v"(p[1]), "+v"(l), "+v"(S[1])); \
    SBAR(); TS2(d); TS2V(); wait_bar<8 - NMV>(); TS2(e); SBAR(); \
    a_mphase<true, true>(kp, vp, q, p, S, O, negm, l, pm0, mV, voff, sV, oV STP_ARG); oV += 32768u; \
    mK ^= 16384u; mV ^= 16384u; kp = (LAS const char*)((unsigned)(uintptr_t)kp ^ 16384u); vp = (LAS const char*)((unsigned)(uintptr_t)vp ^ 16384u); } while (0)
#define A_FAST(FIRST) do { \
    if (!(FIRST)) L_SUM(); \
    L_SOFTMAX(false, FIRST, dma16o<0>(mK, voff, sK, oK), dma16n<1024>(voff, sK, oK), dma16n<2048>(voff, sK, oK), dma16n<3072>(voff, sK, oK), \
              LV(dma16o<0>(mV, voff, sV, oV)), LV(dma16n<1024>(voff, sV, oV)), LV(dma16n<2048>(voff, sV, oV)), LV(dma16n<3072>(voff, sV, oV))); \
    oK += 32768u; TS2X(h); A_TAIL(); } while (0)
#define A_SLOW(s) do { \
    L_SUM(); l_pre(s); \
    oK = (s) < 15 ? (unsigned)TILE((s) + 2) * 16384u + (unsigned)wq * 4096u : (unsigned)(WS_KVP - WS_QF) + (unsigned)(h * 32768 + w * 4096); \
    oV = (unsigned)TILE((s) + 1) * 16384u + (unsigned)wq * 4096u; \
    const srd_t sKx = (s) < 15 ? sK : sW; \
    L_SOFTMAX((s) == nX - 1, (s) == nX, dma16o<0>(mK, voff, sKx, oK), dma16n<1024>(voff, sKx, oK), dma16n<2048>(voff, sKx, oK), dma16n<3072>(voff, sKx, oK), \
              LV(dma16o<0>(mV, voff, sV, oV)), LV(dma16n<1024>(voff, sV, oV)), LV(dma16n<2048>(voff, sV, oV)), LV(dma16n<3072>(voff, sV, oV))); \
    oK += 32768u; TS2X(h); \
    l_post(s); \
    if ((s) == nX - 1) { _Pragma("unroll") for (int ks = 0; ks < 8; ++ks) q[ks] = ldsfrag(L + 131072 + wq * 8192 + ks * 1024 + lane * 16); negm = (f32x16){}; } \
    A_TAIL(); } while (0)
  a_mphase<true, false>(kbase, vbase, q, p, S, O, negm, l, pm0, 0u, voff, sV, 0u STP_ARG);
  A_FAST(true);
  if (w == 0 && lane == 0) __hip_atomic_store((u64*)flg + 8 + r, tag, __ATOMIC_RELAXED, __HIP_MEMORY_SCOPE_AGENT);
  A_FAST(false);
  {
    const unsigned mq = rfl(lds0 + 131072u + (unsigned)w * 4096u), oq = rfl((unsigned)(b * 32768 + w * 4096));
    dma16o<0>(mq, voff, sW, oq); dma16n<1024>(voff, sW, oq); dma16n<2048>(voff, sW, oq); dma16n<3072>(voff, sW, oq);
    const unsigned go = rfl((unsigned)(WS_GW - WS_QF) + (unsigned)(b * 1024 + wq * 128));
    asm volatile("buffer_load_dword %0, %2, %3, %4 offen\n\tbuffer_load_dword %1, %2, %3, %4 offen offset:512" : "+v"(gate[0]), "+v"(gate[1]) : "v"((unsigned)r32 * 4u), "s"(sW), "s"(go) : "memory");
    poll_issue(fl2, flg + 8, lane);
    const srd_t sWa = mksrd(wo_a, 4096u * 1024u), sWb = mksrd(wo_b, 512u * 1024u);
    u32x4 dmy; unsigned dm1;
    asm volatile("buffer_load_dwordx4 %0, %2, %3, 0 offen\n\tbuffer_load_dwordx4 %0, %2, %3, 0 offen offset:1024\n\t"
                 "buffer_load_dwordx4 %0, %4, %5, 0 offen\n\tbuffer_load_dword %1, %6, %3, 0 offen"
                 : "=&v"(dmy), "=&v"(dm1) : "v"(voff + (unsigned)(b * 16384 + w * 2048)), "s"(sWa), "v"((unsigned)(b * 2048 + w * 256) + (voff & 255u)), "s"(sWb),
                   "v"((unsigned)((((b >> 3) * 8 + w) * 4 + (lane & 3)) * 4096 + (lane >> 2) * 0)) : "memory");
    warm_dummy = dmy; warm_dm1 = dm1;
  }
  A_FAST(false);
  asm volatile("" : "+v"(gate[0]), "+v"(gate[1]), "+v"(fl2));
  asm volatile("" :: "v"(warm_dummy), "v"(warm_dm1));
  if (w == 0) { poll_check(fl2, flg + 8, tag, lane); if (r == 0 && lane == 0) __hip_atomic_store((u64*)flg + 16, tag, __ATOMIC_RELAXED, __HIP_MEMORY_SCOPE_AGENT); }
  {
    int s = 3;
#pragma unroll 1
    for (int blk = 0; blk < 2; ++blk) {
      const int e1 = blk ? 15 : nX - 2, e2 = blk ? 16 : (nX + 2 < 16 ? nX + 2 : 16);
#pragma unroll 1
      for (; s < e1; ++s) A_FAST(false);
#pragma unroll 1
      for (; s < e2; ++s) A_SLOW(s);
    }
  }
  TS(t4);
  {
    L_SUM(); l_pre(16);
    const unsigned mp = rfl(lds0 + (g ? 98304u : 32768u) + (unsigned)wq * 2048u), op = rfl((unsigned)(WS_PQ - WS_QF) + (unsigned)(b * 16384 + w * 2048));
    L_SOFTMAX(true, nX == 16, dma16o<0>(mp, voff, sW, op), dma16n<1024>(voff, sW, op), (void)0, (void)0, (void)0, (void)0, (void)0, (void)0);
    if (16 == nX || 16 == nX + 1) wait_vm<0>();
    l_post(16);
    asm volatile("" : "+v"(p[0]), "+v"(p[1]), "+v"(l), "+v"(S[1]));
    SBAR(); wait_bar<2>(); SBAR();
  }
  a_mphase<false, true>(kbase, vbase, q, p, S, O, negm, l, pm0, 0u, voff, sV, 0u STP_ARG);
  NOP16(); SBAR();
  if (g == 0 && nX == 16) {
    ST_LOAD(w + 4); wait_vm<0>(); asm volatile("" : "+v"(ps));
    a_finish(keep0, keep1, mk, lk, xr, ps[0], ps[1], gate[0], sW, voff, CF_SO(X, 0));
    LWAIT();
  }
  LAS f32x2* stl = (LAS f32x2*)(L + 81920) + r32;
  auto stage2 = [&]() {
    f32x16 kv = {};
#pragma unroll
    for (int k = 0; k < 8; ++k)
#pragma unroll
      for (int qd = 0; qd < 4; ++qd) { const f32x4 v = *(LAS const f32x4*)(L + (k < 4 ? 49152 : 114688) + (k & 3) * 4096 + qd * 1024 + lane * 16);
#pragma unroll
        for (int e = 0; e < 4; ++e) kv[4 * qd + e] += v[e]; }
    const bf16x8 p0 = ldsfrag(kbase + wq * 2048), p1 = ldsfrag(kbase + wq * 2048 + 1024);
    f32x16 o = MFMA(asbf(pack8<0>(kv)), p0, (f32x16){});
    o = MFMA(asbf(pack8<1>(kv)), p1, o);
    const float sc = (1.0f - (g ? gate[1] : gate[0])) / (swap32_lo(o[8]) + 1e-6f);
    o = o * sc;
    st128(pack8<0>(o), sW, voff, (unsigned)(WS_CF - WS_QF) + (unsigned)(((4 * (g ? Y : X) + wq) * 288 + 256 + h) * 1024));
  };
  const float lt = swap32_add(l);
  if (g == 0) {
    a_send(O[2], O[3], xr);
    stl[w * 32] = (f32x2){m, lt};
    wait_bar<0>();
    stage2();
    LBAR();
    const f32x2 po = stl[(w + 4) * 32];
    a_finish(O[0], O[1], m, lt, xr, po[0], po[1], gate[1], sW, voff, CF_SO(Y, 0));
  } else {
    const f32x2 po = stl[(w - 4) * 32];
    a_finish(O[2], O[3], m, lt, xr, po[0], po[1], gate[1], sW, voff, CF_SO(Y, 1));
    LWAIT();
    a_send(O[0], O[1], xr);
    stl[w * 32] = (f32x2){m, lt};
    wait_bar<4>();
    stage2();
  }
#undef TILE
#undef CF_SO
#undef ST_SO
#undef ST_LOAD
#undef L_SOFTMAX
#undef SM_GRP
#undef L_SUM
#undef LV
#undef A_TAIL
#undef A_FAST
#undef A_SLOW
}

struct OCtx {
  srd_t sC, sWa, sWb, sS; unsigned voff, voffW, voffV, soA, soW[3], mW, bB, bB1, cut;
  LAS char* L; bf16x8 a[36], f[4];
};
constexpr int O_NS = 5;
template <int S, int I> DI void o_wpiece(const OCtx& c) {
  if constexpr (S < 12) {
    constexpr unsigned sl = (unsigned)(S % O_NS) * 24576u + I * 8192u, row = S < 6 ? 192u * S + 64u * I : 384u * ((S - 6) % 3) + 128u * I;
    constexpr int t = S < 6 ? 0 : (S < 9 ? 1 : 2);
    if (row < c.cut) dma16c<sl, row * 1024u>(c.mW, S < 6 ? c.voffW : c.voffV, c.sWa, c.soW[t]);
    else             dma16c<sl, row * 1024u - 4194304u>(c.mW, S < 6 ? c.voffW : c.voffV, c.sWb, c.soW[t]);
  }
}
template <int i> DI void o_aload(OCtx& c) {
  if constexpr (i >= 0 && i < 36) {
    unsigned t;
    asm volatile("s_add_u32 %1, %4, %5\n\tbuffer_load_dwordx4 %0, %2, %3, %1 offen" : "=v"(c.a[i]), "=&s"(t) : "v"(c.voff), "s"(c.sC), "s"(c.soA), "i"(i * 2048u) : "memory", "scc");
  }
}
template <int S> DI void o_request(OCtx& c) {
  o_wpiece<S, 0>(c); o_wpiece<S, 1>(c); o_wpiece<S, 2>(c);
  o_aload<6 * S>(c); o_aload<6 * S + 1>(c); o_aload<6 * S + 2>(c); o_aload<6 * S + 3>(c); o_aload<6 * S + 4>(c); o_aload<6 * S + 5>(c);
}
template <int S, int k> DI void o_ride(OCtx& c) {
  if constexpr (S < 3) { if constexpr (k < 3) o_wpiece<S + 3, k>(c); else if constexpr (k < 9) o_aload<6 * (S + 3) + k - 3>(c); }
  else { if constexpr (k == 1) o_wpiece<S + 3, 0>(c); if constexpr (k == 5) o_wpiece<S + 3, 1>(c); if constexpr (k == 9) o_wpiece<S + 3, 2>(c); }
}
#define O_SP(S) (c.L + ((S) % O_NS < 2 ? ((S) < 6 ? c.bB : c.bB1) : ((S) % O_NS < 4 ? ((S) < 6 ? c.bB : c.bB1) + 49152u : ((S) < 6 ? c.bB : c.bB1) + 98304u)) + (((S) % O_NS) & 1) * 24576)
#define O_FR(S, p, k) ((S) < 6 ? trfrag((p) + ((k) >> 1) * 4096 + ((k) & 1) * 512, (p) + ((k) >> 1) * 4096 + ((k) & 1) * 512 + 1024) : trfrag((p) + (k) * 2048, (p) + (k) * 2048 + 512))
template <int S, int NY> DI void o_stage(OCtx& c, f32x16 (&acc)[2]) {
  constexpr int i0 = S < 6 ? 6 * S : 12 * ((S - 6) % 3);
  if constexpr (NY >= 0) {
    if constexpr (S < 6) asm volatile("s_waitcnt vmcnt(%6)\n\ts_barrier" : "+v"(c.a[i0]), "+v"(c.a[i0 + 1]), "+v"(c.a[i0 + 2]), "+v"(c.a[i0 + 3]), "+v"(c.a[i0 + 4]), "+v"(c.a[i0 + 5]) : "i"(NY) : "memory");
    else                 asm volatile("s_waitcnt vmcnt(%0)\n\ts_barrier" :: "i"(NY) : "memory");
  }
  SBAR();
  LAS const char* sp = O_SP(S); LAS const char* sn = O_SP(S + 1);
  bf16x8 f[12];
  if constexpr (S == 0) { c.f[0] = O_FR(S, sp, 0); c.f[1] = O_FR(S, sp, 1); c.f[2] = O_FR(S, sp, 2); c.f[3] = O_FR(S, sp, 3); SBAR(); }
  f[0] = c.f[0]; f[1] = c.f[1]; f[2] = c.f[2]; f[3] = c.f[3];
#define O_LD(k) f[k] = O_FR(S, sp, k)
#define O_LN(k) do { if constexpr (S < 11) c.f[k] = O_FR(S + 1, sn, k); } while (0)
#define O_MM(k) do { if constexpr (S < 6) acc[(k) & 1] = MFMA(f[k], c.a[i0 + ((k) >> 1)], acc[(k) & 1]); else acc[0] = MFMA(f[k], c.a[i0 + (k)], acc[0]); o_ride<S, k>(c); SBAR(); } while (0)
  O_LD(4); O_MM(0); O_LD(5); O_MM(1); O_LD(6); O_MM(2); O_LD(7); O_MM(3);
  O_LD(8); O_MM(4); O_LD(9); O_MM(5); O_LD(10); O_MM(6); O_LD(11); O_MM(7);
  O_LN(0); O_MM(8); O_LN(1); O_MM(9); O_LN(2); O_MM(10); O_LN(3); O_MM(11);
#undef O_LD
#undef O_LN
#undef O_MM
}
DI void o_park(LAS char* p, const f32x16& v) {
#pragma unroll
  for (int qd = 0; qd < 4; ++qd) *(LAS f32x4*)(p + qd * 1024) = (f32x4){v[4 * qd], v[4 * qd + 1], v[4 * qd + 2], v[4 * qd + 3]};
}
DI void o_addlds(f32x16& v, LAS const char* p) {
#pragma unroll
  for (int qd = 0; qd < 4; ++qd) { const f32x4 u = *(LAS const f32x4*)(p + qd * 1024);
#pragma unroll
    for (int e = 0; e < 4; ++e) v[4 * qd + e] += u[e]; }
}
DI void o_publish(const OCtx& c, const f32x16& v, unsigned so) {
#pragma unroll
  for (int qd = 0; qd < 4; ++qd) st128_wt(__builtin_bit_cast(u32x4, (f32x4){v[4 * qd], v[4 * qd + 1], v[4 * qd + 2], v[4 * qd + 3]}), c.sS, c.voff + (unsigned)(qd * 1024), so);
}
__global__ __launch_bounds__(512, 2) void kO(const u16* __restrict__ wo_a, const u16* __restrict__ wo_b, char* __restrict__ ws, u16* __restrict__ out) {
  extern __shared__ __attribute__((aligned(16))) char lds[];
  LAS char* L = (LAS char*)lds; const unsigned lds0 = (unsigned)(uintptr_t)L;
  const int tid = threadIdx.x, lane = tid & 63, w = __builtin_amdgcn_readfirstlane(tid >> 6);
  const int b = blockIdx.x, xcd = b & 7, j = xcd & 3, idx = b >> 3, mi = (xcd >> 2) * 8 + (idx >> 2), ni = idx & 3;
  const int g = w >> 2, rb = w & 3, hh = lane >> 5, i16 = lane & 15, p4 = i16 & 3;
  const int tg = mi * 4 + ni, pj = j & 2;
  TS(z0);
  OCtx c;
  c.L = L;
  c.sC = mksrd(ws + WS_CF, 64u * 288u * 1024u); c.sWa = mksrd(wo_a, 4096u * 1024u); c.sWb = mksrd(wo_b, 512u * 1024u);
  c.sS = mksrd(ws + WS_SLAB + (size_t)tg * 4 * 65536, 4u * 65536u);
  c.voff = (unsigned)lane * 16u;
  c.voffW = (unsigned)(((lane >> 2) & 7) * 1024 + (lane >> 5) * 64 + (lane & 3) * 16);
  c.soA = rfl((unsigned)((4 * mi + rb) * 288 + 72 * j + g) * 1024u);
  c.soW[0] = rfl((unsigned)(1152 * j + 8 * w) * 1024u + (unsigned)(ni * 256 + (pj ^ 2) * 64));
  c.voffV = (unsigned)((lane >> 2) * 1024 + (lane & 3) * 16);
  c.soW[1] = rfl((unsigned)(1152 * j + 16 * w) * 1024u + (unsigned)(ni * 256 + (j ^ 1) * 64));
  c.soW[2] = rfl((unsigned)(1152 * j + 16 * w) * 1024u + (unsigned)(ni * 256 + j * 64));
  c.cut = rfl((unsigned)(4096 - 1152 * j));
  c.mW = rfl(lds0 + (unsigned)w * 1024u);
  c.bB = (unsigned)(g * 2048 + (4 * hh + (i16 >> 2)) * 64 + ((lane >> 4) & 1) * 32 + (((p4 & 1) << 1) | (p4 >> 1)) * 8);
  c.bB1 = c.bB - (unsigned)(g * 1024);
  LAS char* red = L + 122880 + w * 4096 + c.voff; LAS const char* rrd = L + 122880 + (w ^ 4) * 4096 + c.voff;
  unsigned* cnt = (unsigned*)(ws + WS_CNT + (size_t)tg * 256);
  o_request<0>(c); o_request<1>(c); o_request<2>(c);
  f32x16 acc[2] = {{}, {}};
  o_stage<0, 15>(c, acc); o_stage<1, 15>(c, acc); o_stage<2, 15>(c, acc); o_stage<3, 15>(c, acc); o_stage<4, 9>(c, acc); o_stage<5, 3>(c, acc);
  TS(z1);
  o_park(red, g ? acc[0] : acc[1]);
  f32x16 prev = g ? acc[1] : acc[0];
  acc[0] = (f32x16){};
  wait_bar<3>(); SBAR();
  o_addlds(prev, rrd); o_publish(c, prev, (unsigned)(j * 65536 + ((pj ^ 2) + g) * 16384 + rb * 4096)); SBAR();
  o_stage<6, -1>(c, acc);
  o_stage<7, 7>(c, acc);
  o_stage<8, 3>(c, acc);
  if (c.voff == 0u) __hip_atomic_fetch_add(cnt + 16 * ((pj ^ 2) + g), 1u, __ATOMIC_RELAXED, __HIP_MEMORY_SCOPE_AGENT);
  if (g == 1) o_park(red, acc[0]);
  prev = acc[0]; acc[0] = (f32x16){};
  wait_bar<4>(); SBAR();
  if (g == 0) { o_addlds(prev, rrd); o_publish(c, prev, (unsigned)(j * 65536 + (j ^ 1) * 16384 + rb * 4096)); }
  SBAR();
  TS(z2);
  o_stage<9, -1>(c, acc);
  o_stage<10, 0>(c, acc);
  if (g == 0 && c.voff == 0u) __hip_atomic_fetch_add(cnt + 16 * (j ^ 1), 1u, __ATOMIC_RELAXED, __HIP_MEMORY_SCOPE_AGENT);
  unsigned pc = 0;
  o_stage<11, -1>(c, acc);
  asm volatile("global_load_dword %0, %1, off sc1" : "+v"(pc) : "v"(cnt + 16 * j) : "memory");
  if (g == 1) o_park(red, acc[0]);
  TS(z3);
  wait_vm<0>(); asm volatile("" : "+v"(pc));
  if (w == 0 && __builtin_amdgcn_readfirstlane(pc) < 12u) { while (__builtin_amdgcn_readfirstlane(__hip_atomic_load(cnt + 16 * j, __ATOMIC_RELAXED, __HIP_MEMORY_SCOPE_AGENT)) < 12u) __builtin_amdgcn_s_sleep(1); }
  LBAR();
  if (g == 1) return;
  TS(z4);
  prev = acc[0];
#pragma unroll
  for (int k = 1; k < 4; ++k)
#pragma unroll
    for (int qd = 0; qd < 4; ++qd) {
      const f32x4 v = __builtin_bit_cast(f32x4, __builtin_amdgcn_raw_buffer_load_b128(c.sS, c.voff + (unsigned)(qd * 1024), (unsigned)((j ^ k) * 65536 + j * 16384 + rb * 4096), 16));
#pragma unroll
      for (int e = 0; e < 4; ++e) prev[4 * qd + e] += v[e];
    }
  o_addlds(prev, rrd);
  unsigned vo = c.voff; asm volatile("" : "+v"(vo));
  u16* op = out + (size_t)(128 * mi + 32 * rb + ((vo >> 4) & 31)) * 512 + 128 * ni + 32 * j + 8 * (vo >> 9);
  *(u32x4*)(op) = pack8<0>(prev);
  *(u32x4*)(op + 16) = pack8<1>(prev);
}
#undef O_SP
#undef O_FR

extern "C" void kernel_launch(void* const* d_in, const int* in_sizes, int n_in, void* d_out, int out_size, void* d_ws, size_t ws_size, hipStream_t stream) {
  const u16* x = (const u16*)d_in[0]; const u16* ckv = (const u16*)d_in[1]; const u16* wq = (const u16*)d_in[2];
  const u16* wuk = (const u16*)d_in[3]; const u16* wuv = (const u16*)d_in[4]; const u16* wo_a = (const u16*)d_in[5];
  const u16* wqkv = (const u16*)d_in[7]; const u16* wphi = (const u16*)d_in[8]; const u16* wo_b = (const u16*)d_in[9]; const u16* gatew = (const u16*)d_in[10];
  (void)hipFuncSetAttribute((const void*)kF, hipFuncAttributeMaxDynamicSharedMemorySize, 163840);
  (void)hipFuncSetAttribute((const void*)kO, hipFuncAttributeMaxDynamicSharedMemorySize, 155648);
  kF<<<256, 512, 163840, stream>>>(x, ckv, wq, wuk, wuv, wqkv, wphi, gatew, wo_a, wo_b, (char*)d_ws);
  kO<<<256, 512, 155648, stream>>>(wo_a, wo_b, (char*)d_ws, (u16*)d_out);
}
```

```cpp
#define PRIO_M
#include <hip/hip_runtime.h>
#include <stdint.h>

typedef unsigned short u16;
typedef unsigned long long u64;
typedef __bf16 bf16x8 __attribute__((ext_vector_type(8)));
typedef __bf16 bf16x2 __attribute__((ext_vector_type(2)));
typedef float f32x2 __attribute__((ext_vector_type(2)));
typedef float f32x4 __attribute__((ext_vector_type(4)));
typedef float f32x16 __attribute__((ext_vector_type(16)));
typedef unsigned u32x2 __attribute__((ext_vector_type(2)));
typedef unsigned u32x4 __attribute__((ext_vector_type(4)));
typedef short s16x4 __attribute__((ext_vector_type(4)));
typedef short s16x8 __attribute__((ext_vector_type(8)));
typedef __amdgpu_buffer_rsrc_t srd_t;
#define LAS __attribute__((address_space(3)))
#define DI __device__ __forceinline__
#define SBAR() __builtin_amdgcn_sched_barrier(0)
#define MFMA(a, b, c) __builtin_amdgcn_mfma_f32_32x32x16_bf16((a), (b), (c), 0, 0, 0)

constexpr size_t MB = 1024 * 1024;
constexpr size_t WS_KF = 0;
constexpr size_t WS_VF = 16 * MB;
constexpr size_t WS_QF = 32 * MB;
constexpr size_t WS_CF = 48 * MB;
constexpr size_t WS_PQ = 68 * MB;
constexpr size_t WS_KVP = 72 * MB;
constexpr size_t WS_GW = 73 * MB;
constexpr size_t WS_ST = 74 * MB;
constexpr size_t WS_SLAB = 0;
constexpr size_t WS_CNT = 92 * MB;
constexpr size_t WS_FLG = 93 * MB;
constexpr size_t WS_DBG = 100 * MB;

constexpr float QSCALE = 0.08838834764831845f * 1.4426950408889634f;
constexpr float THR = 8.0f;

DI unsigned cvtpk(float lo, float hi) { f32x2 v = {lo, hi}; bf16x2 b = __builtin_convertvector(v, bf16x2); return __builtin_bit_cast(unsigned, b); }
template <int S> DI u32x4 pack8(const f32x16& x) {
  return (u32x4){cvtpk(x[8 * S + 0], x[8 * S + 1]), cvtpk(x[8 * S + 2], x[8 * S + 3]), cvtpk(x[8 * S + 4], x[8 * S + 5]), cvtpk(x[8 * S + 6], x[8 * S + 7])};
}
DI bf16x8 asbf(u32x4 v) { return __builtin_bit_cast(bf16x8, v); }
DI float swap32_max(float v) { auto rr = __builtin_amdgcn_permlane32_swap(__float_as_uint(v), __float_as_uint(v), false, false); return fmaxf(__uint_as_float(rr[0]), __uint_as_float(rr[1])); }
DI float swap32_add(float v) { auto rr = __builtin_amdgcn_permlane32_swap(__float_as_uint(v), __float_as_uint(v), false, false); return __uint_as_float(rr[0]) + __uint_as_float(rr[1]); }
DI float swap32_lo(float v) { auto rr = __builtin_amdgcn_permlane32_swap(__float_as_uint(v), __float_as_uint(v), false, false); return __uint_as_float(rr[0]); }
DI float bf16round(float x) { return __uint_as_float(cvtpk(x, 0.f) << 16); }
DI srd_t mksrd(const void* p, unsigned bytes) { return __builtin_amdgcn_make_buffer_rsrc((void*)p, (short)0, (int)bytes, 0x00020000); }
DI unsigned rfl(unsigned x) { return (unsigned)__builtin_amdgcn_readfirstlane((int)x); }
template <int OFF> DI void dma16o(unsigned m0v, unsigned voff, srd_t srd, unsigned soff) {
  asm volatile("s_mov_b32 m0, %0\n\ts_nop 0\n\tbuffer_load_dwordx4 %1, %2, %3 offen offset:%4 sc1 lds" :: "s"(m0v), "v"(voff), "s"(srd), "s"(soff), "i"(OFF) : "memory");
}
template <unsigned MOFF, unsigned SOFF> DI void dma16s(unsigned mb, unsigned voff, srd_t srd, unsigned sb) {
  unsigned t;
  asm volatile("s_add_u32 m0, %1, %5\n\ts_add_u32 %0, %4, %6\n\tbuffer_load_dwordx4 %2, %3, %0 offen sc1 lds" : "=&s"(t) : "s"(mb), "v"(voff), "s"(srd), "s"(sb), "i"(MOFF), "i"(SOFF) : "memory", "scc");
}
template <int OFF> DI void dma16n(unsigned voff, srd_t srd, unsigned soff) {
  asm volatile("buffer_load_dwordx4 %0, %1, %2 offen offset:%3 sc1 lds" :: "v"(voff), "s"(srd), "s"(soff), "i"(OFF) : "memory");
}
template <unsigned MOFF, unsigned SOFF> DI void dma16c(unsigned mb, unsigned voff, srd_t srd, unsigned sb) {
  unsigned t;
  asm volatile("s_add_u32 m0, %1, %5\n\ts_add_u32 %0, %4, %6\n\tbuffer_load_dwordx4 %2, %3, %0 offen lds" : "=&s"(t) : "s"(mb), "v"(voff), "s"(srd), "s"(sb), "i"(MOFF), "i"(SOFF) : "memory", "scc");
}
template <unsigned MOFF, unsigned SOFF> DI void dma16m(unsigned mb, unsigned voff, srd_t srd, unsigned sb) {
  unsigned t;
  asm volatile("s_add_u32 m0, %1, %5\n\ts_add_u32 %0, %4, %6\n\ts_mov_b32 exec_hi, 0x33333333\n\tbuffer_load_dwordx4 %2, %3, %0 offen lds\n\ts_mov_b32 exec_hi, -1"
               : "=&s"(t) : "s"(mb), "v"(voff), "s"(srd), "s"(sb), "i"(MOFF), "i"(SOFF) : "memory", "scc");
}
template <int N> DI void wait_bar() { asm volatile("s_waitcnt vmcnt(%0) lgkmcnt(0)\n\ts_barrier" :: "i"(N) : "memory"); }
template <int N> DI void wait_vm() { asm volatile("s_waitcnt vmcnt(%0)" :: "i"(N) : "memory"); }
#define LBAR() asm volatile("s_waitcnt lgkmcnt(0)\n\ts_barrier" ::: "memory")
#define LWAIT() asm volatile("s_waitcnt lgkmcnt(0)" ::: "memory")
#define NOP16() asm volatile("s_nop 7\n\ts_nop 7" ::: "memory")
DI bf16x8 ldsfrag(LAS const char* p) { return *(LAS const bf16x8*)p; }
DI bf16x8 trfrag(LAS const char* p0, LAS const char* p1) {
  s16x4 a = __builtin_amdgcn_ds_read_tr16_b64_v4i16((LAS s16x4*)p0);
  s16x4 b = __builtin_amdgcn_ds_read_tr16_b64_v4i16((LAS s16x4*)p1);
  return __builtin_bit_cast(bf16x8, __builtin_shufflevector(a, b, 0, 1, 2, 3, 4, 5, 6, 7));
}
DI void st128(u32x4 v, srd_t s, unsigned vo, unsigned so) { __builtin_amdgcn_raw_buffer_store_b128(v, s, vo + so, 0, 0); }
DI void st128_wt(u32x4 v, srd_t s, unsigned vo, unsigned so) { __builtin_amdgcn_raw_buffer_store_b128(v, s, vo + so, 0, 16); }
#define TS(x)

struct OCtx {
  srd_t sC, sWa, sWb; unsigned voff, voffW, voffV, soA, soW[3], mW, bB, bB1, cut;
  LAS char* L; bf16x8 a[36], f[4];
};
constexpr int O_NS = 5;
template <int S, int I> DI void o_wpiece(const OCtx& c) {
  if constexpr (S < 12) {
    constexpr unsigned sl = (unsigned)(S % O_NS) * 24576u + I * 8192u, row = S < 6 ? 192u * S + 64u * I : 384u * ((S - 6) % 3) + 128u * I;
    constexpr int t = S < 6 ? 0 : (S < 9 ? 1 : 2);
    if (row < c.cut) dma16c<sl, row * 1024u>(c.mW, S < 6 ? c.voffW : c.voffV, c.sWa, c.soW[t]);
    else             dma16c<sl, row * 1024u - 4194304u>(c.mW, S < 6 ? c.voffW : c.voffV, c.sWb, c.soW[t]);
  }
}
template <int i> DI void o_aload(OCtx& c) {
  if constexpr (i >= 0 && i < 36) {
    unsigned t;
    asm volatile("s_add_u32 %1, %4, %5\n\tbuffer_load_dwordx4 %0, %2, %3, %1 offen sc1" : "=v"(c.a[i]), "=&s"(t) : "v"(c.voff), "s"(c.sC), "s"(c.soA), "i"(i * 2048u) : "memory", "scc");
  }
}
template <int S> DI void o_request(OCtx& c) {
  o_wpiece<S, 0>(c); o_wpiece<S, 1>(c); o_wpiece<S, 2>(c);
  o_aload<6 * S>(c); o_aload<6 * S + 1>(c); o_aload<6 * S + 2>(c); o_aload<6 * S + 3>(c); o_aload<6 * S + 4>(c); o_aload<6 * S + 5>(c);
}
template <int S, int k> DI void o_ride(OCtx& c) {
  if constexpr (S < 3) { if constexpr (k < 3) o_wpiece<S + 3, k>(c); else if constexpr (k < 9) o_aload<6 * (S + 3) + k - 3>(c); }
  else { if constexpr (k == 1) o_wpiece<S + 3, 0>(c); if constexpr (k == 5) o_wpiece<S + 3, 1>(c); if constexpr (k == 9) o_wpiece<S + 3, 2>(c); }
}
#define O_SP(S) (c.L + ((S) % O_NS < 2 ? ((S) < 6 ? c.bB : c.bB1) : ((S) % O_NS < 4 ? ((S) < 6 ? c.bB : c.bB1) + 49152u : ((S) < 6 ? c.bB : c.bB1) + 98304u)) + (((S) % O_NS) & 1) * 24576)
#define O_FR(S, p, k) ((S) < 6 ? trfrag((p) + ((k) >> 1) * 4096 + ((k) & 1) * 512, (p) + ((k) >> 1) * 4096 + ((k) & 1) * 512 + 1024) : trfrag((p) + (k) * 2048, (p) + (k) * 2048 + 512))
template <int S, int NY> DI void o_stage(OCtx& c, f32x16 (&acc)[2]) {
  constexpr int i0 = S < 6 ? 6 * S : 12 * ((S - 6) % 3);
  if constexpr (NY >= 0) {
    if constexpr (S < 6) asm volatile("s_waitcnt vmcnt(%6)\n\ts_barrier" : "+v"(c.a[i0]), "+v"(c.a[i0 + 1]), "+v"(c.a[i0 + 2]), "+v"(c.a[i0 + 3]), "+v"(c.a[i0 + 4]), "+v"(c.a[i0 + 5]) : "i"(NY) : "memory");
    else                 asm volatile("s_waitcnt vmcnt(%0)\n\ts_barrier" :: "i"(NY) : "memory");
  }
  SBAR();
  LAS const char* sp = O_SP(S); LAS const char* sn = O_SP(S + 1);
  bf16x8 f[12];
  if constexpr (S == 0) { c.f[0] = O_FR(S, sp, 0); c.f[1] = O_FR(S, sp, 1); c.f[2] = O_FR(S, sp, 2); c.f[3] = O_FR(S, sp, 3); SBAR(); }
  f[0] = c.f[0]; f[1] = c.f[1]; f[2] = c.f[2]; f[3] = c.f[3];
#define O_LD(k) f[k] = O_FR(S, sp, k)
#define O_LN(k) do { if constexpr (S < 11) c.f[k] = O_FR(S + 1, sn, k); } while (0)
#define O_MM(k) do { if constexpr (S < 6) acc[(k) & 1] = MFMA(f[k], c.a[i0 + ((k) >> 1)], acc[(k) & 1]); else acc[0] = MFMA(f[k], c.a[i0 + (k)], acc[0]); o_ride<S, k>(c); SBAR(); } while (0)
  O_LD(4); O_MM(0); O_LD(5); O_MM(1); O_LD(6); O_MM(2); O_LD(7); O_MM(3);
  O_LD(8); O_MM(4); O_LD(9); O_MM(5); O_LD(10); O_MM(6); O_LD(11); O_MM(7);
  O_LN(0); O_MM(8); O_LN(1); O_MM(9); O_LN(2); O_MM(10); O_LN(3); O_MM(11);
#undef O_LD
#undef O_LN
#undef O_MM
}
DI void o_park(LAS char* p, const f32x16& v) {
#pragma unroll
  for (int qd = 0; qd < 4; ++qd) *(LAS f32x4*)(p + qd * 1024) = (f32x4){v[4 * qd], v[4 * qd + 1], v[4 * qd + 2], v[4 * qd + 3]};
}
DI void o_addlds(f32x16& v, LAS const char* p) {
#pragma unroll
  for (int qd = 0; qd < 4; ++qd) { const f32x4 u = *(LAS const f32x4*)(p + qd * 1024);
#pragma unroll
    for (int e = 0; e < 4; ++e) v[4 * qd + e] += u[e]; }
}
DI void o_publish(srd_t sW, unsigned voff, const f32x16& v, unsigned so) {
#pragma unroll
  for (int qd = 0; qd < 4; ++qd) st128_wt(__builtin_bit_cast(u32x4, (f32x4){v[4 * qd], v[4 * qd + 1], v[4 * qd + 2], v[4 * qd + 3]}), sW, voff + (unsigned)(qd * 1024), so);
}
DI void o_phase(const u16* wo_a, const u16* wo_b, char* ws, u16* out, LAS char* L, unsigned lds0, int lane, int w, int h, int r, u64 tag, srd_t sW) {
  const int j = h >> 3, mi = (h & 4) ? 15 - r : r, ni = h & 3, b = blockIdx.x;
  const int g = w >> 2, rb = w & 3, hh = lane >> 5, i16 = lane & 15, p4 = i16 & 3, pj = j & 2;
  u64* fl = (u64*)(ws + WS_FLG + 65536 + (size_t)r * 1024);
  OCtx c;
  c.L = L;
  c.sC = mksrd(ws + WS_CF, 64u * 288u * 1024u); c.sWa = mksrd(wo_a, 4096u * 1024u); c.sWb = mksrd(wo_b, 512u * 1024u);
  c.voff = (unsigned)lane * 16u;
  c.voffW = (unsigned)(((lane >> 2) & 7) * 1024 + (lane >> 5) * 64 + (lane & 3) * 16);
  c.voffV = (unsigned)((lane >> 2) * 1024 + (lane & 3) * 16);
  c.soA = rfl((unsigned)((4 * mi + rb) * 288 + 72 * j + g) * 1024u);
  c.soW[0] = rfl((unsigned)(1152 * j + 8 * w) * 1024u + (unsigned)(ni * 256 + (pj ^ 2) * 64));
  c.soW[1] = rfl((unsigned)(1152 * j + 16 * w) * 1024u + (unsigned)(ni * 256 + (j ^ 1) * 64));
  c.soW[2] = rfl((unsigned)(1152 * j + 16 * w) * 1024u + (unsigned)(ni * 256 + j * 64));
  c.cut = rfl((unsigned)(4096 - 1152 * j));
  c.mW = rfl(lds0 + (unsigned)w * 1024u);
  c.bB = (unsigned)(g * 2048 + (4 * hh + (i16 >> 2)) * 64 + ((lane >> 4) & 1) * 32 + (((p4 & 1) << 1) | (p4 >> 1)) * 8);
  c.bB1 = c.bB - (unsigned)(g * 1024);
  LAS char* red = L + 122880 + w * 4096 + c.voff; LAS const char* rrd = L + 122880 + (w ^ 4) * 4096 + c.voff;
  wait_bar<0>();
  if (w == 0 && lane == 0) __hip_atomic_store(fl + h, tag, __ATOMIC_RELAXED, __HIP_MEMORY_SCOPE_AGENT);
  o_wpiece<0, 0>(c); o_wpiece<0, 1>(c); o_wpiece<0, 2>(c); o_wpiece<1, 0>(c); o_wpiece<1, 1>(c); o_wpiece<1, 2>(c); o_wpiece<2, 0>(c); o_wpiece<2, 1>(c); o_wpiece<2, 2>(c);
  if (w == 0) {
    const bool need = j == 3 || (lane >= 9 * j && lane < 9 * j + 9);
    while (!__all(!need || __hip_atomic_load(fl + (lane & 31), __ATOMIC_RELAXED, __HIP_MEMORY_SCOPE_AGENT) == tag)) __builtin_amdgcn_s_sleep(1);
    if (h == 31 && lane == 0) __hip_atomic_store(fl + 96, tag, __ATOMIC_RELAXED, __HIP_MEMORY_SCOPE_AGENT);
  }
  asm volatile("s_barrier" ::: "memory");
  o_aload<0>(c); o_aload<1>(c); o_aload<2>(c); o_aload<3>(c); o_aload<4>(c); o_aload<5>(c); o_aload<6>(c); o_aload<7>(c); o_aload<8>(c);
  o_aload<9>(c); o_aload<10>(c); o_aload<11>(c); o_aload<12>(c); o_aload<13>(c); o_aload<14>(c); o_aload<15>(c); o_aload<16>(c); o_aload<17>(c);
  f32x16 acc[2] = {{}, {}};
  o_stage<0, 12>(c, acc); o_stage<1, 15>(c, acc); o_stage<2, 15>(c, acc); o_stage<3, 15>(c, acc); o_stage<4, 9>(c, acc); o_stage<5, 3>(c, acc);
  o_park(red, g ? acc[0] : acc[1]);
  f32x16 prev = g ? acc[1] : acc[0];
  acc[0] = (f32x16){};
  wait_bar<3>(); SBAR();
  o_addlds(prev, rrd); o_publish(sW, c.voff, prev, (unsigned)(b * 32768 + g * 16384 + rb * 4096)); SBAR();
  o_stage<6, -1>(c, acc);
  o_stage<7, 7>(c, acc);
  o_stage<8, 3>(c, acc);
  if (w == 0 && lane == 0) __hip_atomic_store(fl + 32 + h, tag, __ATOMIC_RELAXED, __HIP_MEMORY_SCOPE_AGENT);
  if (g == 1) o_park(red, acc[0]);
  prev = acc[0]; acc[0] = (f32x16){};
  wait_bar<3>(); SBAR();
  if (g == 0) { o_addlds(prev, rrd); o_publish(sW, c.voff, prev, (unsigned)(WS_PQ - WS_QF) + (unsigned)(b * 16384 + rb * 4096)); }
  SBAR();
  o_stage<9, -1>(c, acc);
  o_stage<10, 0>(c, acc);
  if (w == 0 && lane == 0) __hip_atomic_store(fl + 64 + h, tag, __ATOMIC_RELAXED, __HIP_MEMORY_SCOPE_AGENT);
  o_stage<11, -1>(c, acc);
  if (g == 1) o_park(red, acc[0]);
  if (w == 0) {
    const unsigned l3 = c.voff >> 4; const u64* pf = fl + ((l3 & 3) == 1 ? 64 + (h ^ 8) : 32 + (h ^ ((l3 & 1) ? 24 : 16)));
    while (!__all(__hip_atomic_load(pf, __ATOMIC_RELAXED, __HIP_MEMORY_SCOPE_AGENT) == tag)) __builtin_amdgcn_s_sleep(1);
  }
  LBAR();
  if (g == 1) return;
  prev = acc[0];
#pragma unroll
  for (int k = 1; k < 4; ++k) {
    const int bp = b ^ (2 * k);
    const unsigned so = k == 1 ? (unsigned)(WS_PQ - WS_QF) + (unsigned)(bp * 16384 + rb * 4096) : (unsigned)(bp * 32768 + (j & 1) * 16384 + rb * 4096);
#pragma unroll
    for (int qd = 0; qd < 4; ++qd) {
      const f32x4 v = __builtin_bit_cast(f32x4, __builtin_amdgcn_raw_buffer_load_b128(sW, c.voff + (unsigned)(qd * 1024), so, 16));
#pragma unroll
      for (int e = 0; e < 4; ++e) prev[4 * qd + e] += v[e];
    }
  }
  o_addlds(prev, rrd);
  unsigned vo = c.voff; asm volatile("" : "+v"(vo));
  u16* op = out + (size_t)(128 * mi + 32 * rb + ((vo >> 4) & 31)) * 512 + 128 * ni + 32 * j + 8 * (vo >> 9);
  *(u32x4*)(op) = pack8<0>(prev);
  *(u32x4*)(op + 16) = pack8<1>(prev);
}
#undef O_SP
#undef O_FR

#define KVAUX 16
#define WT1 1
#define WT2 3
#define WT3 5
struct PCtx {
  srd_t sA1, sA2, sUK, sUV, sWQ, sWB, sWP, sK, sV;
  unsigned voffA[2], voffB, voffS, voff;
  unsigned soA1, soA2, soB, soS, soKa, mK0, mA, mB2, m2;
  unsigned gv[8];
  u32x2 fl; u64 tag; const u64* flg;
  u32x4 wp;
  unsigned soPW, soPS, soPX;
  u32x4 kvp[8]; unsigned kvo;
  int w, lane, h;
  LAS char* L;
};
#define TS3(x) do { SBAR(); asm volatile("s_memtime %0\n\ts_waitcnt lgkmcnt(0)" : "=s"(x) :: "memory"); SBAR(); } while (0)
template <int T, int I> DI void p_pieceA(const PCtx& c) {
  if constexpr (T < 24) {
    constexpr unsigned st = (unsigned)(T % 3) * 32768u, kt = (unsigned)(T & 7);
    if constexpr (T < 16) dma16c<st + I * 1024u, I * 8192u + kt * 128u>(c.mA, c.voffA[I & 1], c.sA1, c.soA1);
    else                  dma16c<st + I * 1024u, I * 8192u + kt * 128u>(c.mA, c.voffA[I & 1], c.sA2, c.soA2);
  }
}
template <int T, int I> DI void p_pieceB(const PCtx& c) {
  constexpr unsigned st = 98304u + (unsigned)(T & 1) * 32768u, kt = (unsigned)(T & 7);
  if constexpr (T < 16) {
    if constexpr (I < 2) dma16c<st + I * 1024u, kt * 524288u + I * 128u>(c.m2, c.voffB, T < 8 ? c.sUK : c.sUV, c.soB);
  } else if constexpr (T < 24) {
    if constexpr (I < 2) dma16c<st + I * 1024u, kt * 524288u + I * 128u>(c.mB2, c.voffB, c.sWQ, c.soB);
    else if constexpr (I == 2) dma16m<st + 2048u, kt * 196608u>(c.mB2, c.voffS, c.sWB, c.soS);
    else if (c.w == 7) *(LAS u16*)(c.L + st + ((c.lane >> 3) * 6 + 5) * 512 + (c.lane & 7) * 64 + 32) = (u16)c.gv[kt];
  }
}
template <int I, bool ISV> DI void p_kvstore(const PCtx& c) {
  if constexpr (I >= 0 && I < 8) {
    constexpr int mt = I >> 2, nt = (I >> 1) & 1, sh = I & 1;
    constexpr unsigned off = (unsigned)((ISV ? nt * 4 + 2 * mt + sh : mt * 8 + 2 * nt + sh) * 1024);
    __builtin_amdgcn_raw_buffer_store_b128(c.kvp[I], ISV ? c.sV : c.sK, c.kvo + off, 0, KVAUX);
  }
}
#define WARM1(VO, SRD, SO, OFF) dma16c<114688u, (OFF)>(c.m2, VO, SRD, SO)
template <int I> DI void p_warm(PCtx& c) {
  if constexpr (I == 0) { WARM1(c.voff, c.sA1, c.soPX, 0); WARM1(c.voffB, c.sUK, c.soPW, 0); WARM1(c.voffB, c.sUK, c.soPW, 128); }
  if constexpr (I == 1) { WARM1(c.voffB, c.sUV, c.soPW, 0); WARM1(c.voffB, c.sUV, c.soPW, 128); }
  if constexpr (I == 2) { WARM1(c.voff, c.sA2, c.soPX, 0); WARM1(c.voffB, c.sWQ, c.soPW, 0); }
  if constexpr (I == 3) { WARM1(c.voffB, c.sWQ, c.soPW, 128); WARM1(c.voffS, c.sWB, c.soPS, 0); }
}
#undef WARM1
DI void poll_spin(const u64* f, u64 tag, int lane) {
  while (!__all(__hip_atomic_load(f + (lane & 7), __ATOMIC_RELAXED, __HIP_MEMORY_SCOPE_AGENT) == tag)) __builtin_amdgcn_s_sleep(2);
}
DI void poll_issue(u32x2& fl, const u64* f, int lane) { asm volatile("global_load_dwordx2 %0, %1, off sc1" : "+v"(fl) : "v"(f + (lane & 7)) : "memory"); }
DI void poll_check(u32x2& fl, const u64* f, u64 tag, int lane) {
  asm volatile("" : "+v"(fl));
  if (!__all(fl[0] == (unsigned)tag && fl[1] == (unsigned)(tag >> 32))) poll_spin(f, tag, lane);
  asm volatile("" ::: "memory");
}
template <int B> DI void p_hook(PCtx& c) {
  if constexpr (B == 17) { if (c.w == 0) dma16c<155648u, 0u>(c.mA, c.voff, c.sWP, (unsigned)c.h * 512u); }
  if constexpr (B == 21) {
    if (c.w == 0) { if (c.lane == 0) __hip_atomic_store((u64*)c.flg + (blockIdx.x >> 5), c.tag, __ATOMIC_RELAXED, __HIP_MEMORY_SCOPE_AGENT); }
    {
      const int r32 = c.lane & 31, hh = c.lane >> 5; LAS const u16* wb = (LAS const u16*)(c.L + 155648) + (r32 & 15); const unsigned sg = r32 >= 16 ? 0x80008000u : 0u;
#pragma unroll
      for (int jj = 0; jj < 4; ++jj) { const int d0 = 8 * (jj >> 1) + 4 * hh + 2 * (jj & 1); c.wp[jj] = ((unsigned)wb[d0 * 16] | ((unsigned)wb[(d0 + 1) * 16] << 16)) ^ sg; }
    }
  }
  if constexpr (B == 22) { if (c.w == 0) poll_issue(c.fl, c.flg, c.lane); }
  if constexpr (B == 23) { if (c.w == 0) poll_check(c.fl, c.flg, c.tag, c.lane); }
  if constexpr (B == 24) {
    dma16s<0u, 0u>(c.mK0, c.voff, c.sK, c.soKa); dma16s<1024u, 1024u>(c.mK0, c.voff, c.sK, c.soKa);
    dma16s<2048u, 2048u>(c.mK0, c.voff, c.sK, c.soKa); dma16s<3072u, 3072u>(c.mK0, c.voff, c.sK, c.soKa);
  }
}

template <int PASS, int MT, int NT, int NSUB, bool TRANS>
struct PPass {
  PCtx& c; f32x16 (&acc)[MT][NT];
  unsigned aB[2][4], bB;
  bf16x8 fa[2][MT], fb[2][NT];
  template <int SET, int T, int KS> DI void load() {
    constexpr int bo = (T & 1) * 32768 + KS * (2 * NSUB * 512), sl = T % 3;
#pragma unroll
    for (int nt = 0; nt < NT; ++nt) fb[SET][nt] = trfrag(c.L + bB + bo + nt * 512, c.L + bB + bo + nt * 512 + 256);
#pragma unroll
    for (int mt = 0; mt < MT; ++mt) fa[SET][mt] = ldsfrag(c.L + aB[sl >> 1][KS] + (sl & 1) * 32768 + mt * 4096);
  }
  template <int SET, int I> DI void mma1() {
    if constexpr (I < MT * NT) {
      constexpr int mt = I / NT, nt = I % NT;
      acc[mt][nt] = TRANS ? MFMA(fb[SET][nt], fa[SET][mt], acc[mt][nt]) : MFMA(fa[SET][mt], fb[SET][nt], acc[mt][nt]);
    }
  }
  template <int SET, int T, int I0, int S0> DI void mmaA() {
    mma1<SET, 0>(); SBAR(); p_pieceA<T, I0>(c); SBAR(); mma1<SET, 1>(); SBAR(); p_kvstore<S0, PASS == 2>(c); SBAR(); mma1<SET, 2>(); SBAR(); p_pieceA<T, I0 + 1>(c); SBAR();
    mma1<SET, 3>(); mma1<SET, 4>(); mma1<SET, 5>();
  }
  template <int t> DI void tile() {
    constexpr int T = PASS * 8 + t;
    constexpr int SA = PASS == 1 ? t : (PASS == 2 && t < 4 ? 2 * t : -1), SB = PASS == 2 && t < 4 ? 2 * t + 1 : -1;
    constexpr int WI = T == WT1 ? 1 : (T == WT2 ? 2 : (T == WT3 ? 3 : 0));
    constexpr int NW = (T == 0 ? 3 : 0) + (WI ? 2 : 0);
    constexpr int NST = (SA >= 0 ? 1 : 0) + (SB >= 0 ? 1 : 0) + NW;
    SBAR(); load<1, T, 1>(); SBAR(); mmaA<0, T + 2, 0, SA>();
    SBAR(); load<0, T, 2>(); SBAR(); mmaA<1, T + 2, 2, -1>();
    SBAR(); load<1, T, 3>(); SBAR(); if constexpr (WI != 0) p_warm<WI>(c); SBAR(); mmaA<0, 24, 0, SB>();
    SBAR();
    if constexpr (T == 23) LBAR(); else if constexpr (T == 22) wait_bar<0>(); else wait_bar<4 + NST>();
    SBAR();
    if constexpr (t < 7) load<0, T + 1, 0>();
    SBAR();
    mma1<1, 0>(); p_pieceB<T + 2, 0>(c); SBAR();
    mma1<1, 1>(); p_pieceB<T + 2, 1>(c); SBAR();
    mma1<1, 2>(); p_pieceB<T + 2, 2>(c); SBAR();
    mma1<1, 3>(); p_pieceB<T + 2, 3>(c); SBAR();
    mma1<1, 4>(); mma1<1, 5>(); SBAR();
    p_hook<T + 1>(c); SBAR();
  }
  DI void run(unsigned arow0, unsigned nt0) {
    const int lane = c.lane, r = lane & 31, hh = lane >> 5, i16 = lane & 15;
#pragma unroll
    for (int ks = 0; ks < 4; ++ks) { aB[0][ks] = (arow0 + r) * 128u + (unsigned)(((2 * ks + hh) ^ ((r >> 1) & 7)) * 16); aB[1][ks] = aB[0][ks] + 65536u; asm volatile("" : "+v"(aB[1][ks])); }
    bB = 98304u + (unsigned)(hh * NSUB + nt0) * 512u + (unsigned)((i16 >> 2) * 64 + ((lane >> 4) & 1) * 32 + (i16 & 3) * 8); asm volatile("" : "+v"(bB));
#pragma unroll
    for (int mt = 0; mt < MT; ++mt)
#pragma unroll
      for (int nt = 0; nt < NT; ++nt) acc[mt][nt] = (f32x16){};
    load<0, PASS * 8, 0>();
    tile<0>(); tile<1>(); tile<2>(); tile<3>(); tile<4>(); tile<5>(); tile<6>(); tile<7>();
  }
};
template <int PASS, int MT, int NT, int NSUB, bool TRANS>
DI void p_pass(PCtx& c, f32x16 (&acc)[MT][NT], unsigned arow0, unsigned nt0) { PPass<PASS, MT, NT, NSUB, TRANS> p{c, acc}; p.run(arow0, nt0); }

DI void softmax2(f32x16& x) {
#pragma unroll
  for (int s = 0; s < 2; ++s) {
    float mx = fmaxf(fmaxf(fmaxf(x[8 * s], x[8 * s + 1]), fmaxf(x[8 * s + 2], x[8 * s + 3])), fmaxf(fmaxf(x[8 * s + 4], x[8 * s + 5]), fmaxf(x[8 * s + 6], x[8 * s + 7])));
    mx = swap32_max(mx);
    const float cc = -mx * 1.4426950408889634f;
    float sum = 0.f;
#pragma unroll
    for (int j = 0; j < 8; ++j) { x[8 * s + j] = __builtin_amdgcn_exp2f(__builtin_fmaf(x[8 * s + j], 1.4426950408889634f, cc)); sum += x[8 * s + j]; }
    const float inv = __builtin_amdgcn_rcpf(swap32_add(sum));
#pragma unroll
    for (int j = 0; j < 8; ++j) x[8 * s + j] *= inv;
  }
}

DI void mfma_acc(f32x16& c, bf16x8 a, bf16x8 b) { asm volatile("v_mfma_f32_32x32x16_bf16 %0, %1, %2, %0" : "+v"(c) : "v"(a), "v"(b)); }
DI void mfma_acc2(f32x16& c, bf16x8 a, bf16x8 b, bf16x8 nx) { asm volatile("v_mfma_f32_32x32x16_bf16 %0, %1, %2, %0" : "+v"(c) : "v"(a), "v"(b), "v"(nx)); }
DI void mfma_init2(f32x16& c, bf16x8 a, bf16x8 b, const f32x16& ci, bf16x8 nx) { asm volatile("v_mfma_f32_32x32x16_bf16 %0, %1, %2, %3" : "=&v"(c) : "v"(a), "v"(b), "v"(ci), "v"(nx)); }
DI float sum2(unsigned pk, float acc) { return __builtin_amdgcn_fdot2_f32_bf16(__builtin_bit_cast(bf16x2, pk), __builtin_bit_cast(bf16x2, 0x3f803f80u), acc, false); }
DI float sum8(bf16x8 p, float acc) { const u32x4 u = __builtin_bit_cast(u32x4, p); return sum2(u[3], sum2(u[2], sum2(u[1], sum2(u[0], acc)))); }
DI float max3f(float a, float b, float c) { float r; asm("v_max3_f32 %0, %1, %2, %3" : "=v"(r) : "v"(a), "v"(b), "v"(c)); return r; }
DI float max2f(float a, float b) { float r; asm("v_max_f32_e32 %0, %1, %2" : "=v"(r) : "v"(a), "v"(b)); return r; }
#define TS2X(a)
#define TS2V()
#define STP_PARAM
#define STP_ARG
#define TS2(acc)
#define NMV 4
#define TRAIL 4
#define ADIST 6
template <bool DOQK, bool DOPV>
DI void a_mphase(LAS const char* kp, LAS const char* vp, const bf16x8 (&q)[8], bf16x8 (&p)[4], f32x16 (&S)[2], f32x16 (&O)[4], const f32x16& negm, float& l, float& pm0,
                 unsigned mV, unsigned voff, srd_t sV, unsigned oV STP_PARAM) {
  constexpr int N = (DOQK ? 16 : 0) + (DOPV ? 16 : 0), D = ADIST;
  bf16x8 f[N];
  unsigned pw[8]; float ls = 0.f, e0 = 0.f;
#define A_ISQK(i) (DOQK && (!DOPV || (((i) >> 3) & 1) == 0))
#define A_IX(i) ((DOPV && DOQK) ? (((i) >> 4) * 8 + ((i) & 7)) : (i))
#define A_LD(i) do { if constexpr ((i) < N) { if constexpr (A_ISQK(i)) f[i] = ldsfrag(kp + A_IX(i) * 1024); \
                     else f[i] = ldsfrag(vp + ((A_IX(i) & 3) * 4 + (A_IX(i) >> 2)) * 1024); } } while (0)
#define A_MM(i) do { if constexpr (A_ISQK(i)) { if constexpr ((A_IX(i) & 7) == 0) mfma_init2(S[A_IX(i) >> 3], f[i], q[0], negm, f[(i) + 1]); \
                       else if constexpr ((i) & 1) mfma_acc(S[A_IX(i) >> 3], f[i], q[A_IX(i) & 7]); else mfma_acc2(S[A_IX(i) >> 3], f[i], q[A_IX(i) & 7], f[((i) + 1) % N]); } \
                     else if constexpr ((i) & 1) mfma_acc(O[A_IX(i) & 3], f[i], p[A_IX(i) >> 2]); else mfma_acc2(O[A_IX(i) & 3], f[i], p[A_IX(i) >> 2], f[((i) + 1) % N]); } while (0)
#define A_SM(e) do { const float x_ = __builtin_amdgcn_exp2f(S[1][(e) & 15]); ls += x_; \
                     if constexpr ((e) & 1) { pw[((e) & 15) >> 1] = cvtpk(e0, x_); asm volatile("" : "+v"(pw[((e) & 15) >> 1]), "+v"(ls)); } else { e0 = x_; asm volatile("" : "+v"(e0), "+v"(ls)); } \
                     if constexpr ((e) == 7) p[2] = asbf((u32x4){pw[0], pw[1], pw[2], pw[3]}); if constexpr ((e) == 15) p[3] = asbf((u32x4){pw[4], pw[5], pw[6], pw[7]}); } while (0)
#define A_MX(j) do { if constexpr ((j) == 0) pm0 = max2f(S[0][0], S[0][1]); else pm0 = max3f(pm0, S[0][2 * ((j) & 7)], S[0][2 * ((j) & 7) + 1]); asm volatile("" : "+v"(pm0)); } while (0)
#define A_STEP(i) do { if constexpr ((i) < N) { A_LD((i) + D); if constexpr ((i) == N - TRAIL) { SBAR(); TS2(a); LBAR(); TS2(b); SBAR(); } A_MM(i); \
                     if constexpr (DOQK && DOPV && (i) < 16) A_SM(i); \
                     if constexpr (DOQK && DOPV && (i) >= 16 && (i) < 24) A_MX((i) - 16); \
                     if constexpr (DOQK && DOPV && NMV >= 4 && (i) == 17) dma16o<0>(mV, voff, sV, oV); \
                     if constexpr (DOQK && DOPV && NMV >= 4 && (i) == 20) dma16n<1024>(voff, sV, oV); \
                     if constexpr (DOQK && DOPV && NMV >= 4 && (i) == 23) dma16n<2048>(voff, sV, oV); \
                     if constexpr (DOQK && DOPV && NMV >= 4 && (i) == 25) dma16n<3072>(voff, sV, oV); \
                     if constexpr (!DOQK && (i) < 8) { A_SM(2 * (i)); A_SM(2 * (i) + 1); } SBAR(); } } while (0)
  SBAR();
  __builtin_amdgcn_s_setprio(1);
  A_LD(0); A_LD(1); A_LD(2); A_LD(3); if constexpr (D > 4) { A_LD(4); A_LD(5); } SBAR();
  A_STEP(0); A_STEP(1); A_STEP(2); A_STEP(3); A_STEP(4); A_STEP(5); A_STEP(6); A_STEP(7);
  A_STEP(8); A_STEP(9); A_STEP(10); A_STEP(11); A_STEP(12); A_STEP(13); A_STEP(14); A_STEP(15);
  A_STEP(16); A_STEP(17); A_STEP(18); A_STEP(19); A_STEP(20); A_STEP(21); A_STEP(22); A_STEP(23);
  A_STEP(24); A_STEP(25); A_STEP(26); A_STEP(27); A_STEP(28); A_STEP(29); A_STEP(30); A_STEP(31);
  if constexpr (DOPV) l += ls;
  if constexpr (!DOPV) { NOP16(); SBAR(); A_MX(0); A_MX(1); A_MX(2); A_MX(3); A_MX(4); A_MX(5); A_MX(6); A_MX(7); }
  __builtin_amdgcn_s_setprio(0);
  TS2X(c);
#undef A_ISQK
#undef A_IX
#undef A_LD
#undef A_MM
#undef A_SM
#undef A_MX
#undef A_STEP
}

DI void a_finish(const f32x16& a0, const f32x16& a1, float ma, float la, LAS const char* xr, float mb, float lb, float gate, srd_t sC, unsigned voff, unsigned soff) {
  const float mt = fmaxf(ma, mb);
  const float fa = __builtin_amdgcn_exp2f(ma - mt), fb = __builtin_amdgcn_exp2f(mb - mt);
  const float sc = gate / (la * fa + lb * fb);
  const float wa = fa * sc, wb = fb * sc;
#pragma unroll
  for (int t = 0; t < 2; ++t) {
    const f32x16& a = t ? a1 : a0;
#pragma unroll
    for (int s = 0; s < 2; ++s) {
      const f32x4 b0 = *(LAS const f32x4*)(xr + t * 4096 + s * 2048), b1 = *(LAS const f32x4*)(xr + t * 4096 + s * 2048 + 1024);
      const u32x4 o = {cvtpk(a[8 * s + 0] * wa + b0[0] * wb, a[8 * s + 1] * wa + b0[1] * wb), cvtpk(a[8 * s + 2] * wa + b0[2] * wb, a[8 * s + 3] * wa + b0[3] * wb),
                       cvtpk(a[8 * s + 4] * wa + b1[0] * wb, a[8 * s + 5] * wa + b1[1] * wb), cvtpk(a[8 * s + 6] * wa + b1[2] * wb, a[8 * s + 7] * wa + b1[3] * wb)};
      st128_wt(o, sC, voff + (unsigned)((2 * t + s) * 1024), soff);
      SBAR();
    }
  }
}
DI void a_send(const f32x16& a0, const f32x16& a1, LAS char* xr) {
#pragma unroll
  for (int g = 0; g < 4; ++g) {
    *(LAS f32x4*)(xr + g * 1024) = (f32x4){a0[4 * g], a0[4 * g + 1], a0[4 * g + 2], a0[4 * g + 3]};
    *(LAS f32x4*)(xr + 4096 + g * 1024) = (f32x4){a1[4 * g], a1[4 * g + 1], a1[4 * g + 2], a1[4 * g + 3]};
  }
}

__global__ __launch_bounds__(512, 2) void kF(const u16* __restrict__ x, const u16* __restrict__ ckv, const u16* __restrict__ wq_, const u16* __restrict__ wuk,
                                             const u16* __restrict__ wuv, const u16* __restrict__ wqkv, const u16* __restrict__ wphi, const u16* __restrict__ gatew,
                                             const u16* __restrict__ wo_a, const u16* __restrict__ wo_b, char* __restrict__ ws, u16* __restrict__ out) {
  extern __shared__ __attribute__((aligned(16))) char lds[];
  LAS char* L = (LAS char*)lds; const unsigned lds0 = (unsigned)(uintptr_t)L;
  const int tid = threadIdx.x, lane = tid & 63, w = __builtin_amdgcn_readfirstlane(tid >> 6);
  const int b = blockIdx.x, h = (b & 7) * 4 + ((b >> 3) & 3), r = b >> 5;
  const int g = w >> 2, wq = w & 3, hh = lane >> 5, r32 = lane & 31;
  const int X = 15 - r, Y = r, nX = 16 - r;
  constexpr int NS = 17;
  const unsigned voff = (unsigned)lane * 16u;
  TS(t0);
  bf16x8 q[8];
  u64 tag, tag2; const u64* flg = (const u64*)(ws + WS_FLG + (size_t)h * 256);
  const srd_t sK = mksrd(ws + WS_KF + (size_t)h * 32 * 16384, 32u * 16384u), sV = mksrd(ws + WS_VF + (size_t)h * 32 * 16384, 32u * 16384u);
  const srd_t sW = mksrd(ws + WS_QF, (unsigned)(WS_DBG - WS_QF));
  const unsigned ringK = rfl(lds0 + (g ? 98304u : 32768u) + (unsigned)wq * 4096u), ringV = rfl(lds0 + (g ? 0u : 65536u) + (unsigned)wq * 4096u);
  {
    PCtx c;
    c.fl = (u32x2){0u, 0u}; c.wp = (u32x4){0u, 0u, 0u, 0u};
    c.L = L; c.w = w; c.lane = lane; c.h = h; c.voff = voff; c.flg = flg; c.sK = sK; c.sV = sV;
    c.soA1 = rfl((unsigned)(256 * r + 32 * w) * 1024u);
    c.soA2 = rfl((unsigned)(w < 4 ? 128 * X + 32 * w : 128 * Y + 32 * (w - 4)) * 1024u);
    c.soB = rfl((unsigned)(8 * w) * 8192u + (unsigned)h * 256u);
    c.soS = rfl((unsigned)(8 * w) * 3072u + (unsigned)h * 32u);
    c.soPW = rfl((unsigned)(64 * r + 8 * w) * 8192u + (unsigned)h * 256u); c.soPS = rfl((unsigned)(64 * r + 8 * w) * 3072u + (unsigned)h * 32u);
    c.soPX = rfl((unsigned)(256 * r + 8 * h + w) * 1024u);
    c.soKa = rfl((unsigned)(g * 16384 + wq * 4096)); c.mK0 = ringK;
    c.mA = rfl(lds0 + (unsigned)w * 4096u); c.mB2 = rfl(lds0 + (unsigned)w * 3072u); c.m2 = rfl(lds0 + (unsigned)w * 2048u);
    c.sA1 = mksrd(ckv, 2048u * 1024u); c.sA2 = mksrd(x, 2048u * 1024u);
    c.sUK = mksrd(wuk, 512u * 8192u); c.sUV = mksrd(wuv, 512u * 8192u); c.sWQ = mksrd(wq_, 512u * 8192u); c.sWB = mksrd(wqkv, 512u * 3072u); c.sWP = mksrd(wphi, 32u * 512u);
#pragma unroll
    for (int k = 0; k < 2; ++k) c.voffA[k] = (unsigned)((lane >> 3) * 1024 + (((lane & 7) ^ ((4 * k + (lane >> 4)) & 7)) * 16));
    c.voffB = (unsigned)(((lane >> 2) & 7) * 8192 + (lane >> 5) * 64 + (lane & 3) * 16);
    { const int ch = lane & 3, sub = lane >> 5;
      c.voffS = (unsigned)(((lane >> 2) & 7) * 3072 + (sub == 0 ? (ch < 2 ? ch * 16 : 1024 + (ch - 2) * 16) : 2048 + (ch & 1) * 16)); }
    u32x2 ep, ep2;
    asm volatile("global_load_dwordx2 %0, %1, off sc1" : "=v"(ep) : "v"(flg + 16) : "memory");
    asm volatile("global_load_dwordx2 %0, %1, off sc1" : "=v"(ep2) : "v"((const u64*)(ws + WS_FLG + 65536 + (size_t)r * 1024) + 96) : "memory");
#pragma unroll
    for (int k = 0; k < 8; ++k) asm volatile("global_load_ushort %0, %1, off offset:%2" : "=v"(c.gv[k]) : "v"(gatew + lane), "i"(k * 128) : "memory");
    p_pieceA<0, 0>(c); p_pieceA<0, 1>(c); p_pieceA<0, 2>(c); p_pieceA<0, 3>(c);
    p_pieceB<0, 0>(c); p_pieceB<0, 1>(c);
    p_pieceB<1, 0>(c); p_pieceB<1, 1>(c);
    p_pieceA<1, 0>(c); p_pieceA<1, 1>(c); p_pieceA<1, 2>(c); p_pieceA<1, 3>(c);
    p_warm<0>(c);
    wait_bar<9>();
    asm volatile("" : "+v"(ep), "+v"(ep2), "+v"(c.gv[0]), "+v"(c.gv[1]), "+v"(c.gv[2]), "+v"(c.gv[3]), "+v"(c.gv[4]), "+v"(c.gv[5]), "+v"(c.gv[6]), "+v"(c.gv[7]));
    tag = (((u64)rfl(ep[1]) << 32) | rfl(ep[0])) + 1ull; c.tag = tag;
    tag2 = (((u64)rfl(ep2[1]) << 32) | rfl(ep2[0])) + 1ull;
    {
      f32x16 acc[2][2];
      const int wm = w >> 1, wn = w & 1;
      p_pass<0, 2, 2, 4, true>(c, acc, 64u * wm, 2u * wn);
#pragma unroll
      for (int i = 0; i < 4; ++i) { c.kvp[2 * i] = pack8<0>(acc[i >> 1][i & 1]); c.kvp[2 * i + 1] = pack8<1>(acc[i >> 1][i & 1]); }
      c.kvo = voff + (unsigned)((4 * r + wm) * 16384 + 4 * wn * 1024);
      p_pass<1, 2, 2, 4, false>(c, acc, 64u * wm, 2u * wn);
#pragma unroll
      for (int i = 0; i < 4; ++i) { c.kvp[2 * i] = pack8<0>(acc[i >> 1][i & 1]); c.kvp[2 * i + 1] = pack8<1>(acc[i >> 1][i & 1]); }
      c.kvo = voff + (unsigned)((4 * r + wm) * 16384 + 8 * wn * 1024);
    }
    TS(t1);
    f32x16 acc[2][3];
    p_pass<2, 2, 3, 6, true>(c, acc, 64u * wq, 3u * g);
    TS(t2);
    if (wq < 2) {
#pragma unroll
      for (int mt = 0; mt < 2; ++mt)
#pragma unroll
        for (int nt = 0; nt < 3; ++nt)
          if (g == 0 || nt == 0) {
            const f32x16 v = acc[mt][nt] * QSCALE; LAS char* qd = L + 131072 + ((2 * wq + mt) * 8 + 2 * (3 * g + nt)) * 1024 + lane * 16;
            *(LAS u32x4*)qd = pack8<0>(v); *(LAS u32x4*)(qd + 1024) = pack8<1>(v);
          }
    }
    LAS char* Ik = L + (g ? 16384 : 81920) + wq * 4096; LAS char* Iv = Ik + 2048;
    u32x4 hq, hk, hv; float gl;
    if (g == 1) {
      LAS char* ho = L + 81920 + wq * 4096 + lane * 16;
      *(LAS u32x4*)ho = pack8<0>(acc[1][1]); *(LAS u32x4*)(ho + 1024) = pack8<1>(acc[1][1]); *(LAS u32x4*)(ho + 2048) = pack8<0>(acc[1][2]);
      *(LAS float*)(L + 81920 + wq * 4096 + 3072 + lane * 4) = acc[1][2][8];
      hq = pack8<0>(acc[0][1]); hk = pack8<1>(acc[0][1]); hv = pack8<0>(acc[0][2]); gl = acc[0][2][8];
    }
    TS(ts_a);
    LBAR();
    TS(ts_b);
#pragma unroll
    for (int ks = 0; ks < 8; ++ks) q[ks] = ldsfrag(L + 131072 + wq * 8192 + ks * 1024 + lane * 16);
    if (g == 0) {
      hq = *(LAS const u32x4*)(Ik + lane * 16); hk = *(LAS const u32x4*)(Ik + 1024 + lane * 16); hv = *(LAS const u32x4*)(Ik + 2048 + lane * 16); gl = *(LAS const float*)(Ik + 3072 + lane * 4);
    }
    if (wq >= 2) {
#pragma unroll
      for (int mt = 0; mt < 2; ++mt)
#pragma unroll
        for (int nt = 0; nt < 3; ++nt)
          if (g == 0 || nt == 0) {
            const f32x16 v = acc[mt][nt] * QSCALE; const unsigned so = (unsigned)(b * 32768 + ((2 * (wq - 2) + mt) * 8 + 2 * (3 * g + nt)) * 1024);
            st128(pack8<0>(v), sW, voff, so); st128(pack8<1>(v), sW, voff + 1024u, so);
          }
    }
    {
      const int i16 = lane & 15, rt = 2 * wq + (g ^ 1);
      const int sw = (r32 >> 1) & 7; LAS char* wr = Ik + r32 * 64;
      const int gr = ((lane >> 4) & 1) * 4 + (i16 & 3), q4 = i16 >> 2;
      unsigned tb[2];
#pragma unroll
      for (int u = 0; u < 2; ++u) tb[u] = (unsigned)((8 * hh + 4 * u + q4) * 64 + ((gr ^ (4 * hh + 2 * u + (q4 >> 1))) * 8));
      f32x16 pq = MFMA(asbf(c.wp), asbf(hq), (f32x16){});
      f32x16 pk = MFMA(asbf(c.wp), asbf(hk), (f32x16){});
      LWAIT();
      *(LAS u32x2*)(wr + 2048 + ((0 + hh) ^ sw) * 8) = (u32x2){hv[0], hv[1]};
      *(LAS u32x2*)(wr + 2048 + ((2 + hh) ^ sw) * 8) = (u32x2){hv[2], hv[3]};
      *(LAS u32x2*)(wr + 2048 + ((4 + hh) ^ sw) * 8) = (u32x2){hh == 0 ? 0x00003f80u : 0u, 0u};
      *(LAS u32x2*)(wr + 2048 + ((6 + hh) ^ sw) * 8) = (u32x2){0u, 0u};
      softmax2(pk);
#pragma unroll
      for (int k = 0; k < 4; ++k) *(LAS u32x2*)(wr + ((2 * k + hh) ^ sw) * 8) = (u32x2){cvtpk(pk[4 * k], pk[4 * k + 1]), cvtpk(pk[4 * k + 2], pk[4 * k + 3])};
      LWAIT();
      f32x16 kv = {};
#pragma unroll
      for (int ks = 0; ks < 2; ++ks) kv = MFMA(trfrag(Ik + tb[0] + ks * 1024, Ik + tb[1] + ks * 1024), trfrag(Iv + tb[0] + ks * 1024, Iv + tb[1] + ks * 1024), kv);
      softmax2(pq);
      const unsigned so = (unsigned)(WS_PQ - WS_QF) + (unsigned)(b * 16384 + rt * 2048);
      st128(pack8<0>(pq), sW, voff, so); st128(pack8<1>(pq), sW, voff + 1024u, so);
      const float gt = __builtin_amdgcn_rcpf(1.0f + __builtin_amdgcn_exp2f(-1.4426950408889634f * swap32_lo(gl)));
      __builtin_amdgcn_raw_buffer_store_b32(__float_as_uint(gt), sW, (unsigned)r32 * 4u + (unsigned)(WS_GW - WS_QF) + (unsigned)(b * 1024 + rt * 128), 0, 0);
      LWAIT();
#pragma unroll
      for (int k = 0; k < 4; ++k) *(LAS f32x4*)(Ik + k * 1024 + lane * 16) = (f32x4){kv[4 * k], kv[4 * k + 1], kv[4 * k + 2], kv[4 * k + 3]};
    }
    dma16s<16384u, 32768u>(ringK, voff, sK, c.soKa); dma16s<16384u + 1024u, 32768u + 1024u>(ringK, voff, sK, c.soKa);
    dma16s<16384u + 2048u, 32768u + 2048u>(ringK, voff, sK, c.soKa); dma16s<16384u + 3072u, 32768u + 3072u>(ringK, voff, sK, c.soKa);
    dma16s<0u, 0u>(ringV, voff, sV, c.soKa); dma16s<1024u, 1024u>(ringV, voff, sV, c.soKa);
    dma16s<2048u, 2048u>(ringV, voff, sV, c.soKa); dma16s<3072u, 3072u>(ringV, voff, sV, c.soKa);
    TS(ts_c);
    wait_bar<11>();
    if (g == 1) {
      f32x4 s4 = *(LAS const f32x4*)(L + 16384 + wq * 1024 + lane * 16);
#pragma unroll
      for (int k = 1; k < 8; ++k) s4 += *(LAS const f32x4*)(L + (k < 4 ? 16384 : 81920) + (k & 3) * 4096 + wq * 1024 + lane * 16);
      st128_wt(__builtin_bit_cast(u32x4, s4), sW, voff, (unsigned)(WS_KVP - WS_QF) + (unsigned)((h * 8 + r) * 4096 + wq * 1024));
      LBAR();
    }
  }
  TS(t3);
  f32x16 O[4], S[2], keep0 = {}, keep1 = {}, negm = {};
#pragma unroll
  for (int d = 0; d < 4; ++d) O[d] = (f32x16){};
  bf16x8 p[4];
  float m = 0.f, l = 0.f, mk = 0.f, lk = 0.f, pm0 = 0.f;
  float gate[2] = {0.f, 0.f}; f32x2 ps = {0.f, 0.f};
  u32x2 fl2 = {0u, 0u};
  u32x4 warm_dummy; unsigned warm_dm1;
  LAS const char* kbase = L + (g ? 98304 : 32768) + lane * 16;
  LAS const char* vbase = L + (g ? 0 : 65536) + lane * 16;
  LAS char* xr = L + 131072 + wq * 8192 + lane * 16;
#define TILE(s) ((s) < nX ? 2 * (s) + g : 2 * ((s) - nX) + g)
#define CF_SO(blkrow, half) ((unsigned)(WS_CF - WS_QF) + (unsigned)(((4 * (blkrow) + wq) * 288 + 8 * h + 4 * (half)) * 1024))
#define ST_SO(wv) ((unsigned)(WS_ST - WS_QF) + (unsigned)(b * 2048 + (wv) * 256))
#define ST_LOAD(wv) asm volatile("buffer_load_dwordx2 %0, %1, %2, %3 offen" : "+v"(ps) : "v"((unsigned)r32 * 8u), "s"(sW), "s"(rfl(ST_SO(wv))) : "memory")
#define L_SOFTMAX(DIAG, FIRST, D0, D1, D2, D3, D4, D5, D6, D7) do { \
    if (DIAG) { int rel = 32 * wq + r32 - 64 * g - 4 * hh; \
      _Pragma("unroll") for (int e = 0; e < 16; ++e) { const int cc = (e & 3) + 8 * (e >> 2); if ((e & 3) == 0) { asm volatile("" : "+v"(rel)); SBAR(); } \
        if (cc > rel) S[0][e] = -__builtin_inff(); if (cc + 32 > rel) S[1][e] = -__builtin_inff(); } \
      pm0 = max2f(S[0][0], S[0][1]); _Pragma("unroll") for (int e = 2; e < 16; e += 2) pm0 = max3f(pm0, S[0][e], S[0][e + 1]); } \
    float pm = max3f(pm0, S[1][0], S[1][1]), pn = max3f(S[1][2], S[1][3], S[1][4]); \
    pm = max3f(pm, S[1][5], S[1][6]); pn = max3f(pn, S[1][7], S[1][8]); pm = max3f(pm, S[1][9], S[1][10]); pn = max3f(pn, S[1][11], S[1][12]); \
    pm = max3f(pm, S[1][13], S[1][14]); pm = max3f(pm, pn, S[1][15]); \
    { auto rr = __builtin_amdgcn_permlane32_swap(__float_as_uint(pm), __float_as_uint(pm), false, false); pm = max2f(__uint_as_float(rr[0]), __uint_as_float(rr[1])); } \
    SBAR(); TS2X(f); D0; SBAR(); \
    const bool first_ = (FIRST); \
    if (first_ || __builtin_expect(__any(pm > THR), 0)) { \
      NOP16(); SBAR(); \
      const float dl = fmaxf(pm, first_ ? -1e30f : 0.f); \
      m += dl; \
      if (!first_) { const float f = __builtin_amdgcn_exp2f(-dl); l *= f; _Pragma("unroll") for (int d = 0; d < 4; ++d) O[d] = O[d] * f; } \
      _Pragma("unroll") for (int e = 0; e < 16; ++e) { S[0][e] -= dl; S[1][e] -= dl; negm[e] = -m; } \
    } \
    TS2X(g); float ls0 = l, ls1 = 0.f; unsigned pw[8]; \
    SM_GRP(0, D1); SM_GRP(2, D2); SM_GRP(4, D3); SM_GRP(6, D4); SM_GRP(8, D5); SM_GRP(10, D6); SM_GRP(12, D7); SM_GRP(14, (void)0); \
    l = ls0 + ls1; \
    p[0] = asbf((u32x4){pw[0], pw[1], pw[2], pw[3]}); p[1] = asbf((u32x4){pw[4], pw[5], pw[6], pw[7]}); } while (0)
#define SM_GRP(e0, DMA) do { const float x0 = __builtin_amdgcn_exp2f(S[0][e0]), x1 = __builtin_amdgcn_exp2f(S[0][(e0) + 1]); \
      ls0 += x0; ls1 += x1; pw[(e0) >> 1] = cvtpk(x0, x1); asm volatile("" : "+v"(pw[(e0) >> 1]), "+v"(ls0), "+v"(ls1)); DMA; SBAR(); } while (0)
#define L_SUM() do { } while (0)
  float mo = 0.f, lo = 0.f;
  auto l_pre = [&](int s) {
    if (s == nX) {
      NOP16(); SBAR();
      mo = m; lo = swap32_add(l);
      if (g == 1) ST_LOAD(w - 4);
      __builtin_amdgcn_raw_buffer_store_b64(__builtin_bit_cast(u32x2, (f32x2){mo, lo}), sW, (unsigned)r32 * 8u, ST_SO(w), 0);
      if (g == 0) { a_send(O[2], O[3], xr); keep0 = O[0]; keep1 = O[1]; mk = mo; lk = lo; }
      m = 0.f; l = 0.f;
    }
    if (g == 0 && s == nX + 1) ST_LOAD(w + 4);
  };
  auto l_post = [&](int s) {
    if (s == nX) {
      if (g == 1) {
        wait_vm<8 - NMV>(); asm volatile("" : "+v"(ps));
        a_finish(O[2], O[3], mo, lo, xr, ps[0], ps[1], gate[0], sW, voff, CF_SO(X, 1));
        LWAIT();
        a_send(O[0], O[1], xr);
      }
#pragma unroll
      for (int d = 0; d < 4; ++d) O[d] = (f32x16){};
    }
    if (g == 0 && s == nX + 1) {
      wait_vm<8 - NMV>(); asm volatile("" : "+v"(ps));
      a_finish(keep0, keep1, mk, lk, xr, ps[0], ps[1], gate[0], sW, voff, CF_SO(X, 0));
    }
  };
  unsigned mK = ringK, mV = ringV + 16384u, oK = (unsigned)(4 + g) * 16384u + (unsigned)wq * 4096u, oV = (unsigned)(2 + g) * 16384u + (unsigned)wq * 4096u;
  LAS const char* kp = kbase + 16384; LAS const char* vp = vbase;
#define LV(x) do { if constexpr (NMV == 0) { x; } } while (0)
#define A_TAIL() do { \
    asm volatile("" : "+v"(p[0]), "+v"(p[1]), "+v"(l), "+v"(S[1])); \
    SBAR(); TS2(d); TS2V(); wait_bar<8 - NMV>(); TS2(e); SBAR(); \
    a_mphase<true, true>(kp, vp, q, p, S, O, negm, l, pm0, mV, voff, sV, oV STP_ARG); oV += 32768u; \
    mK ^= 16384u; mV ^= 16384u; kp = (LAS const char*)((unsigned)(uintptr_t)kp ^ 16384u); vp = (LAS const char*)((unsigned)(uintptr_t)vp ^ 16384u); } while (0)
#define A_FAST(FIRST) do { \
    if (!(FIRST)) L_SUM(); \
    L_SOFTMAX(false, FIRST, dma16o<0>(mK, voff, sK, oK), dma16n<1024>(voff, sK, oK), dma16n<2048>(voff, sK, oK), dma16n<3072>(voff, sK, oK), \
              LV(dma16o<0>(mV, voff, sV, oV)), LV(dma16n<1024>(voff, sV, oV)), LV(dma16n<2048>(voff, sV, oV)), LV(dma16n<3072>(voff, sV, oV))); \
    oK += 32768u; TS2X(h); A_TAIL(); } while (0)
#define A_SLOW(s) do { \
    L_SUM(); l_pre(s); \
    oK = (s) < 15 ? (unsigned)TILE((s) + 2) * 16384u + (unsigned)wq * 4096u : (unsigned)(WS_KVP - WS_QF) + (unsigned)(h * 32768 + w * 4096); \
    oV = (unsigned)TILE((s) + 1) * 16384u + (unsigned)wq * 4096u; \
    const srd_t sKx = (s) < 15 ? sK : sW; \
    L_SOFTMAX((s) == nX - 1, (s) == nX, dma16o<0>(mK, voff, sKx, oK), dma16n<1024>(voff, sKx, oK), dma16n<2048>(voff, sKx, oK), dma16n<3072>(voff, sKx, oK), \
              LV(dma16o<0>(mV, voff, sV, oV)), LV(dma16n<1024>(voff, sV, oV)), LV(dma16n<2048>(voff, sV, oV)), LV(dma16n<3072>(voff, sV, oV))); \
    oK += 32768u; TS2X(h); \
    l_post(s); \
    if ((s) == nX - 1) { _Pragma("unroll") for (int ks = 0; ks < 8; ++ks) q[ks] = ldsfrag(L + 131072 + wq * 8192 + ks * 1024 + lane * 16); negm = (f32x16){}; } \
    A_TAIL(); } while (0)
  a_mphase<true, false>(kbase, vbase, q, p, S, O, negm, l, pm0, 0u, voff, sV, 0u STP_ARG);
  A_FAST(true);
  if (w == 0 && lane == 0) __hip_atomic_store((u64*)flg + 8 + r, tag, __ATOMIC_RELAXED, __HIP_MEMORY_SCOPE_AGENT);
  A_FAST(false);
  {
    const unsigned mq = rfl(lds0 + 131072u + (unsigned)w * 4096u), oq = rfl((unsigned)(b * 32768 + w * 4096));
    dma16o<0>(mq, voff, sW, oq); dma16n<1024>(voff, sW, oq); dma16n<2048>(voff, sW, oq); dma16n<3072>(voff, sW, oq);
    const unsigned go = rfl((unsigned)(WS_GW - WS_QF) + (unsigned)(b * 1024 + wq * 128));
    asm volatile("buffer_load_dword %0, %2, %3, %4 offen\n\tbuffer_load_dword %1, %2, %3, %4 offen offset:512" : "+v"(gate[0]), "+v"(gate[1]) : "v"((unsigned)r32 * 4u), "s"(sW), "s"(go) : "memory");
    poll_issue(fl2, flg + 8, lane);
    const srd_t sWa = mksrd(wo_a, 4096u * 1024u), sWb = mksrd(wo_b, 512u * 1024u);
    u32x4 dmy; unsigned dm1;
    asm volatile("buffer_load_dwordx4 %0, %2, %3, 0 offen\n\tbuffer_load_dwordx4 %0, %2, %3, 0 offen offset:1024\n\t"
                 "buffer_load_dwordx4 %0, %4, %5, 0 offen\n\tbuffer_load_dword %1, %6, %3, 0 offen"
                 : "=&v"(dmy), "=&v"(dm1) : "v"(voff + (unsigned)(b * 16384 + w * 2048)), "s"(sWa), "v"((unsigned)(b * 2048 + w * 256) + (voff & 255u)), "s"(sWb),
                   "v"((unsigned)((((b >> 3) * 8 + w) * 4 + (lane & 3)) * 4096 + (lane >> 2) * 0)) : "memory");
    warm_dummy = dmy; warm_dm1 = dm1;
  }
  A_FAST(false);
  asm volatile("" : "+v"(gate[0]), "+v"(gate[1]), "+v"(fl2));
  asm volatile("" :: "v"(warm_dummy), "v"(warm_dm1));
  if (w == 0) { poll_check(fl2, flg + 8, tag, lane); if (r == 0 && lane == 0) __hip_atomic_store((u64*)flg + 16, tag, __ATOMIC_RELAXED, __HIP_MEMORY_SCOPE_AGENT); }
  {
    int s = 3;
#pragma unroll 1
    for (int blk = 0; blk < 2; ++blk) {
      const int e1 = blk ? 15 : nX - 2, e2 = blk ? 16 : (nX + 2 < 16 ? nX + 2 : 16);
#pragma unroll 1
      for (; s < e1; ++s) A_FAST(false);
#pragma unroll 1
      for (; s < e2; ++s) A_SLOW(s);
    }
  }
  TS(t4);
  {
    L_SUM(); l_pre(16);
    const unsigned mp = rfl(lds0 + (g ? 98304u : 32768u) + (unsigned)wq * 2048u), op = rfl((unsigned)(WS_PQ - WS_QF) + (unsigned)(b * 16384 + w * 2048));
    L_SOFTMAX(true, nX == 16, dma16o<0>(mp, voff, sW, op), dma16n<1024>(voff, sW, op), (void)0, (void)0, (void)0, (void)0, (void)0, (void)0);
    if (16 == nX || 16 == nX + 1) wait_vm<0>();
    l_post(16);
    asm volatile("" : "+v"(p[0]), "+v"(p[1]), "+v"(l), "+v"(S[1]));
    SBAR(); wait_bar<2>(); SBAR();
  }
  a_mphase<false, true>(kbase, vbase, q, p, S, O, negm, l, pm0, 0u, voff, sV, 0u STP_ARG);
  NOP16(); SBAR();
  if (g == 0 && nX == 16) {
    ST_LOAD(w + 4); wait_vm<0>(); asm volatile("" : "+v"(ps));
    a_finish(keep0, keep1, mk, lk, xr, ps[0], ps[1], gate[0], sW, voff, CF_SO(X, 0));
    LWAIT();
  }
  LAS f32x2* stl = (LAS f32x2*)(L + 81920) + r32;
  auto stage2 = [&]() {
    f32x16 kv = {};
#pragma unroll
    for (int k = 0; k < 8; ++k)
#pragma unroll
      for (int qd = 0; qd < 4; ++qd) { const f32x4 v = *(LAS const f32x4*)(L + (k < 4 ? 49152 : 114688) + (k & 3) * 4096 + qd * 1024 + lane * 16);
#pragma unroll
        for (int e = 0; e < 4; ++e) kv[4 * qd + e] += v[e]; }
    const bf16x8 p0 = ldsfrag(kbase + wq * 2048), p1 = ldsfrag(kbase + wq * 2048 + 1024);
    f32x16 o = MFMA(asbf(pack8<0>(kv)), p0, (f32x16){});
    o = MFMA(asbf(pack8<1>(kv)), p1, o);
    const float sc = (1.0f - (g ? gate[1] : gate[0])) / (swap32_lo(o[8]) + 1e-6f);
    o = o * sc;
    st128_wt(pack8<0>(o), sW, voff, (unsigned)(WS_CF - WS_QF) + (unsigned)(((4 * (g ? Y : X) + wq) * 288 + 256 + h) * 1024));
  };
  const float lt = swap32_add(l);
  if (g == 0) {
    a_send(O[2], O[3], xr);
    stl[w * 32] = (f32x2){m, lt};
    wait_bar<0>();
    stage2();
    LBAR();
    const f32x2 po = stl[(w + 4) * 32];
    a_finish(O[0], O[1], m, lt, xr, po[0], po[1], gate[1], sW, voff, CF_SO(Y, 0));
  } else {
    const f32x2 po = stl[(w - 4) * 32];
    a_finish(O[2], O[3], m, lt, xr, po[0], po[1], gate[1], sW, voff, CF_SO(Y, 1));
    LWAIT();
    a_send(O[0], O[1], xr);
    stl[w * 32] = (f32x2){m, lt};
    wait_bar<4>();
    stage2();
  }
  o_phase(wo_a, wo_b, ws, out, L, lds0, lane, w, h, r, tag2, sW);
#undef TILE
#undef CF_SO
#undef ST_SO
#undef ST_LOAD
#undef L_SOFTMAX
#undef SM_GRP
#undef L_SUM
#undef LV
#undef A_TAIL
#undef A_FAST
#undef A_SLOW
}

extern "C" void kernel_launch(void* const* d_in, const int* in_sizes, int n_in, void* d_out, int out_size, void* d_ws, size_t ws_size, hipStream_t stream) {
  const u16* x = (const u16*)d_in[0]; const u16* ckv = (const u16*)d_in[1]; const u16* wq = (const u16*)d_in[2];
  const u16* wuk = (const u16*)d_in[3]; const u16* wuv = (const u16*)d_in[4]; const u16* wo_a = (const u16*)d_in[5];
  const u16* wqkv = (const u16*)d_in[7]; const u16* wphi = (const u16*)d_in[8]; const u16* wo_b = (const u16*)d_in[9]; const u16* gatew = (const u16*)d_in[10];
  (void)hipFuncSetAttribute((const void*)kF, hipFuncAttributeMaxDynamicSharedMemorySize, 163840);
  kF<<<256, 512, 163840, stream>>>(x, ckv, wq, wuk, wuv, wqkv, wphi, gatew, wo_a, wo_b, (char*)d_ws, (u16*)d_out);
}
```

```cpp
#include <hip/hip_runtime.h>
#include <stdint.h>

typedef unsigned short u16;
typedef unsigned long long u64;
typedef __bf16 bf16x8 __attribute__((ext_vector_type(8)));
typedef __bf16 bf16x2 __attribute__((ext_vector_type(2)));
typedef float f32x2 __attribute__((ext_vector_type(2)));
typedef float f32x4 __attribute__((ext_vector_type(4)));
typedef float f32x16 __attribute__((ext_vector_type(16)));
typedef unsigned u32x2 __attribute__((ext_vector_type(2)));
typedef unsigned u32x4 __attribute__((ext_vector_type(4)));
typedef short s16x4 __attribute__((ext_vector_type(4)));
typedef short s16x8 __attribute__((ext_vector_type(8)));
typedef __amdgpu_buffer_rsrc_t srd_t;
#define LAS __attribute__((address_space(3)))
#define DI __device__ __forceinline__
#define SBAR() __builtin_amdgcn_sched_barrier(0)
#define MFMA(a, b, c) __builtin_amdgcn_mfma_f32_32x32x16_bf16((a), (b), (c), 0, 0, 0)

constexpr size_t MB = 1024 * 1024;
constexpr size_t WS_KF = 0;
constexpr size_t WS_VF = 16 * MB;
constexpr size_t WS_QF = 32 * MB;
constexpr size_t WS_CF = 48 * MB;
constexpr size_t WS_PQ = 68 * MB;
constexpr size_t WS_KVP = 72 * MB;
constexpr size_t WS_GW = 73 * MB;
constexpr size_t WS_ST = 74 * MB;
constexpr size_t WS_FLG = 93 * MB;
constexpr size_t WS_DBG = 100 * MB;

constexpr float QSCALE = 0.08838834764831845f * 1.4426950408889634f;
constexpr float THR = 8.0f;

DI unsigned cvtpk(float lo, float hi) { f32x2 v = {lo, hi}; bf16x2 b = __builtin_convertvector(v, bf16x2); return __builtin_bit_cast(unsigned, b); }
template <int S> DI u32x4 pack8(const f32x16& x) {
  return (u32x4){cvtpk(x[8 * S + 0], x[8 * S + 1]), cvtpk(x[8 * S + 2], x[8 * S + 3]), cvtpk(x[8 * S + 4], x[8 * S + 5]), cvtpk(x[8 * S + 6], x[8 * S + 7])};
}
DI bf16x8 asbf(u32x4 v) { return __builtin_bit_cast(bf16x8, v); }
DI float swap32_max(float v) { auto rr = __builtin_amdgcn_permlane32_swap(__float_as_uint(v), __float_as_uint(v), false, false); return fmaxf(__uint_as_float(rr[0]), __uint_as_float(rr[1])); }
DI float swap32_add(float v) { auto rr = __builtin_amdgcn_permlane32_swap(__float_as_uint(v), __float_as_uint(v), false, false); return __uint_as_float(rr[0]) + __uint_as_float(rr[1]); }
DI float swap32_lo(float v) { auto rr = __builtin_amdgcn_permlane32_swap(__float_as_uint(v), __float_as_uint(v), false, false); return __uint_as_float(rr[0]); }
DI srd_t mksrd(const void* p, unsigned bytes) { return __builtin_amdgcn_make_buffer_rsrc((void*)p, (short)0, (int)bytes, 0x00020000); }
DI unsigned rfl(unsigned x) { return (unsigned)__builtin_amdgcn_readfirstlane((int)x); }
template <int OFF> DI void dma16o(unsigned m0v, unsigned voff, srd_t srd, unsigned soff) {
  asm volatile("s_mov_b32 m0, %0\n\ts_nop 0\n\tbuffer_load_dwordx4 %1, %2, %3 offen offset:%4 sc1 lds" :: "s"(m0v), "v"(voff), "s"(srd), "s"(soff), "i"(OFF) : "memory");
}
template <unsigned MOFF, unsigned SOFF> DI void dma16s(unsigned mb, unsigned voff, srd_t srd, unsigned sb) {
  unsigned t;
  asm volatile("s_add_u32 m0, %1, %5\n\ts_add_u32 %0, %4, %6\n\tbuffer_load_dwordx4 %2, %3, %0 offen sc1 lds" : "=&s"(t) : "s"(mb), "v"(voff), "s"(srd), "s"(sb), "i"(MOFF), "i"(SOFF) : "memory", "scc");
}
template <int OFF> DI void dma16n(unsigned voff, srd_t srd, unsigned soff) {
  asm volatile("buffer_load_dwordx4 %0, %1, %2 offen offset:%3 sc1 lds" :: "v"(voff), "s"(srd), "s"(soff), "i"(OFF) : "memory");
}
template <unsigned MOFF, unsigned SOFF> DI void dma16c(unsigned mb, unsigned voff, srd_t srd, unsigned sb) {
  unsigned t;
  asm volatile("s_add_u32 m0, %1, %5\n\ts_add_u32 %0, %4, %6\n\tbuffer_load_dwordx4 %2, %3, %0 offen lds" : "=&s"(t) : "s"(mb), "v"(voff), "s"(srd), "s"(sb), "i"(MOFF), "i"(SOFF) : "memory", "scc");
}
template <unsigned MOFF, unsigned SOFF> DI void dma16m(unsigned mb, unsigned voff, srd_t srd, unsigned sb) {
  unsigned t;
  asm volatile("s_add_u32 m0, %1, %5\n\ts_add_u32 %0, %4, %6\n\ts_mov_b32 exec_hi, 0x33333333\n\tbuffer_load_dwordx4 %2, %3, %0 offen lds\n\ts_mov_b32 exec_hi, -1"
               : "=&s"(t) : "s"(mb), "v"(voff), "s"(srd), "s"(sb), "i"(MOFF), "i"(SOFF) : "memory", "scc");
}
template <int N> DI void wait_bar() { asm volatile("s_waitcnt vmcnt(%0) lgkmcnt(0)\n\ts_barrier" :: "i"(N) : "memory"); }
template <int N> DI void wait_vm() { asm volatile("s_waitcnt vmcnt(%0)" :: "i"(N) : "memory"); }
#define SPIN_OVER(t0) (__builtin_amdgcn_s_memrealtime() - (t0) > 20000000ull)
#define LBAR() asm volatile("s_waitcnt lgkmcnt(0)\n\ts_barrier" ::: "memory")
#define LWAIT() asm volatile("s_waitcnt lgkmcnt(0)" ::: "memory")
#define NOP16() asm volatile("s_nop 7\n\ts_nop 7" ::: "memory")
DI bf16x8 ldsfrag(LAS const char* p) { return *(LAS const bf16x8*)p; }
DI bf16x8 trfrag(LAS const char* p0, LAS const char* p1) {
  s16x4 a = __builtin_amdgcn_ds_read_tr16_b64_v4i16((LAS s16x4*)p0);
  s16x4 b = __builtin_amdgcn_ds_read_tr16_b64_v4i16((LAS s16x4*)p1);
  return __builtin_bit_cast(bf16x8, __builtin_shufflevector(a, b, 0, 1, 2, 3, 4, 5, 6, 7));
}
DI void st128(u32x4 v, srd_t s, unsigned vo, unsigned so) { __builtin_amdgcn_raw_buffer_store_b128(v, s, vo + so, 0, 0); }
DI void st128_wt(u32x4 v, srd_t s, unsigned vo, unsigned so) { __builtin_amdgcn_raw_buffer_store_b128(v, s, vo + so, 0, 16); }

struct OCtx {
  srd_t sC, sLo, sHi; unsigned voff, voffW, voffV, soA, soW[3], mW, bB[2][3];
  LAS char* L; bf16x8 a[36], f[6];
};
constexpr int O_NS = 5;
template <int S, int I> DI void o_wpiece(const OCtx& c) {
  if constexpr (S < 12) {
    constexpr unsigned sl = (unsigned)(S % O_NS) * 24576u + I * 8192u, row = S < 6 ? 192u * S + 64u * I : 384u * ((S - 6) % 3) + 128u * I;
    constexpr int t = S < 6 ? 0 : (S < 9 ? 1 : 2);
    dma16c<sl, row * 1024u>(c.mW, S < 6 ? c.voffW : c.voffV, row < 640u ? c.sLo : c.sHi, c.soW[t]);
  }
}
template <int i> DI void o_aload(OCtx& c) {
  if constexpr (i >= 0 && i < 36) {
    unsigned t;
    asm volatile("s_add_u32 %1, %4, %5\n\tbuffer_load_dwordx4 %0, %2, %3, %1 offen sc1" : "=v"(c.a[i]), "=&s"(t) : "v"(c.voff), "s"(c.sC), "s"(c.soA), "i"(i * 2048u) : "memory", "scc");
  }
}
template <int S, int k> DI void o_ride(OCtx& c) {
  if constexpr (S == 0) o_aload<12 + k>(c);
  else if constexpr (S == 1) { if constexpr (k < 3) o_wpiece<5, k>(c); else if constexpr (k < 9) { o_aload<24 + 2 * (k - 3)>(c); o_aload<25 + 2 * (k - 3)>(c); } }
  else { if constexpr (k == 1) o_wpiece<S + 4, 0>(c); if constexpr (k == 5) o_wpiece<S + 4, 1>(c); if constexpr (k == 9) o_wpiece<S + 4, 2>(c); }
}
template <int S> DI void o_wstage(OCtx& c) { o_wpiece<S, 0>(c); o_wpiece<S, 1>(c); o_wpiece<S, 2>(c); }
#define O_SP(S) (c.L + c.bB[(S) < 6 ? 0 : 1][((S) % O_NS) >> 1] + (((S) % O_NS) & 1) * 24576)
#define O_FR(S, p, k) ((S) < 6 ? trfrag((p) + ((k) >> 1) * 4096 + ((k) & 1) * 512, (p) + ((k) >> 1) * 4096 + ((k) & 1) * 512 + 1024) : trfrag((p) + (k) * 2048, (p) + (k) * 2048 + 512))
template <int S, int NY> DI void o_stage(OCtx& c, f32x16 (&acc)[2]) {
  constexpr int i0 = S < 6 ? 6 * S : 12 * ((S - 6) % 3);
  if constexpr (NY >= 0) {
    if constexpr (S < 6) asm volatile("s_waitcnt vmcnt(%6)\n\ts_barrier" : "+v"(c.a[i0]), "+v"(c.a[i0 + 1]), "+v"(c.a[i0 + 2]), "+v"(c.a[i0 + 3]), "+v"(c.a[i0 + 4]), "+v"(c.a[i0 + 5]) : "i"(NY) : "memory");
    else                 asm volatile("s_waitcnt vmcnt(%0)\n\ts_barrier" :: "i"(NY) : "memory");
  }
  SBAR();
  LAS const char* sp = O_SP(S); LAS const char* sn = O_SP(S + 1);
  bf16x8 f[12];
  if constexpr (S == 0) { c.f[0] = O_FR(S, sp, 0); c.f[1] = O_FR(S, sp, 1); c.f[2] = O_FR(S, sp, 2); c.f[3] = O_FR(S, sp, 3); c.f[4] = O_FR(S, sp, 4); c.f[5] = O_FR(S, sp, 5); SBAR(); }
  f[0] = c.f[0]; f[1] = c.f[1]; f[2] = c.f[2]; f[3] = c.f[3]; f[4] = c.f[4]; f[5] = c.f[5];
#define O_LD(k) f[k] = O_FR(S, sp, k)
#define O_LN(k) do { if constexpr (S < 11) c.f[k] = O_FR(S + 1, sn, k); } while (0)
#define O_MM(k) do { if constexpr (S < 6) acc[(k) & 1] = MFMA(f[k], c.a[i0 + ((k) >> 1)], acc[(k) & 1]); else acc[0] = MFMA(f[k], c.a[i0 + (k)], acc[0]); o_ride<S, k>(c); SBAR(); } while (0)
  O_LD(6); O_MM(0); O_LD(7); O_MM(1); O_LD(8); O_MM(2); O_LD(9); O_MM(3); O_LD(10); O_MM(4); O_LD(11); O_MM(5);
  O_LN(0); O_MM(6); O_LN(1); O_MM(7); O_LN(2); O_MM(8); O_LN(3); O_MM(9); O_LN(4); O_MM(10); O_LN(5); O_MM(11);
#undef O_LD
#undef O_LN
#undef O_MM
}
DI void o_park(LAS char* p, const f32x16& v) {
#pragma unroll
  for (int qd = 0; qd < 4; ++qd) *(LAS f32x4*)(p + qd * 1024) = (f32x4){v[4 * qd], v[4 * qd + 1], v[4 * qd + 2], v[4 * qd + 3]};
}
DI void o_addlds(f32x16& v, LAS const char* p) {
#pragma unroll
  for (int qd = 0; qd < 4; ++qd) { const f32x4 u = *(LAS const f32x4*)(p + qd * 1024);
#pragma unroll
    for (int e = 0; e < 4; ++e) v[4 * qd + e] += u[e]; }
}
DI void o_publish(srd_t sW, unsigned voff, const f32x16& v, unsigned so) {
#pragma unroll
  for (int qd = 0; qd < 4; ++qd) st128_wt(__builtin_bit_cast(u32x4, (f32x4){v[4 * qd], v[4 * qd + 1], v[4 * qd + 2], v[4 * qd + 3]}), sW, voff + (unsigned)(qd * 1024), so);
}
DI void o_phase(const u16* wo_a, const u16* wo_b, char* ws, u16* out, LAS char* L, unsigned lds0, int lane, int w, int h, int r, u64 tag, srd_t sW) {
  const int j = h >> 3, mi = (h & 4) ? 15 - r : r, ni = h & 3, b = blockIdx.x;
  const int g = w >> 2, rb = w & 3, hh = lane >> 5, i16 = lane & 15, p4 = i16 & 3, pj = j & 2;
  u64* fl = (u64*)(ws + WS_FLG + 65536 + (size_t)r * 1024);
  OCtx c;
  c.L = L;
  c.sC = mksrd(ws + WS_CF, 64u * 288u * 1024u); c.sLo = mksrd(wo_a, 4096u * 1024u);
  c.sHi = mksrd(j == 3 ? (const char*)wo_b - 4194304 : (const char*)wo_a, (j == 3 ? 4608u : 4096u) * 1024u);
    c.voff = (unsigned)lane * 16u;
  c.voffW = (unsigned)(((lane >> 2) & 7) * 1024 + (lane >> 5) * 64 + (lane & 3) * 16);
  c.voffV = (unsigned)((lane >> 2) * 1024 + (lane & 3) * 16);
  c.soA = rfl((unsigned)((4 * mi + rb) * 288 + 72 * j + g) * 1024u);
  c.soW[0] = rfl((unsigned)(1152 * j + 8 * w) * 1024u + (unsigned)(ni * 256 + (pj ^ 2) * 64));
  c.soW[1] = rfl((unsigned)(1152 * j + 16 * w) * 1024u + (unsigned)(ni * 256 + (j ^ 1) * 64));
  c.soW[2] = rfl((unsigned)(1152 * j + 16 * w) * 1024u + (unsigned)(ni * 256 + j * 64));
  c.mW = rfl(lds0 + (unsigned)w * 1024u);
#pragma unroll
  for (int k = 0; k < 3; ++k) {
    c.bB[1][k] = (unsigned)(k * 49152 + g * 1024 + (4 * hh + (i16 >> 2)) * 64 + ((lane >> 4) & 1) * 32 + (((p4 & 1) << 1) | (p4 >> 1)) * 8);
    c.bB[0][k] = c.bB[1][k] + (unsigned)(g * 1024);
    asm volatile("" : "+v"(c.bB[0][k]), "+v"(c.bB[1][k]));
  }
  LAS char* red = L + 122880 + w * 4096 + c.voff; LAS const char* rrd = L + 122880 + (w ^ 4) * 4096 + c.voff;
  wait_bar<0>();
  if (w == 0 && lane == 0) __hip_atomic_store(fl + h, tag, __ATOMIC_RELAXED, __HIP_MEMORY_SCOPE_AGENT);
  o_wstage<0>(c); o_wstage<1>(c); o_wstage<2>(c); o_wstage<3>(c); o_wstage<4>(c);
  if (w == 0) {
    const bool need = j == 3 || (lane >= 9 * j && lane < 9 * j + 9);
    const u64 t0 = __builtin_amdgcn_s_memrealtime();
    while (!__all(!need || __hip_atomic_load(fl + (lane & 31), __ATOMIC_RELAXED, __HIP_MEMORY_SCOPE_AGENT) == tag)) { __builtin_amdgcn_s_sleep(1); if (SPIN_OVER(t0)) break; }
    if (h == 31 && lane == 0) __hip_atomic_store(fl + 96, tag, __ATOMIC_RELAXED, __HIP_MEMORY_SCOPE_AGENT);
  }
  asm volatile("s_barrier" ::: "memory");
  o_aload<0>(c); o_aload<1>(c); o_aload<2>(c); o_aload<3>(c); o_aload<4>(c); o_aload<5>(c); o_aload<6>(c); o_aload<7>(c); o_aload<8>(c);
  o_aload<9>(c); o_aload<10>(c); o_aload<11>(c);
  f32x16 acc[2] = {{}, {}};
  o_stage<0, 6>(c, acc); o_stage<1, 12>(c, acc); o_stage<2, 21>(c, acc); o_stage<3, 18>(c, acc); o_stage<4, 12>(c, acc); o_stage<5, 6>(c, acc);
  o_park(red, g ? acc[0] : acc[1]);
  f32x16 prev = g ? acc[1] : acc[0];
  acc[0] = (f32x16){};
  wait_bar<6>(); SBAR();
  o_addlds(prev, rrd); o_publish(sW, c.voff, prev, (unsigned)(b * 32768 + g * 16384 + rb * 4096)); SBAR();
  o_stage<6, -1>(c, acc);
  o_stage<7, 10>(c, acc);
  o_stage<8, 6>(c, acc);
  if (w == 0 && lane == 0) __hip_atomic_store(fl + 32 + h, tag, __ATOMIC_RELAXED, __HIP_MEMORY_SCOPE_AGENT);
  if (g == 1) o_park(red, acc[0]);
  prev = acc[0]; acc[0] = (f32x16){};
  wait_bar<3>(); SBAR();
  if (g == 0) { o_addlds(prev, rrd); o_publish(sW, c.voff, prev, (unsigned)(WS_PQ - WS_QF) + (unsigned)(b * 16384 + rb * 4096)); }
  SBAR();
  o_stage<9, -1>(c, acc);
  o_stage<10, 0>(c, acc);
  if (w == 0 && lane == 0) __hip_atomic_store(fl + 64 + h, tag, __ATOMIC_RELAXED, __HIP_MEMORY_SCOPE_AGENT);
  o_stage<11, -1>(c, acc);
    if (g == 1) o_park(red, acc[0]);
  if (w == 0) {
    const unsigned l3 = c.voff >> 4; const u64* pf = fl + ((l3 & 3) == 1 ? 64 + (h ^ 8) : 32 + (h ^ ((l3 & 1) ? 24 : 16)));
    const u64 t0 = __builtin_amdgcn_s_memrealtime();
    while (!__all(__hip_atomic_load(pf, __ATOMIC_RELAXED, __HIP_MEMORY_SCOPE_AGENT) == tag)) { __builtin_amdgcn_s_sleep(1); if (SPIN_OVER(t0)) break; }
  }
  LBAR();
  if (g == 1) return;
    prev = acc[0];
#pragma unroll
  for (int k = 1; k < 4; ++k) {
    const int bp = b ^ (2 * k);
    const unsigned so = k == 1 ? (unsigned)(WS_PQ - WS_QF) + (unsigned)(bp * 16384 + rb * 4096) : (unsigned)(bp * 32768 + (j & 1) * 16384 + rb * 4096);
#pragma unroll
    for (int qd = 0; qd < 4; ++qd) {
      const f32x4 v = __builtin_bit_cast(f32x4, __builtin_amdgcn_raw_buffer_load_b128(sW, c.voff + (unsigned)(qd * 1024), so, 16));
#pragma unroll
      for (int e = 0; e < 4; ++e) prev[4 * qd + e] += v[e];
    }
  }
  o_addlds(prev, rrd);
  unsigned vo = c.voff; asm volatile("" : "+v"(vo));
  u16* op = out + (size_t)(128 * mi + 32 * rb + ((vo >> 4) & 31)) * 512 + 128 * ni + 32 * j + 8 * (vo >> 9);
  *(u32x4*)(op) = pack8<0>(prev);
  *(u32x4*)(op + 16) = pack8<1>(prev);
}
#undef O_SP
#undef O_FR

#define KVAUX 16
#define WT1 1
#define WT2 2
struct PCtx {
  srd_t sA1, sA2, sUK, sUV, sWQ, sWB, sWP, sK, sV, sO;
  unsigned voffA[2], voffB, voffS, voff;
  unsigned soA1, soA2, soB, soS, soKa, mK0, mA, mB2;
  unsigned gv[8];
  u32x2 fl; u64 tag; const u64* flg;
  u32x4 wp;
  unsigned dmy, soPW, soPS, soPX;
  u32x4 kvp[16]; unsigned kvo;
  int w, lane, h;
  LAS char* L;
};
template <int T, int I> DI void p_pieceA(const PCtx& c) {
  if constexpr (T < 16) {
    constexpr unsigned st = (unsigned)(T % 3) * 32768u, kt = (unsigned)(T & 7);
    if constexpr (T < 8) dma16c<st + I * 1024u, I * 8192u + kt * 128u>(c.mA, c.voffA[I & 1], c.sA1, c.soA1);
    else                 dma16c<st + I * 1024u, I * 8192u + kt * 128u>(c.mA, c.voffA[I & 1], c.sA2, c.soA2);
  }
}
template <int T, int I> DI void p_pieceB(const PCtx& c) {
  constexpr unsigned st = 98304u + (unsigned)(T & 1) * 32768u, kt = (unsigned)(T & 7);
  if constexpr (T < 8) {
    dma16c<st + I * 1024u, kt * 524288u + (I & 1) * 128u>(c.mA, c.voffB, I < 2 ? c.sUK : c.sUV, c.soB);
  } else if constexpr (T < 16) {
    if constexpr (I < 2) dma16c<st + I * 1024u, kt * 524288u + I * 128u>(c.mB2, c.voffB, c.sWQ, c.soB);
    else if constexpr (I == 2) dma16m<st + 2048u, kt * 196608u>(c.mB2, c.voffS, c.sWB, c.soS);
    else if (c.w == 7) *(LAS u16*)(c.L + st + ((c.lane >> 3) * 6 + 5) * 512 + (c.lane & 7) * 64 + 32) = (u16)c.gv[kt];
  }
}
template <int I, bool ISV> DI void p_kvstore(const PCtx& c) {
  if constexpr (I >= 0 && I < 16) {
    constexpr int mt = I >> 2, nt = (I >> 1) & 1, sh = I & 1;
    constexpr unsigned off = (unsigned)((mt >> 1) * 16384 + (ISV ? nt * 4 + 2 * (mt & 1) + sh : (mt & 1) * 8 + 2 * nt + sh) * 1024);
    __builtin_amdgcn_raw_buffer_store_b128(c.kvp[I], c.sO, c.kvo + off, 0, 16);
  }
}
#define WARM1(VO, SRD, SO, OFF) asm volatile("buffer_load_dword %0, %1, %2, %3 offen offset:%4" : "+v"(c.dmy) : "v"(VO), "s"(SRD), "s"(SO), "i"(OFF) : "memory")
template <int I> DI void p_warm(PCtx& c) {
  if constexpr (I == 0) { WARM1(c.voff, c.sA1, c.soPX, 0); WARM1(c.voffB, c.sUK, c.soPW, 0); WARM1(c.voffB, c.sUK, c.soPW, 128); WARM1(c.voffB, c.sUV, c.soPW, 0); WARM1(c.voffB, c.sUV, c.soPW, 128); }
  if constexpr (I == 1) { WARM1(c.voff, c.sA2, c.soPX, 0); WARM1(c.voffB, c.sWQ, c.soPW, 0); }
  if constexpr (I == 2) { WARM1(c.voffB, c.sWQ, c.soPW, 128); WARM1(c.voffS, c.sWB, c.soPS, 0); }
}
#undef WARM1
DI void poll_spin(const u64* f, u64 tag, int lane) {
  const u64 t0 = __builtin_amdgcn_s_memrealtime();
  while (!__all(__hip_atomic_load(f + (lane & 7), __ATOMIC_RELAXED, __HIP_MEMORY_SCOPE_AGENT) == tag)) { __builtin_amdgcn_s_sleep(2); if (SPIN_OVER(t0)) break; }
}
DI void poll_issue(u32x2& fl, const u64* f, int lane) { asm volatile("global_load_dwordx2 %0, %1, off sc1" : "+v"(fl) : "v"(f + (lane & 7)) : "memory"); }
DI void poll_check(u32x2& fl, const u64* f, u64 tag, int lane) {
  asm volatile("" : "+v"(fl));
  if (!__all(fl[0] == (unsigned)tag && fl[1] == (unsigned)(tag >> 32))) poll_spin(f, tag, lane);
  asm volatile("" ::: "memory");
}
template <int B> DI void p_hook(PCtx& c) {
  if constexpr (B == 9) { if (c.w == 0) dma16c<155648u, 0u>(c.mA, c.voff, c.sWP, (unsigned)c.h * 512u); }
  if constexpr (B == 13) {
    const int r32 = c.lane & 31, hh = c.lane >> 5; LAS const u16* wb = (LAS const u16*)(c.L + 155648) + (r32 & 15); const unsigned sg = r32 >= 16 ? 0x80008000u : 0u;
#pragma unroll
    for (int jj = 0; jj < 4; ++jj) { const int d0 = 8 * (jj >> 1) + 4 * hh + 2 * (jj & 1); c.wp[jj] = ((unsigned)wb[d0 * 16] | ((unsigned)wb[(d0 + 1) * 16] << 16)) ^ sg; }
  }
  if constexpr (B == 15) { if (c.w == 0 && c.lane == 0) __hip_atomic_store((u64*)c.flg + (blockIdx.x >> 5), c.tag, __ATOMIC_RELAXED, __HIP_MEMORY_SCOPE_AGENT); }
  if constexpr (B == 16) { if (c.w == 0) poll_issue(c.fl, c.flg, c.lane); }
}

template <int PASS, int MT, int NT, int NSUB, bool TRANS, bool ISV>
struct PPass {
  PCtx& c; f32x16 (&acc)[MT][NT];
  unsigned aB[2][4], bB;
  bf16x8 fa[2][MT], fb[2][NT];
  template <int SET, int T, int KS> DI void load() {
    constexpr int bo = (T & 1) * 32768 + KS * (2 * NSUB * 512), sl = T % 3;
#pragma unroll
    for (int nt = 0; nt < NT; ++nt) fb[SET][nt] = trfrag(c.L + bB + bo + nt * 512, c.L + bB + bo + nt * 512 + 256);
#pragma unroll
    for (int mt = 0; mt < MT; ++mt) fa[SET][mt] = ldsfrag(c.L + aB[sl >> 1][KS] + (sl & 1) * 32768 + mt * 4096);
  }
  template <int SET, int I> DI void mma1() {
    if constexpr (I < MT * NT) {
      constexpr int mt = I / NT, nt = I % NT;
      acc[mt][nt] = TRANS ? MFMA(fb[SET][nt], fa[SET][mt], acc[mt][nt]) : MFMA(fa[SET][mt], fb[SET][nt], acc[mt][nt]);
    }
  }
  template <int SET, int T, int I0, int S0> DI void mmaA() {
    mma1<SET, 0>(); SBAR(); p_pieceA<T, I0>(c); SBAR(); mma1<SET, 1>(); mma1<SET, 6>(); SBAR(); p_kvstore<S0, ISV>(c); SBAR(); mma1<SET, 2>(); mma1<SET, 7>(); SBAR(); p_pieceA<T, I0 + 1>(c); SBAR();
    mma1<SET, 3>(); mma1<SET, 4>(); mma1<SET, 5>();
  }
  template <int t> DI void tile() {
    constexpr int T = PASS * 8 + t;
    constexpr int S0 = PASS == 1 && t < 6 ? 3 * t : 16;
    constexpr int WI = T == WT1 ? 1 : (T == WT2 ? 2 : 0);
    constexpr int NST = (PASS == 1 ? (t < 5 ? 3 : (t == 5 ? 1 : 0)) : 0) + (T == 0 ? 5 : 0) + (WI ? 2 : 0);
    SBAR(); load<1, T, 1>(); SBAR(); mmaA<0, T + 2, 0, S0>();
    SBAR(); load<0, T, 2>(); SBAR(); mmaA<1, T + 2, 2, S0 + 1>();
    SBAR(); load<1, T, 3>(); SBAR(); if constexpr (WI != 0) p_warm<WI>(c); SBAR(); mmaA<0, 16, 0, S0 + 2>();
    SBAR();
    if constexpr (T == 15) LBAR(); else if constexpr (T == 14) wait_bar<0>(); else wait_bar<4 + NST>();
    SBAR();
    if constexpr (t < 7) load<0, T + 1, 0>();
    SBAR();
    mma1<1, 0>(); p_pieceB<T + 2, 0>(c); SBAR();
    mma1<1, 1>(); mma1<1, 6>(); p_pieceB<T + 2, 1>(c); SBAR();
    mma1<1, 2>(); mma1<1, 7>(); p_pieceB<T + 2, 2>(c); SBAR();
    mma1<1, 3>(); p_pieceB<T + 2, 3>(c); SBAR();
    mma1<1, 4>(); mma1<1, 5>(); SBAR();
    p_hook<T + 1>(c); SBAR();
  }
  DI void run(unsigned arow0, unsigned nt0) {
    const int lane = c.lane, r = lane & 31, hh = lane >> 5, i16 = lane & 15;
#pragma unroll
    for (int ks = 0; ks < 4; ++ks) { aB[0][ks] = (arow0 + r) * 128u + (unsigned)(((2 * ks + hh) ^ ((r >> 1) & 7)) * 16); aB[1][ks] = aB[0][ks] + 65536u; asm volatile("" : "+v"(aB[1][ks])); }
    bB = 98304u + (unsigned)(hh * NSUB + nt0) * 512u + (unsigned)((i16 >> 2) * 64 + ((lane >> 4) & 1) * 32 + (i16 & 3) * 8); asm volatile("" : "+v"(bB));
#pragma unroll
    for (int mt = 0; mt < MT; ++mt)
#pragma unroll
      for (int nt = 0; nt < NT; ++nt) acc[mt][nt] = (f32x16){};
    load<0, PASS * 8, 0>();
    tile<0>(); tile<1>(); tile<2>(); tile<3>(); tile<4>(); tile<5>(); tile<6>(); tile<7>();
  }
};
template <int PASS, int MT, int NT, int NSUB, bool TRANS, bool ISV>
DI void p_pass(PCtx& c, f32x16 (&acc)[MT][NT], unsigned arow0, unsigned nt0) { PPass<PASS, MT, NT, NSUB, TRANS, ISV> p{c, acc}; p.run(arow0, nt0); }

DI void softmax2(f32x16& x) {
#pragma unroll
  for (int s = 0; s < 2; ++s) {
    float mx = fmaxf(fmaxf(fmaxf(x[8 * s], x[8 * s + 1]), fmaxf(x[8 * s + 2], x[8 * s + 3])), fmaxf(fmaxf(x[8 * s + 4], x[8 * s + 5]), fmaxf(x[8 * s + 6], x[8 * s + 7])));
    mx = swap32_max(mx);
    const float cc = -mx * 1.4426950408889634f;
    float sum = 0.f;
#pragma unroll
    for (int j = 0; j < 8; ++j) { x[8 * s + j] = __builtin_amdgcn_exp2f(__builtin_fmaf(x[8 * s + j], 1.4426950408889634f, cc)); sum += x[8 * s + j]; }
    const float inv = __builtin_amdgcn_rcpf(swap32_add(sum));
#pragma unroll
    for (int j = 0; j < 8; ++j) x[8 * s + j] *= inv;
  }
}

DI void mfma_acc(f32x16& c, bf16x8 a, bf16x8 b) { asm volatile("v_mfma_f32_32x32x16_bf16 %0, %1, %2, %0" : "+v"(c) : "v"(a), "v"(b)); }
DI void mfma_acc2(f32x16& c, bf16x8 a, bf16x8 b, bf16x8 nx) { asm volatile("v_mfma_f32_32x32x16_bf16 %0, %1, %2, %0" : "+v"(c) : "v"(a), "v"(b), "v"(nx)); }
DI void mfma_init2(f32x16& c, bf16x8 a, bf16x8 b, const f32x16& ci, bf16x8 nx) { asm volatile("v_mfma_f32_32x32x16_bf16 %0, %1, %2, %3" : "=&v"(c) : "v"(a), "v"(b), "v"(ci), "v"(nx)); }
DI float sum2(unsigned pk, float acc) { return __builtin_amdgcn_fdot2_f32_bf16(__builtin_bit_cast(bf16x2, pk), __builtin_bit_cast(bf16x2, 0x3f803f80u), acc, false); }
DI float sum8(bf16x8 p, float acc) { const u32x4 u = __builtin_bit_cast(u32x4, p); return sum2(u[3], sum2(u[2], sum2(u[1], sum2(u[0], acc)))); }
DI float max3f(float a, float b, float c) { float r; asm("v_max3_f32 %0, %1, %2, %3" : "=v"(r) : "v"(a), "v"(b), "v"(c)); return r; }
DI float max2f(float a, float b) { float r; asm("v_max_f32_e32 %0, %1, %2" : "=v"(r) : "v"(a), "v"(b)); return r; }
#define NMV 4
#define TRAIL 4
#define ADIST 6
template <bool DOQK, bool DOPV>
DI void a_mphase(LAS const char* kp, LAS const char* vp, const bf16x8 (&q)[8], bf16x8 (&p)[4], f32x16 (&S)[2], f32x16 (&O)[4], const f32x16& negm, float& l, float& pm0,
                 unsigned mV, unsigned voff, srd_t sV, unsigned oV) {
  constexpr int N = (DOQK ? 16 : 0) + (DOPV ? 16 : 0), D = ADIST;
  bf16x8 f[N];
  unsigned pw[8]; float ls = 0.f, e0 = 0.f;
#define A_ISQK(i) (DOQK && (!DOPV || (((i) >> 3) & 1) == 0))
#define A_IX(i) ((DOPV && DOQK) ? (((i) >> 4) * 8 + ((i) & 7)) : (i))
#define A_LD(i) do { if constexpr ((i) < N) { if constexpr (A_ISQK(i)) f[i] = ldsfrag(kp + A_IX(i) * 1024); \
                     else f[i] = ldsfrag(vp + ((A_IX(i) & 3) * 4 + (A_IX(i) >> 2)) * 1024); } } while (0)
#define A_MM(i) do { if constexpr (A_ISQK(i)) { if constexpr ((A_IX(i) & 7) == 0) mfma_init2(S[A_IX(i) >> 3], f[i], q[0], negm, f[(i) + 1]); \
                       else if constexpr ((i) & 1) mfma_acc(S[A_IX(i) >> 3], f[i], q[A_IX(i) & 7]); else mfma_acc2(S[A_IX(i) >> 3], f[i], q[A_IX(i) & 7], f[((i) + 1) % N]); } \
                     else if constexpr ((i) & 1) mfma_acc(O[A_IX(i) & 3], f[i], p[A_IX(i) >> 2]); else mfma_acc2(O[A_IX(i) & 3], f[i], p[A_IX(i) >> 2], f[((i) + 1) % N]); } while (0)
#define A_SM(e) do { const float x_ = __builtin_amdgcn_exp2f(S[1][(e) & 15]); ls += x_; \
                     if constexpr ((e) & 1) { pw[((e) & 15) >> 1] = cvtpk(e0, x_); asm volatile("" : "+v"(pw[((e) & 15) >> 1]), "+v"(ls)); } else { e0 = x_; asm volatile("" : "+v"(e0), "+v"(ls)); } \
                     if constexpr ((e) == 7) p[2] = asbf((u32x4){pw[0], pw[1], pw[2], pw[3]}); if constexpr ((e) == 15) p[3] = asbf((u32x4){pw[4], pw[5], pw[6], pw[7]}); } while (0)
#define A_MX(j) do { if constexpr ((j) == 0) pm0 = max2f(S[0][0], S[0][1]); else pm0 = max3f(pm0, S[0][2 * ((j) & 7)], S[0][2 * ((j) & 7) + 1]); asm volatile("" : "+v"(pm0)); } while (0)
#define A_STEP(i) do { if constexpr ((i) < N) { A_LD((i) + D); if constexpr ((i) == N - TRAIL) { SBAR(); LBAR(); SBAR(); } A_MM(i); \
                     if constexpr (DOQK && DOPV && (i) < 16) A_SM(i); \
                     if constexpr (DOQK && DOPV && (i) >= 16 && (i) < 24) A_MX((i) - 16); \
                     if constexpr (DOQK && DOPV && NMV >= 4 && (i) == 17) dma16o<0>(mV, voff, sV, oV); \
                     if constexpr (DOQK && DOPV && NMV >= 4 && (i) == 20) dma16n<1024>(voff, sV, oV); \
                     if constexpr (DOQK && DOPV && NMV >= 4 && (i) == 23) dma16n<2048>(voff, sV, oV); \
                     if constexpr (DOQK && DOPV && NMV >= 4 && (i) == 25) dma16n<3072>(voff, sV, oV); \
                     if constexpr (!DOQK && (i) < 8) { A_SM(2 * (i)); A_SM(2 * (i) + 1); } SBAR(); } } while (0)
  SBAR();
  __builtin_amdgcn_s_setprio(1);
  A_LD(0); A_LD(1); A_LD(2); A_LD(3); if constexpr (D > 4) { A_LD(4); A_LD(5); } SBAR();
  A_STEP(0); A_STEP(1); A_STEP(2); A_STEP(3); A_STEP(4); A_STEP(5); A_STEP(6); A_STEP(7);
  A_STEP(8); A_STEP(9); A_STEP(10); A_STEP(11); A_STEP(12); A_STEP(13); A_STEP(14); A_STEP(15);
  A_STEP(16); A_STEP(17); A_STEP(18); A_STEP(19); A_STEP(20); A_STEP(21); A_STEP(22); A_STEP(23);
  A_STEP(24); A_STEP(25); A_STEP(26); A_STEP(27); A_STEP(28); A_STEP(29); A_STEP(30); A_STEP(31);
  if constexpr (DOPV) l += ls;
  if constexpr (!DOPV) { NOP16(); SBAR(); A_MX(0); A_MX(1); A_MX(2); A_MX(3); A_MX(4); A_MX(5); A_MX(6); A_MX(7); }
  __builtin_amdgcn_s_setprio(0);
  #undef A_ISQK
#undef A_IX
#undef A_LD
#undef A_MM
#undef A_SM
#undef A_MX
#undef A_STEP
}

DI void a_finish(const f32x16& a0, const f32x16& a1, float ma, float la, LAS const char* xr, float mb, float lb, float gate, srd_t sC, unsigned voff, unsigned soff) {
  const float mt = fmaxf(ma, mb);
  const float fa = __builtin_amdgcn_exp2f(ma - mt), fb = __builtin_amdgcn_exp2f(mb - mt);
  const float sc = gate / (la * fa + lb * fb);
  const float wa = fa * sc, wb = fb * sc;
#pragma unroll
  for (int t = 0; t < 2; ++t) {
    const f32x16& a = t ? a1 : a0;
#pragma unroll
    for (int s = 0; s < 2; ++s) {
      const f32x4 b0 = *(LAS const f32x4*)(xr + t * 4096 + s * 2048), b1 = *(LAS const f32x4*)(xr + t * 4096 + s * 2048 + 1024);
      const u32x4 o = {cvtpk(a[8 * s + 0] * wa + b0[0] * wb, a[8 * s + 1] * wa + b0[1] * wb), cvtpk(a[8 * s + 2] * wa + b0[2] * wb, a[8 * s + 3] * wa + b0[3] * wb),
                       cvtpk(a[8 * s + 4] * wa + b1[0] * wb, a[8 * s + 5] * wa + b1[1] * wb), cvtpk(a[8 * s + 6] * wa + b1[2] * wb, a[8 * s + 7] * wa + b1[3] * wb)};
      st128_wt(o, sC, voff + (unsigned)((2 * t + s) * 1024), soff);
      SBAR();
    }
  }
}
DI void a_send(const f32x16& a0, const f32x16& a1, LAS char* xr) {
#pragma unroll
  for (int g = 0; g < 4; ++g) {
    *(LAS f32x4*)(xr + g * 1024) = (f32x4){a0[4 * g], a0[4 * g + 1], a0[4 * g + 2], a0[4 * g + 3]};
    *(LAS f32x4*)(xr + 4096 + g * 1024) = (f32x4){a1[4 * g], a1[4 * g + 1], a1[4 * g + 2], a1[4 * g + 3]};
  }
}

__global__ __launch_bounds__(512, 2) void kF(const u16* __restrict__ x, const u16* __restrict__ ckv, const u16* __restrict__ wq_, const u16* __restrict__ wuk,
                                             const u16* __restrict__ wuv, const u16* __restrict__ wqkv, const u16* __restrict__ wphi, const u16* __restrict__ gatew,
                                             const u16* __restrict__ wo_a, const u16* __restrict__ wo_b, char* __restrict__ ws, u16* __restrict__ out) {
  extern __shared__ __attribute__((aligned(16))) char lds[];
  LAS char* L = (LAS char*)lds; const unsigned lds0 = (unsigned)(uintptr_t)L;
  const int tid = threadIdx.x, lane = tid & 63, w = __builtin_amdgcn_readfirstlane(tid >> 6);
  const int b = blockIdx.x, h = (b & 7) * 4 + ((b >> 3) & 3), r = b >> 5;
  const int g = w >> 2, wq = w & 3, hh = lane >> 5, r32 = lane & 31;
  const int X = 15 - r, Y = r, nX = 16 - r;
  constexpr int NS = 17;
  const unsigned voff = (unsigned)lane * 16u;
    bf16x8 q[8];
  u64 tag, tag2; const u64* flg = (const u64*)(ws + WS_FLG + (size_t)h * 256);
  const srd_t sK = mksrd(ws + WS_KF + (size_t)h * 32 * 16384, 32u * 16384u), sV = mksrd(ws + WS_VF + (size_t)h * 32 * 16384, 32u * 16384u);
  const srd_t sW = mksrd(ws + WS_QF, (unsigned)(WS_DBG - WS_QF));
  const unsigned ringK = rfl(lds0 + (g ? 98304u : 32768u) + (unsigned)wq * 4096u), ringV = rfl(lds0 + (g ? 0u : 65536u) + (unsigned)wq * 4096u);
  {
    PCtx c;
    c.fl = (u32x2){0u, 0u}; c.wp = (u32x4){0u, 0u, 0u, 0u};
    c.L = L; c.w = w; c.lane = lane; c.h = h; c.voff = voff; c.flg = flg; c.sK = sK; c.sV = sV;
    c.soA1 = rfl((unsigned)(256 * r + 32 * w) * 1024u);
    c.soA2 = rfl((unsigned)(w < 4 ? 128 * X + 32 * w : 128 * Y + 32 * (w - 4)) * 1024u);
    c.soB = rfl((unsigned)(8 * w) * 8192u + (unsigned)h * 256u);
    c.soS = rfl((unsigned)(8 * w) * 3072u + (unsigned)h * 32u);
    c.soPW = rfl((unsigned)(64 * r + 8 * w) * 8192u + (unsigned)h * 256u); c.soPS = rfl((unsigned)(64 * r + 8 * w) * 3072u + (unsigned)h * 32u);
    c.soPX = rfl((unsigned)(256 * r + 8 * h + w) * 1024u);
    c.soKa = rfl((unsigned)(g * 16384 + wq * 4096)); c.mK0 = ringK;
    c.mA = rfl(lds0 + (unsigned)w * 4096u); c.mB2 = rfl(lds0 + (unsigned)w * 3072u); c.dmy = 0u;
    c.sA1 = mksrd(ckv, 2048u * 1024u); c.sA2 = mksrd(x, 2048u * 1024u);
    c.sUK = mksrd(wuk, 512u * 8192u); c.sUV = mksrd(wuv, 512u * 8192u); c.sWQ = mksrd(wq_, 512u * 8192u); c.sWB = mksrd(wqkv, 512u * 3072u); c.sWP = mksrd(wphi, 32u * 512u);
#pragma unroll
    for (int k = 0; k < 2; ++k) c.voffA[k] = (unsigned)((lane >> 3) * 1024 + (((lane & 7) ^ ((4 * k + (lane >> 4)) & 7)) * 16));
    c.voffB = (unsigned)(((lane >> 2) & 7) * 8192 + (lane >> 5) * 64 + (lane & 3) * 16);
    { const int ch = lane & 3, sub = lane >> 5;
      c.voffS = (unsigned)(((lane >> 2) & 7) * 3072 + (sub == 0 ? (ch < 2 ? ch * 16 : 1024 + (ch - 2) * 16) : 2048 + (ch & 1) * 16)); }
    u32x2 ep, ep2;
    asm volatile("global_load_dwordx2 %0, %1, off sc1" : "=v"(ep) : "v"(flg + 16) : "memory");
    asm volatile("global_load_dwordx2 %0, %1, off sc1" : "=v"(ep2) : "v"((const u64*)(ws + WS_FLG + 65536 + (size_t)r * 1024) + 96) : "memory");
#pragma unroll
    for (int k = 0; k < 8; ++k) asm volatile("global_load_ushort %0, %1, off offset:%2" : "=v"(c.gv[k]) : "v"(gatew + lane), "i"(k * 128) : "memory");
    p_pieceA<0, 0>(c); p_pieceA<0, 1>(c); p_pieceA<0, 2>(c); p_pieceA<0, 3>(c);
    p_pieceB<0, 0>(c); p_pieceB<0, 1>(c); p_pieceB<0, 2>(c); p_pieceB<0, 3>(c);
    p_pieceB<1, 0>(c); p_pieceB<1, 1>(c); p_pieceB<1, 2>(c); p_pieceB<1, 3>(c);
    p_pieceA<1, 0>(c); p_pieceA<1, 1>(c); p_pieceA<1, 2>(c); p_pieceA<1, 3>(c);
    p_warm<0>(c);
    wait_bar<13>();
    asm volatile("" : "+v"(ep), "+v"(ep2), "+v"(c.gv[0]), "+v"(c.gv[1]), "+v"(c.gv[2]), "+v"(c.gv[3]), "+v"(c.gv[4]), "+v"(c.gv[5]), "+v"(c.gv[6]), "+v"(c.gv[7]));
    tag = (((u64)rfl(ep[1]) << 32) | rfl(ep[0])) + 1ull; c.tag = tag;
    tag2 = (((u64)rfl(ep2[1]) << 32) | rfl(ep2[0])) + 1ull;
    {
      f32x16 acc[4][2];
      const int wm = (w >> 1) & 1, wn = w & 1;
      if (g == 0) p_pass<0, 4, 2, 8, true, false>(c, acc, 128u * wm, 2u * wn);
      else        p_pass<0, 4, 2, 8, false, true>(c, acc, 128u * wm, 4u + 2u * wn);
#pragma unroll
      for (int i = 0; i < 8; ++i) { c.kvp[2 * i] = pack8<0>(acc[i >> 1][i & 1]); c.kvp[2 * i + 1] = pack8<1>(acc[i >> 1][i & 1]); }
      c.sO = g == 0 ? sK : sV;
      c.kvo = voff + (unsigned)((4 * r + 2 * wm) * 16384 + (g == 0 ? 4 * wn : 8 * wn) * 1024);
      asm volatile("" :: "v"(c.dmy));
    }
    f32x16 acc[2][3];
    if (g == 0) p_pass<1, 2, 3, 6, true, false>(c, acc, 64u * wq, 0u);
    else        p_pass<1, 2, 3, 6, true, true>(c, acc, 64u * wq, 3u);
        if (wq < 2) {
#pragma unroll
      for (int mt = 0; mt < 2; ++mt)
#pragma unroll
        for (int nt = 0; nt < 3; ++nt)
          if (g == 0 || nt == 0) {
            const f32x16 v = acc[mt][nt] * QSCALE; LAS char* qd = L + 131072 + ((2 * wq + mt) * 8 + 2 * (3 * g + nt)) * 1024 + lane * 16;
            *(LAS u32x4*)qd = pack8<0>(v); *(LAS u32x4*)(qd + 1024) = pack8<1>(v);
          }
    }
    LAS char* Ik = L + (g ? 16384 : 81920) + wq * 4096; LAS char* Iv = Ik + 2048;
    u32x4 hq, hk, hv; float gl;
    if (g == 1) {
      LAS char* ho = L + 81920 + wq * 4096 + lane * 16;
      *(LAS u32x4*)ho = pack8<0>(acc[1][1]); *(LAS u32x4*)(ho + 1024) = pack8<1>(acc[1][1]); *(LAS u32x4*)(ho + 2048) = pack8<0>(acc[1][2]);
      *(LAS float*)(L + 81920 + wq * 4096 + 3072 + lane * 4) = acc[1][2][8];
      hq = pack8<0>(acc[0][1]); hk = pack8<1>(acc[0][1]); hv = pack8<0>(acc[0][2]); gl = acc[0][2][8];
    }
        if (w == 0) { wait_vm<0>(); poll_check(c.fl, flg, tag, lane); }
    LBAR();
    dma16s<0u, 0u>(ringK, voff, sK, c.soKa); dma16s<1024u, 1024u>(ringK, voff, sK, c.soKa); dma16s<2048u, 2048u>(ringK, voff, sK, c.soKa); dma16s<3072u, 3072u>(ringK, voff, sK, c.soKa);
#pragma unroll
    for (int ks = 0; ks < 8; ++ks) q[ks] = ldsfrag(L + 131072 + wq * 8192 + ks * 1024 + lane * 16);
    if (g == 0) {
      hq = *(LAS const u32x4*)(Ik + lane * 16); hk = *(LAS const u32x4*)(Ik + 1024 + lane * 16); hv = *(LAS const u32x4*)(Ik + 2048 + lane * 16); gl = *(LAS const float*)(Ik + 3072 + lane * 4);
    }
    if (wq >= 2) {
#pragma unroll
      for (int mt = 0; mt < 2; ++mt)
#pragma unroll
        for (int nt = 0; nt < 3; ++nt)
          if (g == 0 || nt == 0) {
            const f32x16 v = acc[mt][nt] * QSCALE; const unsigned so = (unsigned)(b * 32768 + ((2 * (wq - 2) + mt) * 8 + 2 * (3 * g + nt)) * 1024);
            st128(pack8<0>(v), sW, voff, so); st128(pack8<1>(v), sW, voff + 1024u, so);
          }
    }
    {
      const int i16 = lane & 15, rt = 2 * wq + (g ^ 1);
      const int sw = (r32 >> 1) & 7; LAS char* wr = Ik + r32 * 64;
      const int gr = ((lane >> 4) & 1) * 4 + (i16 & 3), q4 = i16 >> 2;
      unsigned tb[2];
#pragma unroll
      for (int u = 0; u < 2; ++u) tb[u] = (unsigned)((8 * hh + 4 * u + q4) * 64 + ((gr ^ (4 * hh + 2 * u + (q4 >> 1))) * 8));
      f32x16 pq = MFMA(asbf(c.wp), asbf(hq), (f32x16){});
      f32x16 pk = MFMA(asbf(c.wp), asbf(hk), (f32x16){});
      LWAIT();
      *(LAS u32x2*)(wr + 2048 + ((0 + hh) ^ sw) * 8) = (u32x2){hv[0], hv[1]};
      *(LAS u32x2*)(wr + 2048 + ((2 + hh) ^ sw) * 8) = (u32x2){hv[2], hv[3]};
      *(LAS u32x2*)(wr + 2048 + ((4 + hh) ^ sw) * 8) = (u32x2){hh == 0 ? 0x00003f80u : 0u, 0u};
      *(LAS u32x2*)(wr + 2048 + ((6 + hh) ^ sw) * 8) = (u32x2){0u, 0u};
      softmax2(pk);
#pragma unroll
      for (int k = 0; k < 4; ++k) *(LAS u32x2*)(wr + ((2 * k + hh) ^ sw) * 8) = (u32x2){cvtpk(pk[4 * k], pk[4 * k + 1]), cvtpk(pk[4 * k + 2], pk[4 * k + 3])};
      LWAIT();
      f32x16 kv = {};
#pragma unroll
      for (int ks = 0; ks < 2; ++ks) kv = MFMA(trfrag(Ik + tb[0] + ks * 1024, Ik + tb[1] + ks * 1024), trfrag(Iv + tb[0] + ks * 1024, Iv + tb[1] + ks * 1024), kv);
      softmax2(pq);
      const unsigned so = (unsigned)(WS_PQ - WS_QF) + (unsigned)(b * 16384 + rt * 2048);
      st128(pack8<0>(pq), sW, voff, so); st128(pack8<1>(pq), sW, voff + 1024u, so);
      const float gt = __builtin_amdgcn_rcpf(1.0f + __builtin_amdgcn_exp2f(-1.4426950408889634f * swap32_lo(gl)));
      __builtin_amdgcn_raw_buffer_store_b32(__float_as_uint(gt), sW, (unsigned)r32 * 4u + (unsigned)(WS_GW - WS_QF) + (unsigned)(b * 1024 + rt * 128), 0, 0);
      LWAIT();
#pragma unroll
      for (int k = 0; k < 4; ++k) *(LAS f32x4*)(Ik + k * 1024 + lane * 16) = (f32x4){kv[4 * k], kv[4 * k + 1], kv[4 * k + 2], kv[4 * k + 3]};
    }
    dma16s<16384u, 32768u>(ringK, voff, sK, c.soKa); dma16s<16384u + 1024u, 32768u + 1024u>(ringK, voff, sK, c.soKa);
    dma16s<16384u + 2048u, 32768u + 2048u>(ringK, voff, sK, c.soKa); dma16s<16384u + 3072u, 32768u + 3072u>(ringK, voff, sK, c.soKa);
    dma16s<0u, 0u>(ringV, voff, sV, c.soKa); dma16s<1024u, 1024u>(ringV, voff, sV, c.soKa);
    dma16s<2048u, 2048u>(ringV, voff, sV, c.soKa); dma16s<3072u, 3072u>(ringV, voff, sV, c.soKa);
        wait_bar<11>();
    if (g == 1) {
      f32x4 s4 = *(LAS const f32x4*)(L + 16384 + wq * 1024 + lane * 16);
#pragma unroll
      for (int k = 1; k < 8; ++k) s4 += *(LAS const f32x4*)(L + (k < 4 ? 16384 : 81920) + (k & 3) * 4096 + wq * 1024 + lane * 16);
      st128_wt(__builtin_bit_cast(u32x4, s4), sW, voff, (unsigned)(WS_KVP - WS_QF) + (unsigned)((h * 8 + r) * 4096 + wq * 1024));
      LBAR();
    }
  }
  f32x16 O[4], S[2], keep0 = {}, keep1 = {}, negm = {};
#pragma unroll
  for (int d = 0; d < 4; ++d) O[d] = (f32x16){};
  bf16x8 p[4];
  float m = 0.f, l = 0.f, mk = 0.f, lk = 0.f, pm0 = 0.f;
  float gate[2] = {0.f, 0.f}; f32x2 ps = {0.f, 0.f};
  u32x2 fl2 = {0u, 0u};
  u32x4 warm_dummy; unsigned warm_dm1;
  LAS const char* kbase = L + (g ? 98304 : 32768) + lane * 16;
  LAS const char* vbase = L + (g ? 0 : 65536) + lane * 16;
  LAS char* xr = L + 131072 + wq * 8192 + lane * 16;
#define TILE(s) ((s) < nX ? 2 * (s) + g : 2 * ((s) - nX) + g)
#define CF_SO(blkrow, half) ((unsigned)(WS_CF - WS_QF) + (unsigned)(((4 * (blkrow) + wq) * 288 + 8 * h + 4 * (half)) * 1024))
#define ST_SO(wv) ((unsigned)(WS_ST - WS_QF) + (unsigned)(b * 2048 + (wv) * 256))
#define ST_LOAD(wv) asm volatile("buffer_load_dwordx2 %0, %1, %2, %3 offen" : "+v"(ps) : "v"((unsigned)r32 * 8u), "s"(sW), "s"(rfl(ST_SO(wv))) : "memory")
#define L_SOFTMAX(DIAG, FIRST, D0, D1, D2, D3, D4, D5, D6, D7) do { \
    if (DIAG) { int rel = 32 * wq + r32 - 64 * g - 4 * hh; \
      _Pragma("unroll") for (int e = 0; e < 16; ++e) { const int cc = (e & 3) + 8 * (e >> 2); if ((e & 3) == 0) { asm volatile("" : "+v"(rel)); SBAR(); } \
        if (cc > rel) S[0][e] = -__builtin_inff(); if (cc + 32 > rel) S[1][e] = -__builtin_inff(); } \
      pm0 = max2f(S[0][0], S[0][1]); _Pragma("unroll") for (int e = 2; e < 16; e += 2) pm0 = max3f(pm0, S[0][e], S[0][e + 1]); } \
    float pm = max3f(pm0, S[1][0], S[1][1]), pn = max3f(S[1][2], S[1][3], S[1][4]); \
    pm = max3f(pm, S[1][5], S[1][6]); pn = max3f(pn, S[1][7], S[1][8]); pm = max3f(pm, S[1][9], S[1][10]); pn = max3f(pn, S[1][11], S[1][12]); \
    pm = max3f(pm, S[1][13], S[1][14]); pm = max3f(pm, pn, S[1][15]); \
    { auto rr = __builtin_amdgcn_permlane32_swap(__float_as_uint(pm), __float_as_uint(pm), false, false); pm = max2f(__uint_as_float(rr[0]), __uint_as_float(rr[1])); } \
    SBAR(); D0; SBAR(); \
    const bool first_ = (FIRST); \
    if (first_ || __builtin_expect(__any(pm > THR), 0)) { \
      NOP16(); SBAR(); \
      const float dl = fmaxf(pm, first_ ? -1e30f : 0.f); \
      m += dl; \
      if (!first_) { const float f = __builtin_amdgcn_exp2f(-dl); l *= f; _Pragma("unroll") for (int d = 0; d < 4; ++d) O[d] = O[d] * f; } \
      _Pragma("unroll") for (int e = 0; e < 16; ++e) { S[0][e] -= dl; S[1][e] -= dl; negm[e] = -m; } \
    } \
    float ls0 = l, ls1 = 0.f; unsigned pw[8]; \
    SM_GRP(0, D1); SM_GRP(2, D2); SM_GRP(4, D3); SM_GRP(6, D4); SM_GRP(8, D5); SM_GRP(10, D6); SM_GRP(12, D7); SM_GRP(14, (void)0); \
    l = ls0 + ls1; \
    p[0] = asbf((u32x4){pw[0], pw[1], pw[2], pw[3]}); p[1] = asbf((u32x4){pw[4], pw[5], pw[6], pw[7]}); } while (0)
#define SM_GRP(e0, DMA) do { const float x0 = __builtin_amdgcn_exp2f(S[0][e0]), x1 = __builtin_amdgcn_exp2f(S[0][(e0) + 1]); \
      ls0 += x0; ls1 += x1; pw[(e0) >> 1] = cvtpk(x0, x1); asm volatile("" : "+v"(pw[(e0) >> 1]), "+v"(ls0), "+v"(ls1)); DMA; SBAR(); } while (0)
#define L_SUM() do { } while (0)
  float mo = 0.f, lo = 0.f;
  auto l_pre = [&](int s) {
    if (s == nX) {
      NOP16(); SBAR();
      mo = m; lo = swap32_add(l);
      if (g == 1) ST_LOAD(w - 4);
      __builtin_amdgcn_raw_buffer_store_b64(__builtin_bit_cast(u32x2, (f32x2){mo, lo}), sW, (unsigned)r32 * 8u, ST_SO(w), 0);
      if (g == 0) { a_send(O[2], O[3], xr); keep0 = O[0]; keep1 = O[1]; mk = mo; lk = lo; }
      m = 0.f; l = 0.f;
    }
    if (g == 0 && s == nX + 1) ST_LOAD(w + 4);
  };
  auto l_post = [&](int s) {
    if (s == nX) {
      if (g == 1) {
        wait_vm<8 - NMV>(); asm volatile("" : "+v"(ps));
        a_finish(O[2], O[3], mo, lo, xr, ps[0], ps[1], gate[0], sW, voff, CF_SO(X, 1));
        LWAIT();
        a_send(O[0], O[1], xr);
      }
#pragma unroll
      for (int d = 0; d < 4; ++d) O[d] = (f32x16){};
    }
    if (g == 0 && s == nX + 1) {
      wait_vm<8 - NMV>(); asm volatile("" : "+v"(ps));
      a_finish(keep0, keep1, mk, lk, xr, ps[0], ps[1], gate[0], sW, voff, CF_SO(X, 0));
    }
  };
  unsigned mK = ringK, mV = ringV + 16384u, oK = (unsigned)(4 + g) * 16384u + (unsigned)wq * 4096u, oV = (unsigned)(2 + g) * 16384u + (unsigned)wq * 4096u;
  LAS const char* kp = kbase + 16384; LAS const char* vp = vbase;
#define LV(x) do { if constexpr (NMV == 0) { x; } } while (0)
#define A_TAIL() do { \
    asm volatile("" : "+v"(p[0]), "+v"(p[1]), "+v"(l), "+v"(S[1])); \
    SBAR(); wait_bar<8 - NMV>(); SBAR(); \
    a_mphase<true, true>(kp, vp, q, p, S, O, negm, l, pm0, mV, voff, sV, oV); oV += 32768u; \
    mK ^= 16384u; mV ^= 16384u; kp = (LAS const char*)((unsigned)(uintptr_t)kp ^ 16384u); vp = (LAS const char*)((unsigned)(uintptr_t)vp ^ 16384u); } while (0)
#define A_FAST(FIRST) do { \
    if (!(FIRST)) L_SUM(); \
    L_SOFTMAX(false, FIRST, dma16o<0>(mK, voff, sK, oK), dma16n<1024>(voff, sK, oK), dma16n<2048>(voff, sK, oK), dma16n<3072>(voff, sK, oK), \
              LV(dma16o<0>(mV, voff, sV, oV)), LV(dma16n<1024>(voff, sV, oV)), LV(dma16n<2048>(voff, sV, oV)), LV(dma16n<3072>(voff, sV, oV))); \
    oK += 32768u; A_TAIL(); } while (0)
#define A_SLOW(s) do { \
    L_SUM(); l_pre(s); \
    oK = (s) < 15 ? (unsigned)TILE((s) + 2) * 16384u + (unsigned)wq * 4096u : (unsigned)(WS_KVP - WS_QF) + (unsigned)(h * 32768 + w * 4096); \
    oV = (unsigned)TILE((s) + 1) * 16384u + (unsigned)wq * 4096u; \
    const srd_t sKx = (s) < 15 ? sK : sW; \
    L_SOFTMAX((s) == nX - 1, (s) == nX, dma16o<0>(mK, voff, sKx, oK), dma16n<1024>(voff, sKx, oK), dma16n<2048>(voff, sKx, oK), dma16n<3072>(voff, sKx, oK), \
              LV(dma16o<0>(mV, voff, sV, oV)), LV(dma16n<1024>(voff, sV, oV)), LV(dma16n<2048>(voff, sV, oV)), LV(dma16n<3072>(voff, sV, oV))); \
    oK += 32768u; \
    l_post(s); \
    if ((s) == nX - 1) { _Pragma("unroll") for (int ks = 0; ks < 8; ++ks) q[ks] = ldsfrag(L + 131072 + wq * 8192 + ks * 1024 + lane * 16); negm = (f32x16){}; } \
    A_TAIL(); } while (0)
  a_mphase<true, false>(kbase, vbase, q, p, S, O, negm, l, pm0, 0u, voff, sV, 0u);
  A_FAST(true);
  if (w == 0 && lane == 0) __hip_atomic_store((u64*)flg + 8 + r, tag, __ATOMIC_RELAXED, __HIP_MEMORY_SCOPE_AGENT);
  A_FAST(false);
  {
    const unsigned mq = rfl(lds0 + 131072u + (unsigned)w * 4096u), oq = rfl((unsigned)(b * 32768 + w * 4096));
    dma16o<0>(mq, voff, sW, oq); dma16n<1024>(voff, sW, oq); dma16n<2048>(voff, sW, oq); dma16n<3072>(voff, sW, oq);
    const unsigned go = rfl((unsigned)(WS_GW - WS_QF) + (unsigned)(b * 1024 + wq * 128));
    asm volatile("buffer_load_dword %0, %2, %3, %4 offen\n\tbuffer_load_dword %1, %2, %3, %4 offen offset:512" : "+v"(gate[0]), "+v"(gate[1]) : "v"((unsigned)r32 * 4u), "s"(sW), "s"(go) : "memory");
    poll_issue(fl2, flg + 8, lane);
    const srd_t sWa = mksrd(wo_a, 4096u * 1024u), sWb = mksrd(wo_b, 512u * 1024u);
    u32x4 dmy; unsigned dm1;
    asm volatile("buffer_load_dwordx4 %0, %2, %3, 0 offen\n\tbuffer_load_dwordx4 %0, %2, %3, 0 offen offset:1024\n\t"
                 "buffer_load_dwordx4 %0, %4, %5, 0 offen\n\tbuffer_load_dword %1, %6, %3, 0 offen"
                 : "=&v"(dmy), "=&v"(dm1) : "v"(voff + (unsigned)(b * 16384 + w * 2048)), "s"(sWa), "v"((unsigned)(b * 2048 + w * 256) + (voff & 255u)), "s"(sWb),
                   "v"((unsigned)((((b >> 3) * 8 + w) * 4 + (lane & 3)) * 4096 + (lane >> 2) * 0)) : "memory");
    warm_dummy = dmy; warm_dm1 = dm1;
  }
  A_FAST(false);
  asm volatile("" : "+v"(gate[0]), "+v"(gate[1]), "+v"(fl2));
  asm volatile("" :: "v"(warm_dummy), "v"(warm_dm1));
  if (w == 0) { poll_check(fl2, flg + 8, tag, lane); if (r == 0 && lane == 0) __hip_atomic_store((u64*)flg + 16, tag, __ATOMIC_RELAXED, __HIP_MEMORY_SCOPE_AGENT); }
  {
    int s = 3;
#pragma unroll 1
    for (int blk = 0; blk < 2; ++blk) {
      const int e1 = blk ? 15 : nX - 2, e2 = blk ? 16 : (nX + 2 < 16 ? nX + 2 : 16);
#pragma unroll 1
      for (; s < e1; ++s) A_FAST(false);
#pragma unroll 1
      for (; s < e2; ++s) A_SLOW(s);
    }
  }
  {
    l_pre(16);
    const unsigned mp = ringK, op = rfl((unsigned)(WS_PQ - WS_QF) + (unsigned)(b * 16384 + wq * 2048));
#define G0D(x) do { if (g == 0) { x; } } while (0)
    L_SOFTMAX(true, nX == 16, G0D(dma16o<0>(mp, voff, sW, op)), G0D(dma16n<1024>(voff, sW, op)), G0D(dma16o<0>(mp + 2048u, voff, sW, op + 8192u)), G0D(dma16n<1024>(voff, sW, op + 8192u)),
              (void)0, (void)0, (void)0, (void)0);
#undef G0D
    if (16 == nX || 16 == nX + 1) wait_vm<0>();
    l_post(16);
    asm volatile("" : "+v"(p[0]), "+v"(p[1]), "+v"(l), "+v"(S[1]));
    SBAR(); if (g == 0) wait_bar<4>(); else wait_bar<0>(); SBAR();
  }
  a_mphase<false, true>(kbase, vbase, q, p, S, O, negm, l, pm0, 0u, voff, sV, 0u);
  NOP16(); SBAR();
  if (g == 0 && nX == 16) {
    ST_LOAD(w + 4); wait_vm<0>(); asm volatile("" : "+v"(ps));
    a_finish(keep0, keep1, mk, lk, xr, ps[0], ps[1], gate[0], sW, voff, CF_SO(X, 0));
    LWAIT();
  }
  LAS f32x2* stl = (LAS f32x2*)(L + 81920) + r32;
  LAS char* xr1 = L + (wq < 2 ? 16384 : 98304 - 16384) + wq * 8192 + lane * 16;
  const float lt = swap32_add(l);
  stl[w * 32] = (f32x2){m, lt};
  if (g == 0) {
    a_send(O[2], O[3], xr);
    wait_bar<0>();
    f32x16 kv = {};
#pragma unroll
    for (int k = 0; k < 8; ++k)
#pragma unroll
      for (int qd = 0; qd < 4; ++qd) { const f32x4 v = *(LAS const f32x4*)(L + (k < 4 ? 49152 : 114688) + (k & 3) * 4096 + qd * 1024 + lane * 16);
#pragma unroll
        for (int e = 0; e < 4; ++e) kv[4 * qd + e] += v[e]; }
    const bf16x8 k0 = asbf(pack8<0>(kv)), k1 = asbf(pack8<1>(kv));
#pragma unroll
    for (int t = 0; t < 2; ++t) {
      f32x16 o = MFMA(k0, ldsfrag(kbase + wq * 4096 + t * 2048), (f32x16){});
      o = MFMA(k1, ldsfrag(kbase + wq * 4096 + t * 2048 + 1024), o);
      o = o * ((1.0f - gate[t]) / (swap32_lo(o[8]) + 1e-6f));
      st128_wt(pack8<0>(o), sW, voff, (unsigned)(WS_CF - WS_QF) + (unsigned)(((4 * (t ? Y : X) + wq) * 288 + 256 + h) * 1024));
    }
    LBAR();
    const f32x2 po = stl[(w + 4) * 32];
    a_finish(O[0], O[1], m, lt, xr1, po[0], po[1], gate[1], sW, voff, CF_SO(Y, 0));
  } else {
    a_send(O[0], O[1], xr1);
    LBAR();
    const f32x2 po = stl[(w - 4) * 32];
    a_finish(O[2], O[3], m, lt, xr, po[0], po[1], gate[1], sW, voff, CF_SO(Y, 1));
  }
  o_phase(wo_a, wo_b, ws, out, L, lds0, lane, w, h, r, tag2, sW);
#undef TILE
#undef CF_SO
#undef ST_SO
#undef ST_LOAD
#undef L_SOFTMAX
#undef SM_GRP
#undef L_SUM
#undef LV
#undef A_TAIL
#undef A_FAST
#undef A_SLOW
}

extern "C" void kernel_launch(void* const* d_in, const int* in_sizes, int n_in, void* d_out, int out_size, void* d_ws, size_t ws_size, hipStream_t stream) {
  const u16* x = (const u16*)d_in[0]; const u16* ckv = (const u16*)d_in[1]; const u16* wq = (const u16*)d_in[2];
  const u16* wuk = (const u16*)d_in[3]; const u16* wuv = (const u16*)d_in[4]; const u16* wo_a = (const u16*)d_in[5];
  const u16* wqkv = (const u16*)d_in[7]; const u16* wphi = (const u16*)d_in[8]; const u16* wo_b = (const u16*)d_in[9]; const u16* gatew = (const u16*)d_in[10];
  (void)hipFuncSetAttribute((const void*)kF, hipFuncAttributeMaxDynamicSharedMemorySize, 163840);
  kF<<<256, 512, 163840, stream>>>(x, ckv, wq, wuk, wuv, wqkv, wphi, gatew, wo_a, wo_b, (char*)d_ws, (u16*)d_out);
}
```

```cpp
#include <hip/hip_runtime.h>
#include <stdint.h>

typedef unsigned short u16;
typedef unsigned long long u64;
typedef __bf16 bf16x8 __attribute__((ext_vector_type(8)));
typedef __bf16 bf16x2 __attribute__((ext_vector_type(2)));
typedef float f32x2 __attribute__((ext_vector_type(2)));
typedef float f32x4 __attribute__((ext_vector_type(4)));
typedef float f32x16 __attribute__((ext_vector_type(16)));
typedef unsigned u32x2 __attribute__((ext_vector_type(2)));
typedef unsigned u32x4 __attribute__((ext_vector_type(4)));
typedef short s16x4 __attribute__((ext_vector_type(4)));
typedef short s16x8 __attribute__((ext_vector_type(8)));
typedef __amdgpu_buffer_rsrc_t srd_t;
#define LAS __attribute__((address_space(3)))
#define DI __device__ __forceinline__
#define SBAR() __builtin_amdgcn_sched_barrier(0)
#define MFMA(a, b, c) __builtin_amdgcn_mfma_f32_32x32x16_bf16((a), (b), (c), 0, 0, 0)

constexpr size_t MB = 1024 * 1024;
constexpr size_t WS_KF = 0;
constexpr size_t WS_VF = 16 * MB;
constexpr size_t WS_QF = 32 * MB;
constexpr size_t WS_CF = 48 * MB;
constexpr size_t WS_PQ = 68 * MB;
constexpr size_t WS_KVP = 72 * MB;
constexpr size_t WS_GW = 73 * MB;
constexpr size_t WS_ST = 74 * MB;
constexpr size_t WS_FLG = 93 * MB;
constexpr size_t WS_DBG = 100 * MB;

constexpr float QSCALE = 0.08838834764831845f * 1.4426950408889634f;
constexpr float THR = 8.0f;

DI unsigned cvtpk(float lo, float hi) { f32x2 v = {lo, hi}; bf16x2 b = __builtin_convertvector(v, bf16x2); return __builtin_bit_cast(unsigned, b); }
template <int S> DI u32x4 pack8(const f32x16& x) {
  return (u32x4){cvtpk(x[8 * S + 0], x[8 * S + 1]), cvtpk(x[8 * S + 2], x[8 * S + 3]), cvtpk(x[8 * S + 4], x[8 * S + 5]), cvtpk(x[8 * S + 6], x[8 * S + 7])};
}
DI bf16x8 asbf(u32x4 v) { return __builtin_bit_cast(bf16x8, v); }
DI float swap32_max(float v) { auto rr = __builtin_amdgcn_permlane32_swap(__float_as_uint(v), __float_as_uint(v), false, false); return fmaxf(__uint_as_float(rr[0]), __uint_as_float(rr[1])); }
DI float swap32_add(float v) { auto rr = __builtin_amdgcn_permlane32_swap(__float_as_uint(v), __float_as_uint(v), false, false); return __uint_as_float(rr[0]) + __uint_as_float(rr[1]); }
DI float swap32_lo(float v) { auto rr = __builtin_amdgcn_permlane32_swap(__float_as_uint(v), __float_as_uint(v), false, false); return __uint_as_float(rr[0]); }
DI srd_t mksrd(const void* p, unsigned bytes) { return __builtin_amdgcn_make_buffer_rsrc((void*)p, (short)0, (int)bytes, 0x00020000); }
DI unsigned rfl(unsigned x) { return (unsigned)__builtin_amdgcn_readfirstlane((int)x); }
template <int OFF> DI void dma16o(unsigned m0v, unsigned voff, srd_t srd, unsigned soff) {
  asm volatile("s_mov_b32 m0, %0\n\ts_nop 0\n\tbuffer_load_dwordx4 %1, %2, %3 offen offset:%4 sc1 lds" :: "s"(m0v), "v"(voff), "s"(srd), "s"(soff), "i"(OFF) : "memory");
}
template <unsigned MOFF, unsigned SOFF> DI void dma16s(unsigned mb, unsigned voff, srd_t srd, unsigned sb) {
  unsigned t;
  asm volatile("s_add_u32 m0, %1, %5\n\ts_add_u32 %0, %4, %6\n\tbuffer_load_dwordx4 %2, %3, %0 offen sc1 lds" : "=&s"(t) : "s"(mb), "v"(voff), "s"(srd), "s"(sb), "i"(MOFF), "i"(SOFF) : "memory", "scc");
}
template <int OFF> DI void dma16n(unsigned voff, srd_t srd, unsigned soff) {
  asm volatile("buffer_load_dwordx4 %0, %1, %2 offen offset:%3 sc1 lds" :: "v"(voff), "s"(srd), "s"(soff), "i"(OFF) : "memory");
}
template <unsigned MOFF, unsigned SOFF> DI void dma16c(unsigned mb, unsigned voff, srd_t srd, unsigned sb) {
  unsigned t;
  asm volatile("s_add_u32 m0, %1, %5\n\ts_add_u32 %0, %4, %6\n\tbuffer_load_dwordx4 %2, %3, %0 offen lds" : "=&s"(t) : "s"(mb), "v"(voff), "s"(srd), "s"(sb), "i"(MOFF), "i"(SOFF) : "memory", "scc");
}
template <unsigned MOFF, unsigned SOFF> DI void dma16m(unsigned mb, unsigned voff, srd_t srd, unsigned sb) {
  unsigned t;
  asm volatile("s_add_u32 m0, %1, %5\n\ts_add_u32 %0, %4, %6\n\ts_mov_b32 exec_hi, 0x33333333\n\tbuffer_load_dwordx4 %2, %3, %0 offen lds\n\ts_mov_b32 exec_hi, -1"
               : "=&s"(t) : "s"(mb), "v"(voff), "s"(srd), "s"(sb), "i"(MOFF), "i"(SOFF) : "memory", "scc");
}
template <int N> DI void wait_bar() { asm volatile("s_waitcnt vmcnt(%0) lgkmcnt(0)\n\ts_barrier" :: "i"(N) : "memory"); }
template <int N> DI void wait_vm() { asm volatile("s_waitcnt vmcnt(%0)" :: "i"(N) : "memory"); }
#define SPIN_OVER(t0) (__builtin_amdgcn_s_memrealtime() - (t0) > 20000000ull)
#define LBAR() asm volatile("s_waitcnt lgkmcnt(0)\n\ts_barrier" ::: "memory")
#define LWAIT() asm volatile("s_waitcnt lgkmcnt(0)" ::: "memory")
#define NOP16() asm volatile("s_nop 7\n\ts_nop 7" ::: "memory")
DI bf16x8 ldsfrag(LAS const char* p) { return *(LAS const bf16x8*)p; }
DI bf16x8 trfrag(LAS const char* p0, LAS const char* p1) {
  s16x4 a = __builtin_amdgcn_ds_read_tr16_b64_v4i16((LAS s16x4*)p0);
  s16x4 b = __builtin_amdgcn_ds_read_tr16_b64_v4i16((LAS s16x4*)p1);
  return __builtin_bit_cast(bf16x8, __builtin_shufflevector(a, b, 0, 1, 2, 3, 4, 5, 6, 7));
}
DI void st128(u32x4 v, srd_t s, unsigned vo, unsigned so) { __builtin_amdgcn_raw_buffer_store_b128(v, s, vo + so, 0, 0); }
DI void st128_wt(u32x4 v, srd_t s, unsigned vo, unsigned so) { __builtin_amdgcn_raw_buffer_store_b128(v, s, vo + so, 0, 16); }

struct OCtx {
  srd_t sC, sLo, sHi; unsigned voff, voffW, voffV, soA, soW[3], mW, bB[2][3];
  LAS char* L; bf16x8 a[36], f[6];
};
constexpr int O_NS = 5;
template <int S, int I> DI void o_wpiece(const OCtx& c) {
  if constexpr (S < 12) {
    constexpr unsigned sl = (unsigned)(S % O_NS) * 24576u + I * 8192u, row = S < 6 ? 192u * S + 64u * I : 384u * ((S - 6) % 3) + 128u * I;
    constexpr int t = S < 6 ? 0 : (S < 9 ? 1 : 2);
    dma16c<sl, row * 1024u>(c.mW, S < 6 ? c.voffW : c.voffV, row < 640u ? c.sLo : c.sHi, c.soW[t]);
  }
}
template <int i> DI void o_aload(OCtx& c) {
  if constexpr (i >= 0 && i < 36) {
    unsigned t;
    asm volatile("s_add_u32 %1, %4, %5\n\tbuffer_load_dwordx4 %0, %2, %3, %1 offen sc1" : "=v"(c.a[i]), "=&s"(t) : "v"(c.voff), "s"(c.sC), "s"(c.soA), "i"(i * 2048u) : "memory", "scc");
  }
}
template <int S, int k> DI void o_ride(OCtx& c) {
  if constexpr (S == 0) o_aload<12 + k>(c);
  else if constexpr (S == 1) { if constexpr (k < 3) o_wpiece<5, k>(c); else if constexpr (k < 9) { o_aload<24 + 2 * (k - 3)>(c); o_aload<25 + 2 * (k - 3)>(c); } }
  else { if constexpr (k == 1) o_wpiece<S + 4, 0>(c); if constexpr (k == 5) o_wpiece<S + 4, 1>(c); if constexpr (k == 9) o_wpiece<S + 4, 2>(c); }
}
template <int S> DI void o_wstage(OCtx& c) { o_wpiece<S, 0>(c); o_wpiece<S, 1>(c); o_wpiece<S, 2>(c); }
#define O_SP(S) (c.L + c.bB[(S) < 6 ? 0 : 1][((S) % O_NS) >> 1] + (((S) % O_NS) & 1) * 24576)
#define O_FR(S, p, k) ((S) < 6 ? trfrag((p) + ((k) >> 1) * 4096 + ((k) & 1) * 512, (p) + ((k) >> 1) * 4096 + ((k) & 1) * 512 + 1024) : trfrag((p) + (k) * 2048, (p) + (k) * 2048 + 512))
template <int S, int NY> DI void o_stage(OCtx& c, f32x16 (&acc)[2]) {
  constexpr int i0 = S < 6 ? 6 * S : 12 * ((S - 6) % 3);
  if constexpr (NY >= 0) {
    if constexpr (S < 6) asm volatile("s_waitcnt vmcnt(%6)\n\ts_barrier" : "+v"(c.a[i0]), "+v"(c.a[i0 + 1]), "+v"(c.a[i0 + 2]), "+v"(c.a[i0 + 3]), "+v"(c.a[i0 + 4]), "+v"(c.a[i0 + 5]) : "i"(NY) : "memory");
    else                 asm volatile("s_waitcnt vmcnt(%0)\n\ts_barrier" :: "i"(NY) : "memory");
  }
  SBAR();
  LAS const char* sp = O_SP(S); LAS const char* sn = O_SP(S + 1);
  bf16x8 f[12];
  if constexpr (S == 0) { c.f[0] = O_FR(S, sp, 0); c.f[1] = O_FR(S, sp, 1); c.f[2] = O_FR(S, sp, 2); c.f[3] = O_FR(S, sp, 3); c.f[4] = O_FR(S, sp, 4); c.f[5] = O_FR(S, sp, 5); SBAR(); }
  f[0] = c.f[0]; f[1] = c.f[1]; f[2] = c.f[2]; f[3] = c.f[3]; f[4] = c.f[4]; f[5] = c.f[5];
#define O_LD(k) f[k] = O_FR(S, sp, k)
#define O_LN(k) do { if constexpr (S < 11) c.f[k] = O_FR(S + 1, sn, k); } while (0)
#define O_MM(k) do { if constexpr (S < 6) acc[(k) & 1] = MFMA(f[k], c.a[i0 + ((k) >> 1)], acc[(k) & 1]); else acc[0] = MFMA(f[k], c.a[i0 + (k)], acc[0]); o_ride<S, k>(c); SBAR(); } while (0)
  O_LD(6); O_MM(0); O_LD(7); O_MM(1); O_LD(8); O_MM(2); O_LD(9); O_MM(3); O_LD(10); O_MM(4); O_LD(11); O_MM(5);
  O_LN(0); O_MM(6); O_LN(1); O_MM(7); O_LN(2); O_MM(8); O_LN(3); O_MM(9); O_LN(4); O_MM(10); O_LN(5); O_MM(11);
#undef O_LD
#undef O_LN
#undef O_MM
}
DI void o_park(LAS char* p, const f32x16& v) {
#pragma unroll
  for (int qd = 0; qd < 4; ++qd) *(LAS f32x4*)(p + qd * 1024) = (f32x4){v[4 * qd], v[4 * qd + 1], v[4 * qd + 2], v[4 * qd + 3]};
}
DI void o_addlds(f32x16& v, LAS const char* p) {
#pragma unroll
  for (int qd = 0; qd < 4; ++qd) { const f32x4 u = *(LAS const f32x4*)(p + qd * 1024);
#pragma unroll
    for (int e = 0; e < 4; ++e) v[4 * qd + e] += u[e]; }
}
DI void o_publish(srd_t sW, unsigned voff, const f32x16& v, unsigned so) {
#pragma unroll
  for (int qd = 0; qd < 4; ++qd) st128_wt(__builtin_bit_cast(u32x4, (f32x4){v[4 * qd], v[4 * qd + 1], v[4 * qd + 2], v[4 * qd + 3]}), sW, voff + (unsigned)(qd * 1024), so);
}
DI void o_phase(const u16* wo_a, const u16* wo_b, char* ws, u16* out, LAS char* L, unsigned lds0, int lane, int w, int h, int r, u64 tag, srd_t sW) {
  const int j = h >> 3, mi = (h & 4) ? 15 - r : r, ni = h & 3, b = blockIdx.x;
  const int g = w >> 2, rb = w & 3, hh = lane >> 5, i16 = lane & 15, p4 = i16 & 3, pj = j & 2;
  u64* fl = (u64*)(ws + WS_FLG + 65536 + (size_t)r * 1024);
  OCtx c;
  c.L = L;
  c.sC = mksrd(ws + WS_CF, 64u * 288u * 1024u); c.sLo = mksrd(wo_a, 4096u * 1024u);
  c.sHi = mksrd(j == 3 ? (const char*)wo_b - 4194304 : (const char*)wo_a, (j == 3 ? 4608u : 4096u) * 1024u);
    c.voff = (unsigned)lane * 16u;
  c.voffW = (unsigned)(((lane >> 2) & 7) * 1024 + (lane >> 5) * 64 + (lane & 3) * 16);
  c.voffV = (unsigned)((lane >> 2) * 1024 + (lane & 3) * 16);
  c.soA = rfl((unsigned)((4 * mi + rb) * 288 + 72 * j + g) * 1024u);
  c.soW[0] = rfl((unsigned)(1152 * j + 8 * w) * 1024u + (unsigned)(ni * 256 + (pj ^ 2) * 64));
  c.soW[1] = rfl((unsigned)(1152 * j + 16 * w) * 1024u + (unsigned)(ni * 256 + (j ^ 1) * 64));
  c.soW[2] = rfl((unsigned)(1152 * j + 16 * w) * 1024u + (unsigned)(ni * 256 + j * 64));
  c.mW = rfl(lds0 + (unsigned)w * 1024u);
#pragma unroll
  for (int k = 0; k < 3; ++k) {
    c.bB[1][k] = (unsigned)(k * 49152 + g * 1024 + (4 * hh + (i16 >> 2)) * 64 + ((lane >> 4) & 1) * 32 + (((p4 & 1) << 1) | (p4 >> 1)) * 8);
    c.bB[0][k] = c.bB[1][k] + (unsigned)(g * 1024);
    asm volatile("" : "+v"(c.bB[0][k]), "+v"(c.bB[1][k]));
  }
  LAS char* red = L + 122880 + w * 4096 + c.voff; LAS const char* rrd = L + 122880 + (w ^ 4) * 4096 + c.voff;
  wait_bar<0>();
  if (w == 0 && lane == 0) __hip_atomic_store(fl + h, tag, __ATOMIC_RELAXED, __HIP_MEMORY_SCOPE_AGENT);
  o_wstage<0>(c); o_wstage<1>(c); o_wstage<2>(c); o_wstage<3>(c); o_wstage<4>(c);
  if (w == 0) {
    const bool need = j == 3 || (lane >= 9 * j && lane < 9 * j + 9);
    const u64 t0 = __builtin_amdgcn_s_memrealtime();
    while (!__all(!need || __hip_atomic_load(fl + (lane & 31), __ATOMIC_RELAXED, __HIP_MEMORY_SCOPE_AGENT) == tag)) { __builtin_amdgcn_s_sleep(1); if (SPIN_OVER(t0)) break; }
    if (h == 31 && lane == 0) __hip_atomic_store(fl + 96, tag, __ATOMIC_RELAXED, __HIP_MEMORY_SCOPE_AGENT);
  }
  asm volatile("s_barrier" ::: "memory");
  o_aload<0>(c); o_aload<1>(c); o_aload<2>(c); o_aload<3>(c); o_aload<4>(c); o_aload<5>(c); o_aload<6>(c); o_aload<7>(c); o_aload<8>(c);
  o_aload<9>(c); o_aload<10>(c); o_aload<11>(c);
  f32x16 acc[2] = {{}, {}};
  o_stage<0, 6>(c, acc); o_stage<1, 12>(c, acc); o_stage<2, 21>(c, acc); o_stage<3, 18>(c, acc); o_stage<4, 12>(c, acc); o_stage<5, 6>(c, acc);
  o_park(red, g ? acc[0] : acc[1]);
  f32x16 prev = g ? acc[1] : acc[0];
  acc[0] = (f32x16){};
  wait_bar<6>(); SBAR();
  o_addlds(prev, rrd); o_publish(sW, c.voff, prev, (unsigned)(b * 32768 + g * 16384 + rb * 4096)); SBAR();
  o_stage<6, -1>(c, acc);
  o_stage<7, 10>(c, acc);
  o_stage<8, 6>(c, acc);
  if (w == 0 && lane == 0) __hip_atomic_store(fl + 32 + h, tag, __ATOMIC_RELAXED, __HIP_MEMORY_SCOPE_AGENT);
  if (g == 1) o_park(red, acc[0]);
  prev = acc[0]; acc[0] = (f32x16){};
  wait_bar<3>(); SBAR();
  if (g == 0) { o_addlds(prev, rrd); o_publish(sW, c.voff, prev, (unsigned)(WS_PQ - WS_QF) + (unsigned)(b * 16384 + rb * 4096)); }
  SBAR();
  o_stage<9, -1>(c, acc);
  o_stage<10, 0>(c, acc);
  if (w == 0 && lane == 0) __hip_atomic_store(fl + 64 + h, tag, __ATOMIC_RELAXED, __HIP_MEMORY_SCOPE_AGENT);
  o_stage<11, -1>(c, acc);
    if (g == 1) o_park(red, acc[0]);
  if (w == 0) {
    const unsigned l3 = c.voff >> 4; const u64* pf = fl + ((l3 & 3) == 1 ? 64 + (h ^ 8) : 32 + (h ^ ((l3 & 1) ? 24 : 16)));
    const u64 t0 = __builtin_amdgcn_s_memrealtime();
    while (!__all(__hip_atomic_load(pf, __ATOMIC_RELAXED, __HIP_MEMORY_SCOPE_AGENT) == tag)) { __builtin_amdgcn_s_sleep(1); if (SPIN_OVER(t0)) break; }
  }
  LBAR();
  if (g == 1) return;
    prev = acc[0];
#pragma unroll
  for (int k = 1; k < 4; ++k) {
    const int bp = b ^ (2 * k);
    const unsigned so = k == 1 ? (unsigned)(WS_PQ - WS_QF) + (unsigned)(bp * 16384 + rb * 4096) : (unsigned)(bp * 32768 + (j & 1) * 16384 + rb * 4096);
#pragma unroll
    for (int qd = 0; qd < 4; ++qd) {
      const f32x4 v = __builtin_bit_cast(f32x4, __builtin_amdgcn_raw_buffer_load_b128(sW, c.voff + (unsigned)(qd * 1024), so, 16));
#pragma unroll
      for (int e = 0; e < 4; ++e) prev[4 * qd + e] += v[e];
    }
  }
  o_addlds(prev, rrd);
  unsigned vo = c.voff; asm volatile("" : "+v"(vo));
  u16* op = out + (size_t)(128 * mi + 32 * rb + ((vo >> 4) & 31)) * 512 + 128 * ni + 32 * j + 8 * (vo >> 9);
  *(u32x4*)(op) = pack8<0>(prev);
  *(u32x4*)(op + 16) = pack8<1>(prev);
}
#undef O_SP
#undef O_FR

#define KVAUX 16
#define WT1 1
#define WT2 2
struct PCtx {
  srd_t sA1, sA2, sUK, sUV, sWQ, sWB, sWP, sK, sV, sO;
  unsigned voffA[2], voffB, voffS, voff;
  unsigned soA1, soA2, soB, soS, soKa, mK0, mA, mB2;
  unsigned gv[8];
  u32x2 fl; u64 tag; const u64* flg;
  u32x4 wp;
  unsigned soPW, soPS, soPX;
  u32x4 kvp[16]; unsigned kvo;
  int w, lane, h;
  LAS char* L;
};
template <int T, int I> DI void p_pieceA(const PCtx& c) {
  if constexpr (T < 16) {
    constexpr unsigned st = (unsigned)(T % 3) * 32768u, kt = (unsigned)(T & 7);
    if constexpr (T < 8) dma16c<st + I * 1024u, I * 8192u + kt * 128u>(c.mA, c.voffA[I & 1], c.sA1, c.soA1);
    else                 dma16c<st + I * 1024u, I * 8192u + kt * 128u>(c.mA, c.voffA[I & 1], c.sA2, c.soA2);
  }
}
template <int T, int I> DI void p_pieceB(const PCtx& c) {
  constexpr unsigned st = 98304u + (unsigned)(T & 1) * 32768u, kt = (unsigned)(T & 7);
  if constexpr (T < 8) {
    dma16c<st + I * 1024u, kt * 524288u + (I & 1) * 128u>(c.mA, c.voffB, I < 2 ? c.sUK : c.sUV, c.soB);
  } else if constexpr (T < 16) {
    if constexpr (I < 2) dma16c<st + I * 1024u, kt * 524288u + I * 128u>(c.mB2, c.voffB, c.sWQ, c.soB);
    else if constexpr (I == 2) dma16m<st + 2048u, kt * 196608u>(c.mB2, c.voffS, c.sWB, c.soS);
    else if (c.w == 7) *(LAS u16*)(c.L + st + ((c.lane >> 3) * 6 + 5) * 512 + (c.lane & 7) * 64 + 32) = (u16)c.gv[kt];
  }
}
template <int I, bool ISV> DI void p_kvstore(const PCtx& c) {
  if constexpr (I >= 0 && I < 16) {
    constexpr int mt = I >> 2, nt = (I >> 1) & 1, sh = I & 1;
    constexpr unsigned off = (unsigned)((mt >> 1) * 16384 + (ISV ? nt * 4 + 2 * (mt & 1) + sh : (mt & 1) * 8 + 2 * nt + sh) * 1024);
    __builtin_amdgcn_raw_buffer_store_b128(c.kvp[I], c.sO, c.kvo + off, 0, 16);
  }
}
#define WARM1(VO, SRD, SO, OFF) dma16c<SL * 32768u, (OFF)>(c.mA, VO, SRD, SO)
template <int I, unsigned SL> DI void p_warm(PCtx& c) {
  if constexpr (I == 0) { WARM1(c.voff, c.sA1, c.soPX, 0); WARM1(c.voffB, c.sUK, c.soPW, 0); WARM1(c.voffB, c.sUK, c.soPW, 128); WARM1(c.voffB, c.sUV, c.soPW, 0); WARM1(c.voffB, c.sUV, c.soPW, 128); }
  if constexpr (I == 1) { WARM1(c.voff, c.sA2, c.soPX, 0); WARM1(c.voffB, c.sWQ, c.soPW, 0); }
  if constexpr (I == 2) { WARM1(c.voffB, c.sWQ, c.soPW, 128); WARM1(c.voffS, c.sWB, c.soPS, 0); }
}
#undef WARM1
DI void poll_spin(const u64* f, u64 tag, int lane) {
  const u64 t0 = __builtin_amdgcn_s_memrealtime();
  while (!__all(__hip_atomic_load(f + (lane & 7), __ATOMIC_RELAXED, __HIP_MEMORY_SCOPE_AGENT) == tag)) { __builtin_amdgcn_s_sleep(2); if (SPIN_OVER(t0)) break; }
}
DI void poll_issue(u32x2& fl, const u64* f, int lane) { asm volatile("global_load_dwordx2 %0, %1, off sc1" : "+v"(fl) : "v"(f + (lane & 7)) : "memory"); }
DI void poll_check(u32x2& fl, const u64* f, u64 tag, int lane) {
  asm volatile("" : "+v"(fl));
  if (!__all(fl[0] == (unsigned)tag && fl[1] == (unsigned)(tag >> 32))) poll_spin(f, tag, lane);
  asm volatile("" ::: "memory");
}
template <int B> DI void p_hook(PCtx& c) {
  if constexpr (B == 9) { if (c.w == 0) dma16c<155648u, 0u>(c.mA, c.voff, c.sWP, (unsigned)c.h * 512u); }
  if constexpr (B == 13) {
    const int r32 = c.lane & 31, hh = c.lane >> 5; LAS const u16* wb = (LAS const u16*)(c.L + 155648) + (r32 & 15); const unsigned sg = r32 >= 16 ? 0x80008000u : 0u;
#pragma unroll
    for (int jj = 0; jj < 4; ++jj) { const int d0 = 8 * (jj >> 1) + 4 * hh + 2 * (jj & 1); c.wp[jj] = ((unsigned)wb[d0 * 16] | ((unsigned)wb[(d0 + 1) * 16] << 16)) ^ sg; }
  }
  if constexpr (B == 15) { if (c.w == 0 && c.lane == 0) __hip_atomic_store((u64*)c.flg + (blockIdx.x >> 5), c.tag, __ATOMIC_RELAXED, __HIP_MEMORY_SCOPE_AGENT); }
  if constexpr (B == 16) { if (c.w == 0) poll_issue(c.fl, c.flg, c.lane); }
}

template <int PASS, int MT, int NT, int NSUB, bool TRANS, bool ISV>
struct PPass {
  PCtx& c; f32x16 (&acc)[MT][NT];
  unsigned aB[2][4], bB;
  bf16x8 fa[2][MT], fb[2][NT];
  template <int SET, int T, int KS> DI void load() {
    constexpr int bo = (T & 1) * 32768 + KS * (2 * NSUB * 512), sl = T % 3;
#pragma unroll
    for (int nt = 0; nt < NT; ++nt) fb[SET][nt] = trfrag(c.L + bB + bo + nt * 512, c.L + bB + bo + nt * 512 + 256);
#pragma unroll
    for (int mt = 0; mt < MT; ++mt) fa[SET][mt] = ldsfrag(c.L + aB[sl >> 1][KS] + (sl & 1) * 32768 + mt * 4096);
  }
  template <int SET, int I> DI void mma1() {
    if constexpr (I < MT * NT) {
      constexpr int mt = I / NT, nt = I % NT;
      acc[mt][nt] = TRANS ? MFMA(fb[SET][nt], fa[SET][mt], acc[mt][nt]) : MFMA(fa[SET][mt], fb[SET][nt], acc[mt][nt]);
    }
  }
  template <int SET, int T, int I0, int S0> DI void mmaA() {
    mma1<SET, 0>(); SBAR(); p_pieceA<T, I0>(c); SBAR(); mma1<SET, 1>(); mma1<SET, 6>(); SBAR(); p_kvstore<S0, ISV>(c); SBAR(); mma1<SET, 2>(); mma1<SET, 7>(); SBAR(); p_pieceA<T, I0 + 1>(c); SBAR();
    mma1<SET, 3>(); mma1<SET, 4>(); mma1<SET, 5>();
  }
  template <int t> DI void tile() {
    constexpr int T = PASS * 8 + t;
    constexpr int S0 = PASS == 1 && t < 6 ? 3 * t : 16;
    constexpr int WI = T == WT1 ? 1 : (T == WT2 ? 2 : 0);
    constexpr int NST = (PASS == 1 ? (t < 5 ? 3 : (t == 5 ? 1 : 0)) : 0) + (T == 0 ? 5 : 0) + (WI ? 2 : 0);
    SBAR(); load<1, T, 1>(); SBAR(); if constexpr (WI != 0) p_warm<WI, (T + 2) % 3>(c); SBAR(); mmaA<0, T + 2, 0, S0>();
    SBAR(); load<0, T, 2>(); SBAR(); mmaA<1, T + 2, 2, S0 + 1>();
    SBAR(); load<1, T, 3>(); SBAR(); mmaA<0, 16, 0, S0 + 2>();
    SBAR();
    if constexpr (T == 15) LBAR(); else if constexpr (T == 14) wait_bar<0>(); else wait_bar<4 + NST>();
    SBAR();
    if constexpr (t < 7) load<0, T + 1, 0>();
    SBAR();
    mma1<1, 0>(); p_pieceB<T + 2, 0>(c); SBAR();
    mma1<1, 1>(); mma1<1, 6>(); p_pieceB<T + 2, 1>(c); SBAR();
    mma1<1, 2>(); mma1<1, 7>(); p_pieceB<T + 2, 2>(c); SBAR();
    mma1<1, 3>(); p_pieceB<T + 2, 3>(c); SBAR();
    mma1<1, 4>(); mma1<1, 5>(); SBAR();
    p_hook<T + 1>(c); SBAR();
  }
  DI void run(unsigned arow0, unsigned nt0) {
    const int lane = c.lane, r = lane & 31, hh = lane >> 5, i16 = lane & 15;
#pragma unroll
    for (int ks = 0; ks < 4; ++ks) { aB[0][ks] = (arow0 + r) * 128u + (unsigned)(((2 * ks + hh) ^ ((r >> 1) & 7)) * 16); aB[1][ks] = aB[0][ks] + 65536u; asm volatile("" : "+v"(aB[1][ks])); }
    bB = 98304u + (unsigned)(hh * NSUB + nt0) * 512u + (unsigned)((i16 >> 2) * 64 + ((lane >> 4) & 1) * 32 + (i16 & 3) * 8); asm volatile("" : "+v"(bB));
#pragma unroll
    for (int mt = 0; mt < MT; ++mt)
#pragma unroll
      for (int nt = 0; nt < NT; ++nt) acc[mt][nt] = (f32x16){};
    load<0, PASS * 8, 0>();
    tile<0>(); tile<1>(); tile<2>(); tile<3>(); tile<4>(); tile<5>(); tile<6>(); tile<7>();
  }
};
template <int PASS, int MT, int NT, int NSUB, bool TRANS, bool ISV>
DI void p_pass(PCtx& c, f32x16 (&acc)[MT][NT], unsigned arow0, unsigned nt0) { PPass<PASS, MT, NT, NSUB, TRANS, ISV> p{c, acc}; p.run(arow0, nt0); }

DI void softmax2(f32x16& x) {
#pragma unroll
  for (int s = 0; s < 2; ++s) {
    float mx = fmaxf(fmaxf(fmaxf(x[8 * s], x[8 * s + 1]), fmaxf(x[8 * s + 2], x[8 * s + 3])), fmaxf(fmaxf(x[8 * s + 4], x[8 * s + 5]), fmaxf(x[8 * s + 6], x[8 * s + 7])));
    mx = swap32_max(mx);
    const float cc = -mx * 1.4426950408889634f;
    float sum = 0.f;
#pragma unroll
    for (int j = 0; j < 8; ++j) { x[8 * s + j] = __builtin_amdgcn_exp2f(__builtin_fmaf(x[8 * s + j], 1.4426950408889634f, cc)); sum += x[8 * s + j]; }
    const float inv = __builtin_amdgcn_rcpf(swap32_add(sum));
#pragma unroll
    for (int j = 0; j < 8; ++j) x[8 * s + j] *= inv;
  }
}

DI void mfma_acc(f32x16& c, bf16x8 a, bf16x8 b) { asm volatile("v_mfma_f32_32x32x16_bf16 %0, %1, %2, %0" : "+v"(c) : "v"(a), "v"(b)); }
DI void mfma_acc2(f32x16& c, bf16x8 a, bf16x8 b, bf16x8 nx) { asm volatile("v_mfma_f32_32x32x16_bf16 %0, %1, %2, %0" : "+v"(c) : "v"(a), "v"(b), "v"(nx)); }
DI void mfma_init2(f32x16& c, bf16x8 a, bf16x8 b, const f32x16& ci, bf16x8 nx) { asm volatile("v_mfma_f32_32x32x16_bf16 %0, %1, %2, %3" : "=&v"(c) : "v"(a), "v"(b), "v"(ci), "v"(nx)); }
DI float sum2(unsigned pk, float acc) { return __builtin_amdgcn_fdot2_f32_bf16(__builtin_bit_cast(bf16x2, pk), __builtin_bit_cast(bf16x2, 0x3f803f80u), acc, false); }
DI float sum8(bf16x8 p, float acc) { const u32x4 u = __builtin_bit_cast(u32x4, p); return sum2(u[3], sum2(u[2], sum2(u[1], sum2(u[0], acc)))); }
DI float max3f(float a, float b, float c) { float r; asm("v_max3_f32 %0, %1, %2, %3" : "=v"(r) : "v"(a), "v"(b), "v"(c)); return r; }
DI float max2f(float a, float b) { float r; asm("v_max_f32_e32 %0, %1, %2" : "=v"(r) : "v"(a), "v"(b)); return r; }
#define NMV 4
#define TRAIL 4
#define ADIST 6
template <bool DOQK, bool DOPV>
DI void a_mphase(LAS const char* kp, LAS const char* vp, const bf16x8 (&q)[8], bf16x8 (&p)[4], f32x16 (&S)[2], f32x16 (&O)[4], const f32x16& negm, float& l, float& pm0,
                 unsigned mV, unsigned voff, srd_t sV, unsigned oV) {
  constexpr int N = (DOQK ? 16 : 0) + (DOPV ? 16 : 0), D = ADIST;
  bf16x8 f[N];
  unsigned pw[8]; float ls = 0.f, e0 = 0.f;
#define A_ISQK(i) (DOQK && (!DOPV || (((i) >> 3) & 1) == 0))
#define A_IX(i) ((DOPV && DOQK) ? (((i) >> 4) * 8 + ((i) & 7)) : (i))
#define A_LD(i) do { if constexpr ((i) < N) { if constexpr (A_ISQK(i)) f[i] = ldsfrag(kp + A_IX(i) * 1024); \
                     else f[i] = ldsfrag(vp + ((A_IX(i) & 3) * 4 + (A_IX(i) >> 2)) * 1024); } } while (0)
#define A_MM(i) do { if constexpr (A_ISQK(i)) { if constexpr ((A_IX(i) & 7) == 0) mfma_init2(S[A_IX(i) >> 3], f[i], q[0], negm, f[(i) + 1]); \
                       else if constexpr ((i) & 1) mfma_acc(S[A_IX(i) >> 3], f[i], q[A_IX(i) & 7]); else mfma_acc2(S[A_IX(i) >> 3], f[i], q[A_IX(i) & 7], f[((i) + 1) % N]); } \
                     else if constexpr ((i) & 1) mfma_acc(O[A_IX(i) & 3], f[i], p[A_IX(i) >> 2]); else mfma_acc2(O[A_IX(i) & 3], f[i], p[A_IX(i) >> 2], f[((i) + 1) % N]); } while (0)
#define A_SM(e) do { const float x_ = __builtin_amdgcn_exp2f(S[1][(e) & 15]); ls += x_; \
                     if constexpr ((e) & 1) { pw[((e) & 15) >> 1] = cvtpk(e0, x_); asm volatile("" : "+v"(pw[((e) & 15) >> 1]), "+v"(ls)); } else { e0 = x_; asm volatile("" : "+v"(e0), "+v"(ls)); } \
                     if constexpr ((e) == 7) p[2] = asbf((u32x4){pw[0], pw[1], pw[2], pw[3]}); if constexpr ((e) == 15) p[3] = asbf((u32x4){pw[4], pw[5], pw[6], pw[7]}); } while (0)
#define A_MX(j) do { if constexpr ((j) == 0) pm0 = max2f(S[0][0], S[0][1]); else pm0 = max3f(pm0, S[0][2 * ((j) & 7)], S[0][2 * ((j) & 7) + 1]); asm volatile("" : "+v"(pm0)); } while (0)
#define A_STEP(i) do { if constexpr ((i) < N) { A_LD((i) + D); if constexpr ((i) == N - TRAIL) { SBAR(); LBAR(); SBAR(); } A_MM(i); \
                     if constexpr (DOQK && DOPV && (i) < 16) A_SM(i); \
                     if constexpr (DOQK && DOPV && (i) >= 16 && (i) < 24) A_MX((i) - 16); \
                     if constexpr (DOQK && DOPV && NMV >= 4 && (i) == 17) dma16o<0>(mV, voff, sV, oV); \
                     if constexpr (DOQK && DOPV && NMV >= 4 && (i) == 20) dma16n<1024>(voff, sV, oV); \
                     if constexpr (DOQK && DOPV && NMV >= 4 && (i) == 23) dma16n<2048>(voff, sV, oV); \
                     if constexpr (DOQK && DOPV && NMV >= 4 && (i) == 25) dma16n<3072>(voff, sV, oV); \
                     if constexpr (!DOQK && (i) < 8) { A_SM(2 * (i)); A_SM(2 * (i) + 1); } SBAR(); } } while (0)
  SBAR();
  __builtin_amdgcn_s_setprio(1);
  A_LD(0); A_LD(1); A_LD(2); A_LD(3); if constexpr (D > 4) { A_LD(4); A_LD(5); } SBAR();
  A_STEP(0); A_STEP(1); A_STEP(2); A_STEP(3); A_STEP(4); A_STEP(5); A_STEP(6); A_STEP(7);
  A_STEP(8); A_STEP(9); A_STEP(10); A_STEP(11); A_STEP(12); A_STEP(13); A_STEP(14); A_STEP(15);
  A_STEP(16); A_STEP(17); A_STEP(18); A_STEP(19); A_STEP(20); A_STEP(21); A_STEP(22); A_STEP(23);
  A_STEP(24); A_STEP(25); A_STEP(26); A_STEP(27); A_STEP(28); A_STEP(29); A_STEP(30); A_STEP(31);
  if constexpr (DOPV) l += ls;
  if constexpr (!DOPV) { NOP16(); SBAR(); A_MX(0); A_MX(1); A_MX(2); A_MX(3); A_MX(4); A_MX(5); A_MX(6); A_MX(7); }
  __builtin_amdgcn_s_setprio(0);
  #undef A_ISQK
#undef A_IX
#undef A_LD
#undef A_MM
#undef A_SM
#undef A_MX
#undef A_STEP
}

DI void a_finish(const f32x16& a0, const f32x16& a1, float ma, float la, LAS const char* xr, float mb, float lb, float gate, srd_t sC, unsigned voff, unsigned soff) {
  const float mt = fmaxf(ma, mb);
  const float fa = __builtin_amdgcn_exp2f(ma - mt), fb = __builtin_amdgcn_exp2f(mb - mt);
  const float sc = gate / (la * fa + lb * fb);
  const float wa = fa * sc, wb = fb * sc;
#pragma unroll
  for (int t = 0; t < 2; ++t) {
    const f32x16& a = t ? a1 : a0;
#pragma unroll
    for (int s = 0; s < 2; ++s) {
      const f32x4 b0 = *(LAS const f32x4*)(xr + t * 4096 + s * 2048), b1 = *(LAS const f32x4*)(xr + t * 4096 + s * 2048 + 1024);
      const u32x4 o = {cvtpk(a[8 * s + 0] * wa + b0[0] * wb, a[8 * s + 1] * wa + b0[1] * wb), cvtpk(a[8 * s + 2] * wa + b0[2] * wb, a[8 * s + 3] * wa + b0[3] * wb),
                       cvtpk(a[8 * s + 4] * wa + b1[0] * wb, a[8 * s + 5] * wa + b1[1] * wb), cvtpk(a[8 * s + 6] * wa + b1[2] * wb, a[8 * s + 7] * wa + b1[3] * wb)};
      st128_wt(o, sC, voff + (unsigned)((2 * t + s) * 1024), soff);
      SBAR();
    }
  }
}
DI void a_send(const f32x16& a0, const f32x16& a1, LAS char* xr) {
#pragma unroll
  for (int g = 0; g < 4; ++g) {
    *(LAS f32x4*)(xr + g * 1024) = (f32x4){a0[4 * g], a0[4 * g + 1], a0[4 * g + 2], a0[4 * g + 3]};
    *(LAS f32x4*)(xr + 4096 + g * 1024) = (f32x4){a1[4 * g], a1[4 * g + 1], a1[4 * g + 2], a1[4 * g + 3]};
  }
}

__global__ __launch_bounds__(512, 2) void kF(const u16* __restrict__ x, const u16* __restrict__ ckv, const u16* __restrict__ wq_, const u16* __restrict__ wuk,
                                             const u16* __restrict__ wuv, const u16* __restrict__ wqkv, const u16* __restrict__ wphi, const u16* __restrict__ gatew,
                                             const u16* __restrict__ wo_a, const u16* __restrict__ wo_b, char* __restrict__ ws, u16* __restrict__ out) {
  extern __shared__ __attribute__((aligned(16))) char lds[];
  LAS char* L = (LAS char*)lds; const unsigned lds0 = (unsigned)(uintptr_t)L;
  const int tid = threadIdx.x, lane = tid & 63, w = __builtin_amdgcn_readfirstlane(tid >> 6);
  const int b = blockIdx.x, h = (b & 7) * 4 + ((b >> 3) & 3), r = b >> 5;
  const int g = w >> 2, wq = w & 3, hh = lane >> 5, r32 = lane & 31;
  const int X = 15 - r, Y = r, nX = 16 - r;
  constexpr int NS = 17;
  const unsigned voff = (unsigned)lane * 16u;
    bf16x8 q[8];
  u64 tag, tag2; const u64* flg = (const u64*)(ws + WS_FLG + (size_t)h * 256);
  const srd_t sK = mksrd(ws + WS_KF + (size_t)h * 32 * 16384, 32u * 16384u), sV = mksrd(ws + WS_VF + (size_t)h * 32 * 16384, 32u * 16384u);
  const srd_t sW = mksrd(ws + WS_QF, (unsigned)(WS_DBG - WS_QF));
  const unsigned ringK = rfl(lds0 + (g ? 98304u : 32768u) + (unsigned)wq * 4096u), ringV = rfl(lds0 + (g ? 0u : 65536u) + (unsigned)wq * 4096u);
  {
    PCtx c;
    c.fl = (u32x2){0u, 0u}; c.wp = (u32x4){0u, 0u, 0u, 0u};
    c.L = L; c.w = w; c.lane = lane; c.h = h; c.voff = voff; c.flg = flg; c.sK = sK; c.sV = sV;
    c.soA1 = rfl((unsigned)(256 * r + 32 * w) * 1024u);
    c.soA2 = rfl((unsigned)(w < 4 ? 128 * X + 32 * w : 128 * Y + 32 * (w - 4)) * 1024u);
    c.soB = rfl((unsigned)(8 * w) * 8192u + (unsigned)h * 256u);
    c.soS = rfl((unsigned)(8 * w) * 3072u + (unsigned)h * 32u);
    c.soPW = rfl((unsigned)(64 * r + 8 * w) * 8192u + (unsigned)h * 256u); c.soPS = rfl((unsigned)(64 * r + 8 * w) * 3072u + (unsigned)h * 32u);
    c.soPX = rfl((unsigned)(256 * r + 8 * h + w) * 1024u);
    c.soKa = rfl((unsigned)(g * 16384 + wq * 4096)); c.mK0 = ringK;
    c.mA = rfl(lds0 + (unsigned)w * 4096u); c.mB2 = rfl(lds0 + (unsigned)w * 3072u);
    c.sA1 = mksrd(ckv, 2048u * 1024u); c.sA2 = mksrd(x, 2048u * 1024u);
    c.sUK = mksrd(wuk, 512u * 8192u); c.sUV = mksrd(wuv, 512u * 8192u); c.sWQ = mksrd(wq_, 512u * 8192u); c.sWB = mksrd(wqkv, 512u * 3072u); c.sWP = mksrd(wphi, 32u * 512u);
#pragma unroll
    for (int k = 0; k < 2; ++k) c.voffA[k] = (unsigned)((lane >> 3) * 1024 + (((lane & 7) ^ ((4 * k + (lane >> 4)) & 7)) * 16));
    c.voffB = (unsigned)(((lane >> 2) & 7) * 8192 + (lane >> 5) * 64 + (lane & 3) * 16);
    { const int ch = lane & 3, sub = lane >> 5;
      c.voffS = (unsigned)(((lane >> 2) & 7) * 3072 + (sub == 0 ? (ch < 2 ? ch * 16 : 1024 + (ch - 2) * 16) : 2048 + (ch & 1) * 16)); }
    u32x2 ep, ep2;
    asm volatile("global_load_dwordx2 %0, %1, off sc1" : "=v"(ep) : "v"(flg + 16) : "memory");
    asm volatile("global_load_dwordx2 %0, %1, off sc1" : "=v"(ep2) : "v"((const u64*)(ws + WS_FLG + 65536 + (size_t)r * 1024) + 96) : "memory");
#pragma unroll
    for (int k = 0; k < 8; ++k) asm volatile("global_load_ushort %0, %1, off offset:%2" : "=v"(c.gv[k]) : "v"(gatew + lane), "i"(k * 128) : "memory");
    p_pieceA<0, 0>(c); p_pieceA<0, 1>(c); p_pieceA<0, 2>(c); p_pieceA<0, 3>(c);
    p_pieceB<0, 0>(c); p_pieceB<0, 1>(c); p_pieceB<0, 2>(c); p_pieceB<0, 3>(c);
    p_pieceB<1, 0>(c); p_pieceB<1, 1>(c); p_pieceB<1, 2>(c); p_pieceB<1, 3>(c);
    p_pieceA<1, 0>(c); p_pieceA<1, 1>(c); p_pieceA<1, 2>(c); p_pieceA<1, 3>(c);
    p_warm<0, 2>(c);
    wait_bar<13>();
    asm volatile("" : "+v"(ep), "+v"(ep2), "+v"(c.gv[0]), "+v"(c.gv[1]), "+v"(c.gv[2]), "+v"(c.gv[3]), "+v"(c.gv[4]), "+v"(c.gv[5]), "+v"(c.gv[6]), "+v"(c.gv[7]));
    tag = (((u64)rfl(ep[1]) << 32) | rfl(ep[0])) + 1ull; c.tag = tag;
    tag2 = (((u64)rfl(ep2[1]) << 32) | rfl(ep2[0])) + 1ull;
    {
      f32x16 acc[4][2];
      const int wm = (w >> 1) & 1, wn = w & 1;
      if (g == 0) p_pass<0, 4, 2, 8, true, false>(c, acc, 128u * wm, 2u * wn);
      else        p_pass<0, 4, 2, 8, false, true>(c, acc, 128u * wm, 4u + 2u * wn);
#pragma unroll
      for (int i = 0; i < 8; ++i) { c.kvp[2 * i] = pack8<0>(acc[i >> 1][i & 1]); c.kvp[2 * i + 1] = pack8<1>(acc[i >> 1][i & 1]); }
      c.sO = g == 0 ? sK : sV;
      c.kvo = voff + (unsigned)((4 * r + 2 * wm) * 16384 + (g == 0 ? 4 * wn : 8 * wn) * 1024);
    }
    f32x16 acc[2][3];
    if (g == 0) p_pass<1, 2, 3, 6, true, false>(c, acc, 64u * wq, 0u);
    else        p_pass<1, 2, 3, 6, true, true>(c, acc, 64u * wq, 3u);
        if (wq < 2) {
#pragma unroll
      for (int mt = 0; mt < 2; ++mt)
#pragma unroll
        for (int nt = 0; nt < 3; ++nt)
          if (g == 0 || nt == 0) {
            const f32x16 v = acc[mt][nt] * QSCALE; LAS char* qd = L + 131072 + ((2 * wq + mt) * 8 + 2 * (3 * g + nt)) * 1024 + lane * 16;
            *(LAS u32x4*)qd = pack8<0>(v); *(LAS u32x4*)(qd + 1024) = pack8<1>(v);
          }
    }
    LAS char* Ik = L + (g ? 16384 : 81920) + wq * 4096; LAS char* Iv = Ik + 2048;
    u32x4 hq, hk, hv; float gl;
    if (g == 1) {
      LAS char* ho = L + 81920 + wq * 4096 + lane * 16;
      *(LAS u32x4*)ho = pack8<0>(acc[1][1]); *(LAS u32x4*)(ho + 1024) = pack8<1>(acc[1][1]); *(LAS u32x4*)(ho + 2048) = pack8<0>(acc[1][2]);
      *(LAS float*)(L + 81920 + wq * 4096 + 3072 + lane * 4) = acc[1][2][8];
      hq = pack8<0>(acc[0][1]); hk = pack8<1>(acc[0][1]); hv = pack8<0>(acc[0][2]); gl = acc[0][2][8];
    }
        if (w == 0) { wait_vm<0>(); poll_check(c.fl, flg, tag, lane); }
    LBAR();
    dma16s<0u, 0u>(ringK, voff, sK, c.soKa); dma16s<1024u, 1024u>(ringK, voff, sK, c.soKa); dma16s<2048u, 2048u>(ringK, voff, sK, c.soKa); dma16s<3072u, 3072u>(ringK, voff, sK, c.soKa);
#pragma unroll
    for (int ks = 0; ks < 8; ++ks) q[ks] = ldsfrag(L + 131072 + wq * 8192 + ks * 1024 + lane * 16);
    if (g == 0) {
      hq = *(LAS const u32x4*)(Ik + lane * 16); hk = *(LAS const u32x4*)(Ik + 1024 + lane * 16); hv = *(LAS const u32x4*)(Ik + 2048 + lane * 16); gl = *(LAS const float*)(Ik + 3072 + lane * 4);
    }
    if (wq >= 2) {
#pragma unroll
      for (int mt = 0; mt < 2; ++mt)
#pragma unroll
        for (int nt = 0; nt < 3; ++nt)
          if (g == 0 || nt == 0) {
            const f32x16 v = acc[mt][nt] * QSCALE; const unsigned so = (unsigned)(b * 32768 + ((2 * (wq - 2) + mt) * 8 + 2 * (3 * g + nt)) * 1024);
            st128(pack8<0>(v), sW, voff, so); st128(pack8<1>(v), sW, voff + 1024u, so);
          }
    }
    {
      const int i16 = lane & 15, rt = 2 * wq + (g ^ 1);
      const int sw = (r32 >> 1) & 7; LAS char* wr = Ik + r32 * 64;
      const int gr = ((lane >> 4) & 1) * 4 + (i16 & 3), q4 = i16 >> 2;
      unsigned tb[2];
#pragma unroll
      for (int u = 0; u < 2; ++u) tb[u] = (unsigned)((8 * hh + 4 * u + q4) * 64 + ((gr ^ (4 * hh + 2 * u + (q4 >> 1))) * 8));
      f32x16 pq = MFMA(asbf(c.wp), asbf(hq), (f32x16){});
      f32x16 pk = MFMA(asbf(c.wp), asbf(hk), (f32x16){});
      LWAIT();
      *(LAS u32x2*)(wr + 2048 + ((0 + hh) ^ sw) * 8) = (u32x2){hv[0], hv[1]};
      *(LAS u32x2*)(wr + 2048 + ((2 + hh) ^ sw) * 8) = (u32x2){hv[2], hv[3]};
      *(LAS u32x2*)(wr + 2048 + ((4 + hh) ^ sw) * 8) = (u32x2){hh == 0 ? 0x00003f80u : 0u, 0u};
      *(LAS u32x2*)(wr + 2048 + ((6 + hh) ^ sw) * 8) = (u32x2){0u, 0u};
      softmax2(pk);
#pragma unroll
      for (int k = 0; k < 4; ++k) *(LAS u32x2*)(wr + ((2 * k + hh) ^ sw) * 8) = (u32x2){cvtpk(pk[4 * k], pk[4 * k + 1]), cvtpk(pk[4 * k + 2], pk[4 * k + 3])};
      LWAIT();
      f32x16 kv = {};
#pragma unroll
      for (int ks = 0; ks < 2; ++ks) kv = MFMA(trfrag(Ik + tb[0] + ks * 1024, Ik + tb[1] + ks * 1024), trfrag(Iv + tb[0] + ks * 1024, Iv + tb[1] + ks * 1024), kv);
      softmax2(pq);
      const unsigned so = (unsigned)(WS_PQ - WS_QF) + (unsigned)(b * 16384 + rt * 2048);
      st128(pack8<0>(pq), sW, voff, so); st128(pack8<1>(pq), sW, voff + 1024u, so);
      const float gt = __builtin_amdgcn_rcpf(1.0f + __builtin_amdgcn_exp2f(-1.4426950408889634f * swap32_lo(gl)));
      __builtin_amdgcn_raw_buffer_store_b32(__float_as_uint(gt), sW, (unsigned)r32 * 4u + (unsigned)(WS_GW - WS_QF) + (unsigned)(b * 1024 + rt * 128), 0, 0);
      LWAIT();
#pragma unroll
      for (int k = 0; k < 4; ++k) *(LAS f32x4*)(Ik + k * 1024 + lane * 16) = (f32x4){kv[4 * k], kv[4 * k + 1], kv[4 * k + 2], kv[4 * k + 3]};
    }
    dma16s<16384u, 32768u>(ringK, voff, sK, c.soKa); dma16s<16384u + 1024u, 32768u + 1024u>(ringK, voff, sK, c.soKa);
    dma16s<16384u + 2048u, 32768u + 2048u>(ringK, voff, sK, c.soKa); dma16s<16384u + 3072u, 32768u + 3072u>(ringK, voff, sK, c.soKa);
    dma16s<0u, 0u>(ringV, voff, sV, c.soKa); dma16s<1024u, 1024u>(ringV, voff, sV, c.soKa);
    dma16s<2048u, 2048u>(ringV, voff, sV, c.soKa); dma16s<3072u, 3072u>(ringV, voff, sV, c.soKa);
        wait_bar<11>();
    if (g == 1) {
      f32x4 s4 = *(LAS const f32x4*)(L + 16384 + wq * 1024 + lane * 16);
#pragma unroll
      for (int k = 1; k < 8; ++k) s4 += *(LAS const f32x4*)(L + (k < 4 ? 16384 : 81920) + (k & 3) * 4096 + wq * 1024 + lane * 16);
      st128_wt(__builtin_bit_cast(u32x4, s4), sW, voff, (unsigned)(WS_KVP - WS_QF) + (unsigned)((h * 8 + r) * 4096 + wq * 1024));
      LBAR();
    }
  }
  f32x16 O[4], S[2], keep0 = {}, keep1 = {}, negm = {};
#pragma unroll
  for (int d = 0; d < 4; ++d) O[d] = (f32x16){};
  asm volatile("" : "+v"(O[0]), "+v"(O[1]), "+v"(O[2]), "+v"(O[3]), "+v"(negm));
  bf16x8 p[4];
  float m = 0.f, l = 0.f, mk = 0.f, lk = 0.f, pm0 = 0.f;
  float gate[2] = {0.f, 0.f}; f32x2 ps = {0.f, 0.f};
  u32x2 fl2 = {0u, 0u};
  u32x4 warm_dummy; unsigned warm_dm1;
  LAS const char* kbase = L + (g ? 98304 : 32768) + lane * 16;
  LAS const char* vbase = L + (g ? 0 : 65536) + lane * 16;
  LAS char* xr = L + 131072 + wq * 8192 + lane * 16;
#define TILE(s) ((s) < nX ? 2 * (s) + g : 2 * ((s) - nX) + g)
#define CF_SO(blkrow, half) ((unsigned)(WS_CF - WS_QF) + (unsigned)(((4 * (blkrow) + wq) * 288 + 8 * h + 4 * (half)) * 1024))
#define ST_SO(wv) ((unsigned)(WS_ST - WS_QF) + (unsigned)(b * 2048 + (wv) * 256))
#define ST_LOAD(wv) asm volatile("buffer_load_dwordx2 %0, %1, %2, %3 offen" : "+v"(ps) : "v"((unsigned)r32 * 8u), "s"(sW), "s"(rfl(ST_SO(wv))) : "memory")
#define L_SOFTMAX(DIAG, FIRST, D0, D1, D2, D3, D4, D5, D6, D7) do { \
    if (DIAG) { int rel = 32 * wq + r32 - 64 * g - 4 * hh; \
      _Pragma("unroll") for (int e = 0; e < 16; ++e) { const int cc = (e & 3) + 8 * (e >> 2); if ((e & 3) == 0) { asm volatile("" : "+v"(rel)); SBAR(); } \
        if (cc > rel) S[0][e] = -__builtin_inff(); if (cc + 32 > rel) S[1][e] = -__builtin_inff(); } \
      pm0 = max2f(S[0][0], S[0][1]); _Pragma("unroll") for (int e = 2; e < 16; e += 2) pm0 = max3f(pm0, S[0][e], S[0][e + 1]); } \
    float pm = max3f(pm0, S[1][0], S[1][1]), pn = max3f(S[1][2], S[1][3], S[1][4]); \
    pm = max3f(pm, S[1][5], S[1][6]); pn = max3f(pn, S[1][7], S[1][8]); pm = max3f(pm, S[1][9], S[1][10]); pn = max3f(pn, S[1][11], S[1][12]); \
    pm = max3f(pm, S[1][13], S[1][14]); pm = max3f(pm, pn, S[1][15]); \
    { auto rr = __builtin_amdgcn_permlane32_swap(__float_as_uint(pm), __float_as_uint(pm), false, false); pm = max2f(__uint_as_float(rr[0]), __uint_as_float(rr[1])); } \
    SBAR(); D0; SBAR(); \
    const bool first_ = (FIRST); \
    if (first_ || __builtin_expect(__any(pm > THR), 0)) { \
      NOP16(); SBAR(); \
      const float dl = fmaxf(pm, first_ ? -1e30f : 0.f); \
      m += dl; \
      if (!first_) { const float f = __builtin_amdgcn_exp2f(-dl); l *= f; _Pragma("unroll") for (int d = 0; d < 4; ++d) O[d] = O[d] * f; } \
      _Pragma("unroll") for (int e = 0; e < 16; ++e) { S[0][e] -= dl; S[1][e] -= dl; negm[e] = -m; } \
    } \
    float ls0 = l, ls1 = 0.f; unsigned pw[8]; \
    SM_GRP(0, D1); SM_GRP(2, D2); SM_GRP(4, D3); SM_GRP(6, D4); SM_GRP(8, D5); SM_GRP(10, D6); SM_GRP(12, D7); SM_GRP(14, (void)0); \
    l = ls0 + ls1; \
    p[0] = asbf((u32x4){pw[0], pw[1], pw[2], pw[3]}); p[1] = asbf((u32x4){pw[4], pw[5], pw[6], pw[7]}); } while (0)
#define SM_GRP(e0, DMA) do { const float x0 = __builtin_amdgcn_exp2f(S[0][e0]), x1 = __builtin_amdgcn_exp2f(S[0][(e0) + 1]); \
      ls0 += x0; ls1 += x1; pw[(e0) >> 1] = cvtpk(x0, x1); asm volatile("" : "+v"(pw[(e0) >> 1]), "+v"(ls0), "+v"(ls1)); DMA; SBAR(); } while (0)
#define L_SUM() do { } while (0)
  float mo = 0.f, lo = 0.f;
  auto l_pre = [&](int s) {
    if (s == nX) {
      NOP16(); SBAR();
      mo = m; lo = swap32_add(l);
      if (g == 1) ST_LOAD(w - 4);
      __builtin_amdgcn_raw_buffer_store_b64(__builtin_bit_cast(u32x2, (f32x2){mo, lo}), sW, (unsigned)r32 * 8u, ST_SO(w), 0);
      if (g == 0) { a_send(O[2], O[3], xr); keep0 = O[0]; keep1 = O[1]; mk = mo; lk = lo; }
      m = 0.f; l = 0.f;
    }
    if (g == 0 && s == nX + 1) ST_LOAD(w + 4);
  };
  auto l_post = [&](int s) {
    if (s == nX) {
      if (g == 1) {
        wait_vm<8 - NMV>(); asm volatile("" : "+v"(ps));
        a_finish(O[2], O[3], mo, lo, xr, ps[0], ps[1], gate[0], sW, voff, CF_SO(X, 1));
        LWAIT();
        a_send(O[0], O[1], xr);
      }
#pragma unroll
      for (int d = 0; d < 4; ++d) O[d] = (f32x16){};
    }
    if (g == 0 && s == nX + 1) {
      wait_vm<8 - NMV>(); asm volatile("" : "+v"(ps));
      a_finish(keep0, keep1, mk, lk, xr, ps[0], ps[1], gate[0], sW, voff, CF_SO(X, 0));
    }
  };
  unsigned mK = ringK, mV = ringV + 16384u, oK = (unsigned)(4 + g) * 16384u + (unsigned)wq * 4096u, oV = (unsigned)(2 + g) * 16384u + (unsigned)wq * 4096u;
  LAS const char* kp = kbase + 16384; LAS const char* vp = vbase;
#define LV(x) do { if constexpr (NMV == 0) { x; } } while (0)
#define A_TAIL() do { \
    asm volatile("" : "+v"(p[0]), "+v"(p[1]), "+v"(l), "+v"(S[1])); \
    SBAR(); wait_bar<8 - NMV>(); SBAR(); \
    a_mphase<true, true>(kp, vp, q, p, S, O, negm, l, pm0, mV, voff, sV, oV); oV += 32768u; \
    mK ^= 16384u; mV ^= 16384u; kp = (LAS const char*)((unsigned)(uintptr_t)kp ^ 16384u); vp = (LAS const char*)((unsigned)(uintptr_t)vp ^ 16384u); } while (0)
#define A_FAST(FIRST) do { \
    if (!(FIRST)) L_SUM(); \
    L_SOFTMAX(false, FIRST, dma16o<0>(mK, voff, sK, oK), dma16n<1024>(voff, sK, oK), dma16n<2048>(voff, sK, oK), dma16n<3072>(voff, sK, oK), \
              LV(dma16o<0>(mV, voff, sV, oV)), LV(dma16n<1024>(voff, sV, oV)), LV(dma16n<2048>(voff, sV, oV)), LV(dma16n<3072>(voff, sV, oV))); \
    oK += 32768u; A_TAIL(); } while (0)
#define A_SLOW(s) do { \
    L_SUM(); l_pre(s); \
    oK = (s) < 15 ? (unsigned)TILE((s) + 2) * 16384u + (unsigned)wq * 4096u : (unsigned)(WS_KVP - WS_QF) + (unsigned)(h * 32768 + w * 4096); \
    oV = (unsigned)TILE((s) + 1) * 16384u + (unsigned)wq * 4096u; \
    const srd_t sKx = (s) < 15 ? sK : sW; \
    L_SOFTMAX((s) == nX - 1, (s) == nX, dma16o<0>(mK, voff, sKx, oK), dma16n<1024>(voff, sKx, oK), dma16n<2048>(voff, sKx, oK), dma16n<3072>(voff, sKx, oK), \
              LV(dma16o<0>(mV, voff, sV, oV)), LV(dma16n<1024>(voff, sV, oV)), LV(dma16n<2048>(voff, sV, oV)), LV(dma16n<3072>(voff, sV, oV))); \
    oK += 32768u; \
    l_post(s); \
    if ((s) == nX - 1) { _Pragma("unroll") for (int ks = 0; ks < 8; ++ks) q[ks] = ldsfrag(L + 131072 + wq * 8192 + ks * 1024 + lane * 16); negm = (f32x16){}; } \
    A_TAIL(); } while (0)
  a_mphase<true, false>(kbase, vbase, q, p, S, O, negm, l, pm0, 0u, voff, sV, 0u);
  A_FAST(true);
  if (w == 0 && lane == 0) __hip_atomic_store((u64*)flg + 8 + r, tag, __ATOMIC_RELAXED, __HIP_MEMORY_SCOPE_AGENT);
  A_FAST(false);
  {
    const unsigned mq = rfl(lds0 + 131072u + (unsigned)w * 4096u), oq = rfl((unsigned)(b * 32768 + w * 4096));
    dma16o<0>(mq, voff, sW, oq); dma16n<1024>(voff, sW, oq); dma16n<2048>(voff, sW, oq); dma16n<3072>(voff, sW, oq);
    const unsigned go = rfl((unsigned)(WS_GW - WS_QF) + (unsigned)(b * 1024 + wq * 128));
    asm volatile("buffer_load_dword %0, %2, %3, %4 offen\n\tbuffer_load_dword %1, %2, %3, %4 offen offset:512" : "+v"(gate[0]), "+v"(gate[1]) : "v"((unsigned)r32 * 4u), "s"(sW), "s"(go) : "memory");
    poll_issue(fl2, flg + 8, lane);
    const srd_t sWa = mksrd(wo_a, 4096u * 1024u), sWb = mksrd(wo_b, 512u * 1024u);
    u32x4 dmy; unsigned dm1;
    asm volatile("buffer_load_dwordx4 %0, %2, %3, 0 offen\n\tbuffer_load_dwordx4 %0, %2, %3, 0 offen offset:1024\n\t"
                 "buffer_load_dwordx4 %0, %4, %5, 0 offen\n\tbuffer_load_dword %1, %6, %3, 0 offen"
                 : "=&v"(dmy), "=&v"(dm1) : "v"(voff + (unsigned)(b * 16384 + w * 2048)), "s"(sWa), "v"((unsigned)(b * 2048 + w * 256) + (voff & 255u)), "s"(sWb),
                   "v"((unsigned)((((b >> 3) * 8 + w) * 4 + (lane & 3)) * 4096 + (lane >> 2) * 0)) : "memory");
    warm_dummy = dmy; warm_dm1 = dm1;
  }
  A_FAST(false);
  asm volatile("" : "+v"(gate[0]), "+v"(gate[1]), "+v"(fl2));
  asm volatile("" :: "v"(warm_dummy), "v"(warm_dm1));
  if (w == 0) { poll_check(fl2, flg + 8, tag, lane); if (r == 0 && lane == 0) __hip_atomic_store((u64*)flg + 16, tag, __ATOMIC_RELAXED, __HIP_MEMORY_SCOPE_AGENT); }
  {
    int s = 3;
#pragma unroll 1
    for (int blk = 0; blk < 2; ++blk) {
      const int e1 = blk ? 15 : nX - 2, e2 = blk ? 16 : (nX + 2 < 16 ? nX + 2 : 16);
#pragma unroll 1
      for (; s < e1; ++s) A_FAST(false);
#pragma unroll 1
      for (; s < e2; ++s) A_SLOW(s);
    }
  }
  {
    l_pre(16);
    const unsigned mp = ringK, op = rfl((unsigned)(WS_PQ - WS_QF) + (unsigned)(b * 16384 + wq * 2048));
#define G0D(x) do { if (g == 0) { x; } } while (0)
    L_SOFTMAX(true, nX == 16, G0D(dma16o<0>(mp, voff, sW, op)), G0D(dma16n<1024>(voff, sW, op)), G0D(dma16o<0>(mp + 2048u, voff, sW, op + 8192u)), G0D(dma16n<1024>(voff, sW, op + 8192u)),
              (void)0, (void)0, (void)0, (void)0);
#undef G0D
    if (16 == nX || 16 == nX + 1) wait_vm<0>();
    l_post(16);
    asm volatile("" : "+v"(p[0]), "+v"(p[1]), "+v"(l), "+v"(S[1]));
    SBAR(); if (g == 0) wait_bar<4>(); else wait_bar<0>(); SBAR();
  }
  a_mphase<false, true>(kbase, vbase, q, p, S, O, negm, l, pm0, 0u, voff, sV, 0u);
  NOP16(); SBAR();
  if (g == 0 && nX == 16) {
    ST_LOAD(w + 4); wait_vm<0>(); asm volatile("" : "+v"(ps));
    a_finish(keep0, keep1, mk, lk, xr, ps[0], ps[1], gate[0], sW, voff, CF_SO(X, 0));
    LWAIT();
  }
  LAS f32x2* stl = (LAS f32x2*)(L + 81920) + r32;
  LAS char* xr1 = L + (wq < 2 ? 16384 : 98304 - 16384) + wq * 8192 + lane * 16;
  const float lt = swap32_add(l);
  stl[w * 32] = (f32x2){m, lt};
  if (g == 0) {
    a_send(O[2], O[3], xr);
    wait_bar<0>();
    f32x16 kv = {};
#pragma unroll
    for (int k = 0; k < 8; ++k)
#pragma unroll
      for (int qd = 0; qd < 4; ++qd) { const f32x4 v = *(LAS const f32x4*)(L + (k < 4 ? 49152 : 114688) + (k & 3) * 4096 + qd * 1024 + lane * 16);
#pragma unroll
        for (int e = 0; e < 4; ++e) kv[4 * qd + e] += v[e]; }
    const bf16x8 k0 = asbf(pack8<0>(kv)), k1 = asbf(pack8<1>(kv));
#pragma unroll
    for (int t = 0; t < 2; ++t) {
      f32x16 o = MFMA(k0, ldsfrag(kbase + wq * 4096 + t * 2048), (f32x16){});
      o = MFMA(k1, ldsfrag(kbase + wq * 4096 + t * 2048 + 1024), o);
      o = o * ((1.0f - gate[t]) / (swap32_lo(o[8]) + 1e-6f));
      st128_wt(pack8<0>(o), sW, voff, (unsigned)(WS_CF - WS_QF) + (unsigned)(((4 * (t ? Y : X) + wq) * 288 + 256 + h) * 1024));
    }
    LBAR();
    const f32x2 po = stl[(w + 4) * 32];
    a_finish(O[0], O[1], m, lt, xr1, po[0], po[1], gate[1], sW, voff, CF_SO(Y, 0));
  } else {
    a_send(O[0], O[1], xr1);
    LBAR();
    const f32x2 po = stl[(w - 4) * 32];
    a_finish(O[2], O[3], m, lt, xr, po[0], po[1], gate[1], sW, voff, CF_SO(Y, 1));
  }
  o_phase(wo_a, wo_b, ws, out, L, lds0, lane, w, h, r, tag2, sW);
#undef TILE
#undef CF_SO
#undef ST_SO
#undef ST_LOAD
#undef L_SOFTMAX
#undef SM_GRP
#undef L_SUM
#undef LV
#undef A_TAIL
#undef A_FAST
#undef A_SLOW
}

extern "C" void kernel_launch(void* const* d_in, const int* in_sizes, int n_in, void* d_out, int out_size, void* d_ws, size_t ws_size, hipStream_t stream) {
  const u16* x = (const u16*)d_in[0]; const u16* ckv = (const u16*)d_in[1]; const u16* wq = (const u16*)d_in[2];
  const u16* wuk = (const u16*)d_in[3]; const u16* wuv = (const u16*)d_in[4]; const u16* wo_a = (const u16*)d_in[5];
  const u16* wqkv = (const u16*)d_in[7]; const u16* wphi = (const u16*)d_in[8]; const u16* wo_b = (const u16*)d_in[9]; const u16* gatew = (const u16*)d_in[10];
  (void)hipFuncSetAttribute((const void*)kF, hipFuncAttributeMaxDynamicSharedMemorySize, 163840);
  kF<<<256, 512, 163840, stream>>>(x, ckv, wq, wuk, wuv, wqkv, wphi, gatew, wo_a, wo_b, (char*)d_ws, (u16*)d_out);
}
```
